```python
import torch

def _softmax(x, axis=-1):
    return torch.softmax(x, dim=axis)
def _gelu(x):
    return torch.nn.functional.gelu(x, approximate="tanh")
def _silu(x):
    return torch.nn.functional.silu(x)
def _sigmoid(x):
    return torch.sigmoid(x)
def _layernorm(x, axis=-1, eps=1e-5):
    m = x.mean(dim=axis, keepdim=True)
    v = x.var(dim=axis, keepdim=True, unbiased=False)
    return (x - m) / torch.sqrt(v + eps)
def _scatter_add(tgt, idx, src, axis=0):
    return tgt.clone().index_add_(axis, idx, src)
def _segment_sum(src, seg, n):
    out = torch.zeros((n,) + src.shape[1:], dtype=src.dtype)
    return out.index_add_(0, seg, src)
def _pad(x, pw):
    flat = tuple(v for pair in reversed(pw) for v in pair)
    return torch.nn.functional.pad(x, flat)

kernel_name = 'parallel_sum__ssm_conv_s4__dense_activation_relu__prompt'

def setup_inputs():

    _w = (lambda a: (lambda t: t.to(torch.bfloat16) if t.is_floating_point() else t)(torch.as_tensor(a)))
    def _A_setup():
        B, L, D, N = 1, 2048, 576, 128
        A_re = -(lambda _x: torch.exp(torch.as_tensor(_x)))((lambda *s: torch.randn(*s, dtype=torch.bfloat16) * 3.2)(D, N))
        A_im = torch.pi * (lambda *s: torch.randn(*s, dtype=torch.bfloat16) * 0.2)(D, N)
        B_re = (lambda *s: torch.randn(*s, dtype=torch.bfloat16) * 0.4)(D, N)
        B_im = (lambda *s: torch.randn(*s, dtype=torch.bfloat16) * 0.4)(D, N)
        C_re = (lambda *s: torch.randn(*s, dtype=torch.bfloat16) * 0.4)(D, N)
        C_im = (lambda *s: torch.randn(*s, dtype=torch.bfloat16) * 0.4)(D, N)
        log_dt = (lambda *s: torch.randn(*s, dtype=torch.bfloat16) * 0.2)(D) * 0.5 - 4.0
        Dskip = (lambda *s: torch.randn(*s, dtype=torch.bfloat16) * 0.4)(D)
        return {
            "u": _w((lambda *s: torch.randn(*s, dtype=torch.bfloat16) * 0.05)(B, L, D)),
            "A_re": _w(A_re),
            "A_im": _w(A_im),
            "B_re": _w(B_re),
            "B_im": _w(B_im),
            "C_re": _w(C_re),
            "C_im": _w(C_im),
            "log_dt": _w(log_dt),
            "Dskip": _w(Dskip),
        }
    _A_in = _A_setup()

    def _B_setup():
        B, S, D, Dff = 1, 2048, 576, 4608
        return {
            "x": _w((lambda *s: torch.randn(*s, dtype=torch.bfloat16) * 0.05)(B, S, D)),
            "w1": _w((lambda *s: torch.randn(*s, dtype=torch.bfloat16) * 0.05)(D, Dff)),
            "w2": _w((lambda *s: torch.randn(*s, dtype=torch.bfloat16) * 0.05)(Dff, D)),
        }
    _B_in = _B_setup()

    return {
        "A_u": _A_in["u"],
        "A_A_re": _A_in["A_re"],
        "A_A_im": _A_in["A_im"],
        "A_B_re": _A_in["B_re"],
        "A_B_im": _A_in["B_im"],
        "A_C_re": _A_in["C_re"],
        "A_C_im": _A_in["C_im"],
        "A_log_dt": _A_in["log_dt"],
        "A_Dskip": _A_in["Dskip"],
        "B_x": _B_in["x"],
        "B_w1": _B_in["w1"],
        "B_w2": _B_in["w2"],
    }

def reference(A_u, A_A_re, A_A_im, A_B_re, A_B_im, A_C_re, A_C_im, A_log_dt, A_Dskip, B_x, B_w1, B_w2):
    def _A_ref(u, A_re, A_im, B_re, B_im, C_re, C_im, log_dt, Dskip):
        Bsz, L, D = u.shape
        dt = torch.exp(log_dt)[:, None]
        A = A_re + 1j * A_im
        Bc = B_re + 1j * B_im
        Cc = C_re + 1j * C_im
        logdA = dt * A
        dB = dt * Bc
        l = (lambda x, dt: x.to(dt))(torch.arange(L), A_re.dtype)
        P = torch.exp(logdA[:, :, None] * l[None, None, :])
        K = torch.einsum("dn,dnl,dn->dl", Cc, P, dB).real
        L2 = 2 * L
        Kp = _pad(K, ((0, 0), (0, L)))
        up = (lambda x, *d: x.permute(*d))(u, 0, 2, 1)
        up = _pad(up, ((0, 0), (0, 0), (0, L)))
        Kf = (lambda x, axes=(-2,-1): torch.fft.rfft2(x.float(), dim=axes))(Kp[:, None, :], axes=(-2, -1))
        Uf = (lambda x, axes=(-2,-1): torch.fft.rfft2(x.float(), dim=axes))(up[:, :, None, :], axes=(-2, -1))
        Yf = Uf * Kf[None, :, :, :]
        y = (lambda x, s, axes=(-2,-1): torch.fft.irfft2(x, s=s, dim=axes).to(torch.bfloat16))(Yf, (1, L2), axes=(-2, -1))[:, :, 0, :L]
        y = (lambda x, *d: x.permute(*d))(y, 0, 2, 1) + u * Dskip[None, None, :]
        return y

    def _B_ref(x, w1, w2):
        h = torch.einsum("bsd,df->bsf", x, w1)
        zero = torch.zeros_like(h)
        h = torch.maximum(h, zero)
        return torch.einsum("bsf,fd->bsd", h, w2)

    x = A_u
    xn = _layernorm(x, axis=-1)
    return x + _A_ref(xn, A_A_re, A_A_im, A_B_re, A_B_im, A_C_re, A_C_im, A_log_dt, A_Dskip) + _B_ref(xn, B_w1, B_w2)
```

```cpp
#include <hip/hip_runtime.h>
#include <hip/hip_bf16.h>
#include <stdint.h>
typedef unsigned short u16;
constexpr int L = 2048, D = 576, NS = 128, FF = 4608;
__device__ __forceinline__ float bf2f(u16 v) { return __uint_as_float(((unsigned)v) << 16); }
__device__ __forceinline__ u16 f2bf(float f) { unsigned u = __float_as_uint(f); u += 0x7fffu + ((u >> 16) & 1u); return (u16)(u >> 16); }
__device__ __forceinline__ float rbf(float f) { return bf2f(f2bf(f)); }

__global__ void k_ln(const u16* x, u16* xn) {
  int row = blockIdx.x; int lane = threadIdx.x;
  float v[9]; float s = 0;
  for (int i = 0; i < 9; ++i) { v[i] = bf2f(x[row * D + i * 64 + lane]); s += v[i]; }
  for (int o = 32; o; o >>= 1) s += __shfl_xor(s, o);
  float mean = s / D; float q = 0;
  for (int i = 0; i < 9; ++i) { float d = v[i] - mean; q += d * d; }
  for (int o = 32; o; o >>= 1) q += __shfl_xor(q, o);
  float var = rbf(q / D); float m = rbf(mean);
  float sd = rbf(sqrtf(rbf(var + 1e-5f)));
  for (int i = 0; i < 9; ++i) xn[row * D + i * 64 + lane] = f2bf(rbf(v[i] - m) / sd);
}
__global__ void k_kgen(const u16* Are, const u16* Aim, const u16* Bre, const u16* Bim, const u16* Cre, const u16* Cim, const u16* logdt, float* K) {
  int d = blockIdx.x; __shared__ float zr[NS], zi[NS], wr[NS], wi[NS];
  float dt = rbf(expf(bf2f(logdt[d])));
  if (threadIdx.x < NS) { int n = threadIdx.x; int i = d * NS + n;
    zr[n] = dt * bf2f(Are[i]); zi[n] = dt * bf2f(Aim[i]);
    float br = dt * bf2f(Bre[i]), bi = dt * bf2f(Bim[i]); float cr = bf2f(Cre[i]), ci = bf2f(Cim[i]);
    wr[n] = cr * br - ci * bi; wi[n] = cr * bi + ci * br; }
  __syncthreads();
  for (int l = threadIdx.x; l < L; l += blockDim.x) {
    float lf = rbf((float)l); float acc = 0;
    for (int n = 0; n < NS; ++n) { float e = expf(zr[n] * lf); float s, c; sincosf(zi[n] * lf, &s, &c); acc += e * (wr[n] * c - wi[n] * s); }
    K[d * L + l] = acc;
  }
}
__global__ void k_conv(const float* K, const u16* xn, float* y) {
  int d = blockIdx.x; __shared__ float k[L], u[L];
  for (int i = threadIdx.x; i < L; i += blockDim.x) { k[i] = K[d * L + i]; u[i] = bf2f(xn[i * D + d]); }
  __syncthreads();
  for (int t = threadIdx.x; t < L; t += blockDim.x) { float acc = 0; for (int s = 0; s <= t; ++s) acc += k[t - s] * u[s]; y[t * D + d] = acc; }
}
template <int MODE>
__global__ void k_gemm(const u16* A, const u16* B, u16* C, int M, int N, int Kd, const u16* x, const u16* xn, const float* y, const u16* Dskip) {
  __shared__ float As[16][65], Bs[16][65];
  int tx = threadIdx.x & 15, ty = threadIdx.x >> 4; int r0 = blockIdx.y * 64, c0 = blockIdx.x * 64;
  float acc[4][4] = {};
  for (int k0 = 0; k0 < Kd; k0 += 16) {
    for (int i = threadIdx.x; i < 1024; i += 256) { int r = i >> 4, k = i & 15; As[k][r] = bf2f(A[(size_t)(r0 + r) * Kd + k0 + k]); int kk = i >> 6, c = i & 63; Bs[kk][c] = bf2f(B[(size_t)(k0 + kk) * N + c0 + c]); }
    __syncthreads();
    for (int k = 0; k < 16; ++k) for (int i = 0; i < 4; ++i) for (int j = 0; j < 4; ++j) acc[i][j] += As[k][ty * 4 + i] * Bs[k][tx * 4 + j];
    __syncthreads();
  }
  for (int i = 0; i < 4; ++i) for (int j = 0; j < 4; ++j) { int r = r0 + ty * 4 + i, c = c0 + tx * 4 + j;
    if (MODE == 0) C[(size_t)r * N + c] = f2bf(fmaxf(acc[i][j], 0.f));
    else { size_t o = (size_t)r * N + c; float a = rbf(rbf(y[o]) + rbf(bf2f(xn[o]) * bf2f(Dskip[c]))); C[o] = f2bf(rbf(bf2f(x[o]) + a) + rbf(acc[i][j])); } }
}
extern "C" void kernel_launch(void* const* d_in, const int* in_sizes, int n_in, void* d_out, int out_size, void* d_ws, size_t ws_size, hipStream_t stream) {
  const u16* x = (const u16*)d_in[0];
  char* ws = (char*)d_ws;
  u16* xn = (u16*)ws; float* K = (float*)(ws + (4 << 20)); float* y = (float*)(ws + (12 << 20)); u16* h = (u16*)(ws + (20 << 20));
  k_ln<<<L, 64, 0, stream>>>(x, xn);
  k_kgen<<<D, 256, 0, stream>>>((const u16*)d_in[1], (const u16*)d_in[2], (const u16*)d_in[3], (const u16*)d_in[4], (const u16*)d_in[5], (const u16*)d_in[6], (const u16*)d_in[7], K);
  k_conv<<<D, 256, 0, stream>>>(K, xn, y);
  k_gemm<0><<<dim3(FF / 64, L / 64), 256, 0, stream>>>(xn, (const u16*)d_in[10], h, L, FF, D, nullptr, nullptr, nullptr, nullptr);
  k_gemm<1><<<dim3(D / 64, L / 64), 256, 0, stream>>>(h, (const u16*)d_in[11], (u16*)d_out, L, D, FF, x, xn, y, (const u16*)d_in[8]);
}
```

```cpp
#include <hip/hip_runtime.h>
#include <stdint.h>
#define WS_EPOCH 0u
#define WS_ACK 4096u
#define WS_FLN 32768u
#define WS_FCS 851968u
#define WS_FA 49152u
#define WS_FH 65536u
#define WS_FP 163840u
#define WS_STATS 327680u
#define WS_CS 655360u
#define WS_DBG 393216u
#define WS_FT 917504u
#define WS_T 4194304u
#define WS_UT 1048576u
#define UTLEN 2304u
#define WS_A 7340032u
#define WS_H 10485760u
#define WS_P 29360128u
#define WS_END 48234496u
#define SRD_BIAS 4096
#define MLP_ASM \
  "s_load_dwordx8 s[84:91], %10, 8\n\t" \
  "s_load_dwordx4 s[92:95], %10, 40\n\t" \
  "s_load_dwordx2 s[96:97], %10, 56\n\t" \
  "v_mbcnt_lo_u32_b32 v16, -1, 0\n\t" \
  "v_mbcnt_hi_u32_b32 v16, -1, v16\n\t" \
  "s_lshl_b32 s54, %6, 3\n\t" \
  "s_add_u32 s54, s54, %4\n\t" \
  "s_lshl_b32 s69, s54, 3\n\t" \
  "s_add_u32 s69, s69, %5\n\t" \
  "s_mul_hi_u32 s55, s69, 0x55555556\n\t" \
  "s_mul_i32 s55, s55, 6\n\t" \
  "s_add_u32 s55, s55, s69\n\t" \
  "s_add_u32 s55, s55, 6\n\t" \
  "s_and_b32 s57, %7, 1\n\t" \
  "s_lshl_b32 s58, s55, 1\n\t" \
  "s_lshl_b32 s67, s57, 8\n\t" \
  "s_add_u32 s57, s57, s58\n\t" \
  "s_lshl_b32 s57, s57, 7\n\t" \
  "v_lshl_add_u32 v33, v16, 1, s57\n\t" \
  "v_mov_b32 v61, s58\n\t" \
  "v_mov_b32 v230, 0\n\t" \
  "v_mov_b32 v231, 0\n\t" \
  "v_mov_b32 v232, 0\n\t" \
  "v_mov_b32 v233, 0\n\t" \
  "v_mov_b32 v234, 0\n\t" \
  "v_mov_b32 v235, 0\n\t" \
  "v_mov_b32 v236, 0\n\t" \
  "s_add_u32 s67, s67, 36864\n\t" \
  "s_waitcnt lgkmcnt(0)\n\t" \
  "global_load_short_d16_hi v230, v33, s[84:85]\n\t" \
  "global_load_short_d16_hi v231, v33, s[86:87]\n\t" \
  "global_load_short_d16_hi v232, v33, s[88:89]\n\t" \
  "global_load_short_d16_hi v233, v33, s[90:91]\n\t" \
  "global_load_short_d16_hi v234, v33, s[92:93]\n\t" \
  "global_load_short_d16_hi v235, v33, s[94:95]\n\t" \
  "global_load_short_d16_hi v236, v61, s[96:97]\n\t" \
  "buffer_load_dwordx2 v[36:37], off, %3, 0 offset:0\n\t" \
  "s_mul_i32 s49, %5, 3\n\t" \
  "s_add_u32 s49, s49, %6\n\t" \
  "s_mul_i32 s50, s49, 384\n\t" \
  "s_mul_i32 s40, %7, 3072\n\t" \
  "v_lshrrev_b32 v63, 2, v16\n\t" \
  "v_mul_u32_u24 v63, 9216, v63\n\t" \
  "v_lshrrev_b32 v61, 4, v16\n\t" \
  "v_and_b32 v61, 2, v61\n\t" \
  "v_xor_b32 v61, v61, v16\n\t" \
  "v_and_b32 v61, 3, v61\n\t" \
  "v_lshl_add_u32 v63, v61, 4, v63\n\t" \
  "s_and_b32 s57, %7, 1\n\t" \
  "s_mul_i32 s57, s57, 147456\n\t" \
  "s_lshr_b32 s58, %7, 1\n\t" \
  "s_lshl_b32 s58, s58, 6\n\t" \
  "s_add_u32 s57, s57, s58\n\t" \
  "v_add_u32 v63, s57, v63\n\t" \
  "v_add_u32 v17, 4096, v63\n\t" \
  "v_add_u32 v18, 3200, v63\n\t" \
  "v_add_u32 v19, 2304, v63\n\t" \
  "s_add_u32 m0, s40, 0\n\t" \
  "s_add_u32 s43, s50, 0\n\t" \
  "buffer_load_dwordx4 v17, %1, s43 offen offset:0 lds\n\t" \
  "buffer_load_dwordx4 v18, %1, s43 offen offset:1024 lds\n\t" \
  "buffer_load_dwordx4 v19, %1, s43 offen offset:2048 lds\n\t" \
  "v_lshrrev_b32 v33, 3, v16\n\t" \
  "v_mul_u32_u24 v33, 1152, v33\n\t" \
  "v_lshrrev_b32 v61, 4, v16\n\t" \
  "v_xor_b32 v61, v61, v16\n\t" \
  "v_and_b32 v61, 7, v61\n\t" \
  "v_lshl_add_u32 v33, v61, 4, v33\n\t" \
  "v_xor_b32 v61, 64, v33\n\t" \
  "v_add_u32 v20, 4096, v33\n\t" \
  "v_add_u32 v21, 12288, v61\n\t" \
  "v_add_u32 v22, 20480, v33\n\t" \
  "v_add_u32 v23, 28672, v61\n\t" \
  "s_lshl_b32 s42, %4, 2\n\t" \
  "s_add_u32 s42, s42, %7\n\t" \
  "s_mul_i32 s42, s42, 73728\n\t" \
  "s_lshl_b32 s41, %7, 13\n\t" \
  "s_add_u32 s41, s41, 61440\n\t" \
  "s_add_u32 m0, s41, 32768\n\t" \
  "s_add_u32 s44, s42, 0\n\t" \
  "buffer_load_dwordx4 v20, %0, s44 offen offset:0 lds\n\t" \
  "buffer_load_dwordx4 v21, %0, s44 offen offset:1024 lds\n\t" \
  "buffer_load_dwordx4 v22, %0, s44 offen offset:2048 lds\n\t" \
  "buffer_load_dwordx4 v23, %0, s44 offen offset:3072 lds\n\t" \
  "s_getpc_b64 s[56:57]\n\t" \
  "L_pc_%=:\n\t" \
  "s_lshl_b32 s54, %7, 13\n\t" \
  "v_lshlrev_b32 v33, 7, v16\n\t" \
  "v_add_u32 v33, s54, v33\n\t" \
  "v_min_u32 v61, L_end_%=-L_pc_%=-4, v33\n\t" \
  "global_load_dword a255, v61, s[56:57]\n\t" \
  "v_add_u32 v33, 0x8000, v33\n\t" \
  "v_min_u32 v61, L_end_%=-L_pc_%=-4, v33\n\t" \
  "global_load_dword a255, v61, s[56:57]\n\t" \
  "s_mov_b32 s68, 0x3fb8aa3b\n\t" \
  "s_waitcnt vmcnt(10)\n\t" \
  "s_cmp_ge_u32 %7, 2\n\t" \
  "s_cbranch_scc1 L_rows_%=\n\t" \
  "v_mul_f32 v194, 0x3fb8aa3b, v236\n\t" \
  "v_rndne_f32 v195, v194\n\t" \
  "v_fma_f32 v196, v236, s68, -v194\n\t" \
  "v_sub_f32 v194, v194, v195\n\t" \
  "v_fmac_f32 v196, 0x32a5705f, v236\n\t" \
  "v_cvt_i32_f32 v195, v195\n\t" \
  "v_add_f32 v194, v194, v196\n\t" \
  "v_exp_f32 v194, v194\n\t" \
  "s_nop 0\n\t" \
  "v_ldexp_f32 v194, v194, v195\n\t" \
  "v_cvt_pk_bf16_f32 v194, v194, v194\n\t" \
  "v_and_b32 v194, 0xffff0000, v194\n\t" \
  "v_mul_f32 v238, v194, v230\n\t" \
  "s_add_u32 m0, s41, 36864\n\t" \
  "s_add_u32 s44, s42, 36864\n\t" \
  "buffer_load_dwordx4 v20, %0, s44 offen offset:0 lds\n\t" \
  "v_mul_f32 v239, v194, v231\n\t" \
  "v_mul_f32 v238, 0x3fb8aa3b, v238\n\t" \
  "v_mul_f32 v239, 0.15915494, v239\n\t" \
  "v_mul_f32 v194, 0x41000000, v238\n\t" \
  "v_mul_f32 v198, 0xc1000000, v239\n\t" \
  "v_mul_f32 v195, 0x42800000, v238\n\t" \
  "v_mul_f32 v199, 0xc2800000, v239\n\t" \
  "v_fract_f32 v198, v198\n\t" \
  "v_fract_f32 v199, v199\n\t" \
  "v_exp_f32 v194, v194\n\t" \
  "v_exp_f32 v195, v195\n\t" \
  "v_cos_f32 v178, v198\n\t" \
  "v_sin_f32 v179, v198\n\t" \
  "v_cos_f32 v180, v199\n\t" \
  "s_add_u32 m0, s41, 36864\n\t" \
  "s_add_u32 s44, s42, 36864\n\t" \
  "buffer_load_dwordx4 v21, %0, s44 offen offset:1024 lds\n\t" \
  "v_sin_f32 v181, v199\n\t" \
  "s_nop 0\n\t" \
  "v_mul_f32 v178, v178, v194\n\t" \
  "v_mul_f32 v179, v179, v194\n\t" \
  "v_mul_f32 v180, v180, v195\n\t" \
  "v_mul_f32 v181, v181, v195\n\t" \
  "v_xor_b32 v186, 0x80000000, v179\n\t" \
  "v_mov_b32 v187, v178\n\t" \
  "v_xor_b32 v188, 0x80000000, v181\n\t" \
  "v_mov_b32 v189, v180\n\t" \
  "s_mov_b32 m0, s67\n\t" \
  "v_mov_b32 v122, 0x3f80\n\t" \
  "v_cvt_pk_bf16_f32 v123, v178, v179\n\t" \
  "v_cvt_pk_bf16_f32 v124, v180, v181\n\t" \
  "v_pk_mul_f32 v[106:107], v[178:179], v[178:179] op_sel:[0,0] op_sel_hi:[1,0]\n\t" \
  "ds_write_addtid_b32 v122 offset:11088\n\t" \
  "v_pk_mul_f32 v[108:109], v[180:181], v[180:181] op_sel:[0,0] op_sel_hi:[1,0]\n\t" \
  "v_pk_mul_f32 v[110:111], v[180:181], v[178:179] op_sel:[0,0] op_sel_hi:[1,0]\n\t" \
  "ds_write_addtid_b32 v123 offset:11616\n\t" \
  "v_pk_fma_f32 v[64:65], v[186:187], v[178:179], v[106:107] op_sel:[0,1,0] op_sel_hi:[1,1,1]\n\t" \
  "v_pk_fma_f32 v[76:77], v[188:189], v[180:181], v[108:109] op_sel:[0,1,0] op_sel_hi:[1,1,1]\n\t" \
  "ds_write_addtid_b32 v124 offset:15312\n\t" \
  "v_pk_fma_f32 v[80:81], v[188:189], v[178:179], v[110:111] op_sel:[0,1,0] op_sel_hi:[1,1,1]\n\t" \
  "s_add_u32 m0, s41, 36864\n\t" \
  "s_add_u32 s44, s42, 36864\n\t" \
  "buffer_load_dwordx4 v22, %0, s44 offen offset:2048 lds\n\t" \
  "s_mov_b32 m0, s67\n\t" \
  "v_cvt_pk_bf16_f32 v125, v64, v65\n\t" \
  "v_cvt_pk_bf16_f32 v126, v76, v77\n\t" \
  "v_cvt_pk_bf16_f32 v127, v80, v81\n\t" \
  "v_xor_b32 v78, 0x80000000, v65\n\t" \
  "v_mov_b32 v79, v64\n\t" \
  "v_pk_mul_f32 v[106:107], v[64:65], v[178:179] op_sel:[0,0] op_sel_hi:[1,0]\n\t" \
  "ds_write_addtid_b32 v125 offset:12144\n\t" \
  "v_pk_mul_f32 v[108:109], v[64:65], v[64:65] op_sel:[0,0] op_sel_hi:[1,0]\n\t" \
  "v_pk_mul_f32 v[110:111], v[180:181], v[76:77] op_sel:[0,0] op_sel_hi:[1,0]\n\t" \
  "ds_write_addtid_b32 v126 offset:19536\n\t" \
  "v_pk_mul_f32 v[112:113], v[180:181], v[80:81] op_sel:[0,0] op_sel_hi:[1,0]\n\t" \
  "v_pk_fma_f32 v[66:67], v[78:79], v[178:179], v[106:107] op_sel:[0,1,0] op_sel_hi:[1,1,1]\n\t" \
  "v_pk_fma_f32 v[68:69], v[78:79], v[64:65], v[108:109] op_sel:[0,1,0] op_sel_hi:[1,1,1]\n\t" \
  "ds_write_addtid_b32 v127 offset:15840\n\t" \
  "v_pk_fma_f32 v[76:77], v[188:189], v[76:77], v[110:111] op_sel:[0,1,0] op_sel_hi:[1,1,1]\n\t" \
  "v_pk_fma_f32 v[80:81], v[188:189], v[80:81], v[112:113] op_sel:[0,1,0] op_sel_hi:[1,1,1]\n\t" \
  "s_add_u32 m0, s41, 36864\n\t" \
  "s_add_u32 s44, s42, 36864\n\t" \
  "buffer_load_dwordx4 v23, %0, s44 offen offset:3072 lds\n\t" \
  "s_mov_b32 m0, s67\n\t" \
  "v_cvt_pk_bf16_f32 v128, v66, v67\n\t" \
  "v_cvt_pk_bf16_f32 v129, v68, v69\n\t" \
  "v_cvt_pk_bf16_f32 v122, v76, v77\n\t" \
  "v_cvt_pk_bf16_f32 v123, v80, v81\n\t" \
  "v_pk_mul_f32 v[106:107], v[64:65], v[66:67] op_sel:[0,0] op_sel_hi:[1,0]\n\t" \
  "ds_write_addtid_b32 v128 offset:12672\n\t" \
  "v_pk_mul_f32 v[108:109], v[64:65], v[68:69] op_sel:[0,0] op_sel_hi:[1,0]\n\t" \
  "v_pk_mul_f32 v[110:111], v[180:181], v[80:81] op_sel:[0,0] op_sel_hi:[1,0]\n\t" \
  "v_pk_mul_f32 v[112:113], v[180:181], v[64:65] op_sel:[0,0] op_sel_hi:[1,0]\n\t" \
  "ds_write_addtid_b32 v129 offset:13200\n\t" \
  "v_pk_mul_f32 v[114:115], v[180:181], v[66:67] op_sel:[0,0] op_sel_hi:[1,0]\n\t" \
  "v_pk_mul_f32 v[116:117], v[180:181], v[68:69] op_sel:[0,0] op_sel_hi:[1,0]\n\t" \
  "v_pk_fma_f32 v[70:71], v[78:79], v[66:67], v[106:107] op_sel:[0,1,0] op_sel_hi:[1,1,1]\n\t" \
  "ds_write_addtid_b32 v122 offset:23760\n\t" \
  "v_pk_fma_f32 v[72:73], v[78:79], v[68:69], v[108:109] op_sel:[0,1,0] op_sel_hi:[1,1,1]\n\t" \
  "v_pk_fma_f32 v[80:81], v[188:189], v[80:81], v[110:111] op_sel:[0,1,0] op_sel_hi:[1,1,1]\n\t" \
  "v_pk_fma_f32 v[82:83], v[188:189], v[64:65], v[112:113] op_sel:[0,1,0] op_sel_hi:[1,1,1]\n\t" \
  "ds_write_addtid_b32 v123 offset:20064\n\t" \
  "v_pk_fma_f32 v[84:85], v[188:189], v[66:67], v[114:115] op_sel:[0,1,0] op_sel_hi:[1,1,1]\n\t" \
  "v_pk_fma_f32 v[86:87], v[188:189], v[68:69], v[116:117] op_sel:[0,1,0] op_sel_hi:[1,1,1]\n\t" \
  "s_add_u32 m0, s40, 12288\n\t" \
  "s_add_u32 s43, s50, 294912\n\t" \
  "buffer_load_dwordx4 v17, %1, s43 offen offset:0 lds\n\t" \
  "s_mov_b32 m0, s67\n\t" \
  "v_cvt_pk_bf16_f32 v124, v70, v71\n\t" \
  "v_cvt_pk_bf16_f32 v125, v72, v73\n\t" \
  "v_cvt_pk_bf16_f32 v126, v80, v81\n\t" \
  "v_cvt_pk_bf16_f32 v127, v82, v83\n\t" \
  "v_cvt_pk_bf16_f32 v128, v84, v85\n\t" \
  "v_cvt_pk_bf16_f32 v129, v86, v87\n\t" \
  "v_pk_mul_f32 v[106:107], v[64:65], v[70:71] op_sel:[0,0] op_sel_hi:[1,0]\n\t" \
  "ds_write_addtid_b32 v124 offset:13728\n\t" \
  "v_pk_mul_f32 v[108:109], v[180:181], v[82:83] op_sel:[0,0] op_sel_hi:[1,0]\n\t" \
  "v_pk_mul_f32 v[110:111], v[180:181], v[84:85] op_sel:[0,0] op_sel_hi:[1,0]\n\t" \
  "ds_write_addtid_b32 v125 offset:14256\n\t" \
  "v_pk_mul_f32 v[112:113], v[180:181], v[86:87] op_sel:[0,0] op_sel_hi:[1,0]\n\t" \
  "v_pk_mul_f32 v[114:115], v[180:181], v[70:71] op_sel:[0,0] op_sel_hi:[1,0]\n\t" \
  "ds_write_addtid_b32 v126 offset:24288\n\t" \
  "v_pk_mul_f32 v[116:117], v[180:181], v[72:73] op_sel:[0,0] op_sel_hi:[1,0]\n\t" \
  "v_pk_fma_f32 v[74:75], v[78:79], v[70:71], v[106:107] op_sel:[0,1,0] op_sel_hi:[1,1,1]\n\t" \
  "ds_write_addtid_b32 v127 offset:16368\n\t" \
  "v_pk_fma_f32 v[82:83], v[188:189], v[82:83], v[108:109] op_sel:[0,1,0] op_sel_hi:[1,1,1]\n\t" \
  "v_pk_fma_f32 v[84:85], v[188:189], v[84:85], v[110:111] op_sel:[0,1,0] op_sel_hi:[1,1,1]\n\t" \
  "ds_write_addtid_b32 v128 offset:16896\n\t" \
  "v_pk_fma_f32 v[86:87], v[188:189], v[86:87], v[112:113] op_sel:[0,1,0] op_sel_hi:[1,1,1]\n\t" \
  "v_pk_fma_f32 v[88:89], v[188:189], v[70:71], v[114:115] op_sel:[0,1,0] op_sel_hi:[1,1,1]\n\t" \
  "ds_write_addtid_b32 v129 offset:17424\n\t" \
  "v_pk_fma_f32 v[90:91], v[188:189], v[72:73], v[116:117] op_sel:[0,1,0] op_sel_hi:[1,1,1]\n\t" \
  "s_add_u32 m0, s40, 12288\n\t" \
  "s_add_u32 s43, s50, 294912\n\t" \
  "buffer_load_dwordx4 v18, %1, s43 offen offset:1024 lds\n\t" \
  "s_mov_b32 m0, s67\n\t" \
  "v_cvt_pk_bf16_f32 v122, v74, v75\n\t" \
  "v_cvt_pk_bf16_f32 v123, v82, v83\n\t" \
  "v_cvt_pk_bf16_f32 v124, v84, v85\n\t" \
  "v_cvt_pk_bf16_f32 v125, v86, v87\n\t" \
  "v_cvt_pk_bf16_f32 v126, v88, v89\n\t" \
  "v_cvt_pk_bf16_f32 v127, v90, v91\n\t" \
  "v_pk_mul_f32 v[106:107], v[180:181], v[74:75] op_sel:[0,0] op_sel_hi:[1,0]\n\t" \
  "ds_write_addtid_b32 v122 offset:14784\n\t" \
  "v_pk_mul_f32 v[108:109], v[180:181], v[82:83] op_sel:[0,0] op_sel_hi:[1,0]\n\t" \
  "v_pk_mul_f32 v[110:111], v[180:181], v[84:85] op_sel:[0,0] op_sel_hi:[1,0]\n\t" \
  "ds_write_addtid_b32 v123 offset:20592\n\t" \
  "v_pk_mul_f32 v[112:113], v[180:181], v[86:87] op_sel:[0,0] op_sel_hi:[1,0]\n\t" \
  "v_pk_mul_f32 v[114:115], v[180:181], v[88:89] op_sel:[0,0] op_sel_hi:[1,0]\n\t" \
  "ds_write_addtid_b32 v124 offset:21120\n\t" \
  "v_pk_mul_f32 v[116:117], v[180:181], v[90:91] op_sel:[0,0] op_sel_hi:[1,0]\n\t" \
  "v_pk_fma_f32 v[92:93], v[188:189], v[74:75], v[106:107] op_sel:[0,1,0] op_sel_hi:[1,1,1]\n\t" \
  "ds_write_addtid_b32 v125 offset:21648\n\t" \
  "v_pk_fma_f32 v[82:83], v[188:189], v[82:83], v[108:109] op_sel:[0,1,0] op_sel_hi:[1,1,1]\n\t" \
  "v_pk_fma_f32 v[84:85], v[188:189], v[84:85], v[110:111] op_sel:[0,1,0] op_sel_hi:[1,1,1]\n\t" \
  "ds_write_addtid_b32 v126 offset:17952\n\t" \
  "v_pk_fma_f32 v[86:87], v[188:189], v[86:87], v[112:113] op_sel:[0,1,0] op_sel_hi:[1,1,1]\n\t" \
  "v_pk_fma_f32 v[88:89], v[188:189], v[88:89], v[114:115] op_sel:[0,1,0] op_sel_hi:[1,1,1]\n\t" \
  "ds_write_addtid_b32 v127 offset:18480\n\t" \
  "v_pk_fma_f32 v[90:91], v[188:189], v[90:91], v[116:117] op_sel:[0,1,0] op_sel_hi:[1,1,1]\n\t" \
  "s_add_u32 m0, s40, 12288\n\t" \
  "s_add_u32 s43, s50, 294912\n\t" \
  "buffer_load_dwordx4 v19, %1, s43 offen offset:2048 lds\n\t" \
  "s_mov_b32 m0, s67\n\t" \
  "v_cvt_pk_bf16_f32 v128, v92, v93\n\t" \
  "v_cvt_pk_bf16_f32 v129, v82, v83\n\t" \
  "v_cvt_pk_bf16_f32 v122, v84, v85\n\t" \
  "v_cvt_pk_bf16_f32 v123, v86, v87\n\t" \
  "v_cvt_pk_bf16_f32 v124, v88, v89\n\t" \
  "v_cvt_pk_bf16_f32 v125, v90, v91\n\t" \
  "v_pk_mul_f32 v[106:107], v[180:181], v[92:93] op_sel:[0,0] op_sel_hi:[1,0]\n\t" \
  "ds_write_addtid_b32 v128 offset:19008\n\t" \
  "v_pk_mul_f32 v[108:109], v[180:181], v[88:89] op_sel:[0,0] op_sel_hi:[1,0]\n\t" \
  "ds_write_addtid_b32 v129 offset:24816\n\t" \
  "v_pk_mul_f32 v[110:111], v[180:181], v[90:91] op_sel:[0,0] op_sel_hi:[1,0]\n\t" \
  "ds_write_addtid_b32 v122 offset:25344\n\t" \
  "v_pk_fma_f32 v[92:93], v[188:189], v[92:93], v[106:107] op_sel:[0,1,0] op_sel_hi:[1,1,1]\n\t" \
  "ds_write_addtid_b32 v123 offset:25872\n\t" \
  "v_pk_fma_f32 v[88:89], v[188:189], v[88:89], v[108:109] op_sel:[0,1,0] op_sel_hi:[1,1,1]\n\t" \
  "ds_write_addtid_b32 v124 offset:22176\n\t" \
  "v_pk_fma_f32 v[90:91], v[188:189], v[90:91], v[110:111] op_sel:[0,1,0] op_sel_hi:[1,1,1]\n\t" \
  "ds_write_addtid_b32 v125 offset:22704\n\t" \
  "s_add_u32 m0, s41, 65536\n\t" \
  "s_add_u32 s44, s42, 128\n\t" \
  "buffer_load_dwordx4 v20, %0, s44 offen offset:0 lds\n\t" \
  "s_mov_b32 m0, s67\n\t" \
  "v_cvt_pk_bf16_f32 v126, v92, v93\n\t" \
  "v_cvt_pk_bf16_f32 v127, v88, v89\n\t" \
  "v_cvt_pk_bf16_f32 v128, v90, v91\n\t" \
  "v_pk_mul_f32 v[106:107], v[180:181], v[92:93] op_sel:[0,0] op_sel_hi:[1,0]\n\t" \
  "ds_write_addtid_b32 v126 offset:23232\n\t" \
  "ds_write_addtid_b32 v127 offset:26400\n\t" \
  "v_pk_fma_f32 v[92:93], v[188:189], v[92:93], v[106:107] op_sel:[0,1,0] op_sel_hi:[1,1,1]\n\t" \
  "ds_write_addtid_b32 v128 offset:26928\n\t" \
  "s_add_u32 m0, s41, 65536\n\t" \
  "s_add_u32 s44, s42, 128\n\t" \
  "buffer_load_dwordx4 v21, %0, s44 offen offset:1024 lds\n\t" \
  "s_mov_b32 m0, s67\n\t" \
  "v_cvt_pk_bf16_f32 v129, v92, v93\n\t" \
  "s_nop 0\n\t" \
  "ds_write_addtid_b32 v129 offset:27456\n\t" \
  "s_add_u32 m0, s41, 65536\n\t" \
  "s_add_u32 s44, s42, 128\n\t" \
  "buffer_load_dwordx4 v22, %0, s44 offen offset:2048 lds\n\t" \
  "s_add_u32 m0, s41, 65536\n\t" \
  "s_add_u32 s44, s42, 128\n\t" \
  "buffer_load_dwordx4 v23, %0, s44 offen offset:3072 lds\n\t" \
  "s_branch L_tab_%=\n\t" \
  "L_rows_%=:\n\t" \
  "v_mul_f32 v194, 0x3fb8aa3b, v236\n\t" \
  "v_rndne_f32 v195, v194\n\t" \
  "v_fma_f32 v196, v236, s68, -v194\n\t" \
  "v_sub_f32 v194, v194, v195\n\t" \
  "v_fmac_f32 v196, 0x32a5705f, v236\n\t" \
  "v_cvt_i32_f32 v195, v195\n\t" \
  "v_add_f32 v194, v194, v196\n\t" \
  "v_exp_f32 v194, v194\n\t" \
  "s_nop 0\n\t" \
  "v_ldexp_f32 v194, v194, v195\n\t" \
  "v_cvt_pk_bf16_f32 v194, v194, v194\n\t" \
  "v_and_b32 v194, 0xffff0000, v194\n\t" \
  "v_mul_f32 v238, v194, v230\n\t" \
  "s_add_u32 m0, s41, 36864\n\t" \
  "s_add_u32 s44, s42, 36864\n\t" \
  "buffer_load_dwordx4 v20, %0, s44 offen offset:0 lds\n\t" \
  "v_mul_f32 v239, v194, v231\n\t" \
  "v_mul_f32 v195, v194, v232\n\t" \
  "v_mul_f32 v196, v194, v233\n\t" \
  "v_mul_f32 v238, 0x3fb8aa3b, v238\n\t" \
  "v_mul_f32 v239, 0.15915494, v239\n\t" \
  "v_mul_f32 v240, v234, v195\n\t" \
  "v_mul_f32 v241, v234, v196\n\t" \
  "v_fma_f32 v240, -v235, v196, v240\n\t" \
  "v_fmac_f32 v241, v235, v195\n\t" \
  "v_mul_f32 v194, 0x3f800000, v238\n\t" \
  "v_mul_f32 v198, 0x3f800000, v239\n\t" \
  "v_mul_f32 v195, 0x43800000, v238\n\t" \
  "v_mul_f32 v199, 0x43800000, v239\n\t" \
  "v_fract_f32 v198, v198\n\t" \
  "s_add_u32 m0, s41, 36864\n\t" \
  "s_add_u32 s44, s42, 36864\n\t" \
  "buffer_load_dwordx4 v21, %0, s44 offen offset:1024 lds\n\t" \
  "v_fract_f32 v199, v199\n\t" \
  "v_exp_f32 v194, v194\n\t" \
  "v_exp_f32 v195, v195\n\t" \
  "v_cos_f32 v178, v198\n\t" \
  "v_sin_f32 v179, v198\n\t" \
  "v_cos_f32 v180, v199\n\t" \
  "v_sin_f32 v181, v199\n\t" \
  "s_nop 0\n\t" \
  "v_mul_f32 v178, v178, v194\n\t" \
  "v_mul_f32 v179, v179, v194\n\t" \
  "v_mul_f32 v180, v180, v195\n\t" \
  "v_mul_f32 v181, v181, v195\n\t" \
  "v_xor_b32 v186, 0x80000000, v179\n\t" \
  "v_mov_b32 v187, v178\n\t" \
  "v_xor_b32 v188, 0x80000000, v181\n\t" \
  "v_mov_b32 v189, v180\n\t" \
  "s_mov_b32 m0, s67\n\t" \
  "v_cvt_pk_bf16_f32 v122, v240, v241\n\t" \
  "v_pk_mul_f32 v[106:107], v[178:179], v[240:241] op_sel:[0,0] op_sel_hi:[1,0]\n\t" \
  "ds_write_addtid_b32 v122 offset:0\n\t" \
  "v_pk_mul_f32 v[108:109], v[180:181], v[240:241] op_sel:[0,0] op_sel_hi:[1,0]\n\t" \
  "v_pk_fma_f32 v[64:65], v[186:187], v[240:241], v[106:107] op_sel:[0,1,0] op_sel_hi:[1,1,1]\n\t" \
  "v_pk_fma_f32 v[78:79], v[188:189], v[240:241], v[108:109] op_sel:[0,1,0] op_sel_hi:[1,1,1]\n\t" \
  "s_add_u32 m0, s41, 36864\n\t" \
  "s_add_u32 s44, s42, 36864\n\t" \
  "buffer_load_dwordx4 v22, %0, s44 offen offset:2048 lds\n\t" \
  "s_mov_b32 m0, s67\n\t" \
  "v_cvt_pk_bf16_f32 v123, v64, v65\n\t" \
  "v_cvt_pk_bf16_f32 v124, v78, v79\n\t" \
  "v_pk_mul_f32 v[106:107], v[178:179], v[64:65] op_sel:[0,0] op_sel_hi:[1,0]\n\t" \
  "ds_write_addtid_b32 v123 offset:528\n\t" \
  "v_pk_mul_f32 v[108:109], v[180:181], v[78:79] op_sel:[0,0] op_sel_hi:[1,0]\n\t" \
  "v_pk_fma_f32 v[66:67], v[186:187], v[64:65], v[106:107] op_sel:[0,1,0] op_sel_hi:[1,1,1]\n\t" \
  "ds_write_addtid_b32 v124 offset:4224\n\t" \
  "v_pk_fma_f32 v[78:79], v[188:189], v[78:79], v[108:109] op_sel:[0,1,0] op_sel_hi:[1,1,1]\n\t" \
  "s_add_u32 m0, s41, 36864\n\t" \
  "s_add_u32 s44, s42, 36864\n\t" \
  "buffer_load_dwordx4 v23, %0, s44 offen offset:3072 lds\n\t" \
  "s_mov_b32 m0, s67\n\t" \
  "v_cvt_pk_bf16_f32 v125, v66, v67\n\t" \
  "v_cvt_pk_bf16_f32 v126, v78, v79\n\t" \
  "v_pk_mul_f32 v[106:107], v[178:179], v[66:67] op_sel:[0,0] op_sel_hi:[1,0]\n\t" \
  "ds_write_addtid_b32 v125 offset:1056\n\t" \
  "v_pk_mul_f32 v[108:109], v[180:181], v[78:79] op_sel:[0,0] op_sel_hi:[1,0]\n\t" \
  "v_pk_mul_f32 v[110:111], v[180:181], v[66:67] op_sel:[0,0] op_sel_hi:[1,0]\n\t" \
  "v_pk_fma_f32 v[68:69], v[186:187], v[66:67], v[106:107] op_sel:[0,1,0] op_sel_hi:[1,1,1]\n\t" \
  "ds_write_addtid_b32 v126 offset:6336\n\t" \
  "v_pk_fma_f32 v[78:79], v[188:189], v[78:79], v[108:109] op_sel:[0,1,0] op_sel_hi:[1,1,1]\n\t" \
  "v_pk_fma_f32 v[80:81], v[188:189], v[66:67], v[110:111] op_sel:[0,1,0] op_sel_hi:[1,1,1]\n\t" \
  "s_add_u32 m0, s40, 12288\n\t" \
  "s_add_u32 s43, s50, 294912\n\t" \
  "buffer_load_dwordx4 v17, %1, s43 offen offset:0 lds\n\t" \
  "s_mov_b32 m0, s67\n\t" \
  "v_cvt_pk_bf16_f32 v127, v68, v69\n\t" \
  "v_cvt_pk_bf16_f32 v128, v78, v79\n\t" \
  "v_cvt_pk_bf16_f32 v129, v80, v81\n\t" \
  "v_pk_mul_f32 v[106:107], v[178:179], v[68:69] op_sel:[0,0] op_sel_hi:[1,0]\n\t" \
  "ds_write_addtid_b32 v127 offset:1584\n\t" \
  "v_pk_mul_f32 v[108:109], v[180:181], v[78:79] op_sel:[0,0] op_sel_hi:[1,0]\n\t" \
  "ds_write_addtid_b32 v128 offset:7392\n\t" \
  "v_pk_fma_f32 v[70:71], v[186:187], v[68:69], v[106:107] op_sel:[0,1,0] op_sel_hi:[1,1,1]\n\t" \
  "ds_write_addtid_b32 v129 offset:4752\n\t" \
  "v_pk_fma_f32 v[78:79], v[188:189], v[78:79], v[108:109] op_sel:[0,1,0] op_sel_hi:[1,1,1]\n\t" \
  "s_add_u32 m0, s40, 12288\n\t" \
  "s_add_u32 s43, s50, 294912\n\t" \
  "buffer_load_dwordx4 v18, %1, s43 offen offset:1024 lds\n\t" \
  "s_mov_b32 m0, s67\n\t" \
  "v_cvt_pk_bf16_f32 v122, v70, v71\n\t" \
  "v_cvt_pk_bf16_f32 v123, v78, v79\n\t" \
  "v_pk_mul_f32 v[106:107], v[178:179], v[70:71] op_sel:[0,0] op_sel_hi:[1,0]\n\t" \
  "ds_write_addtid_b32 v122 offset:2112\n\t" \
  "v_pk_mul_f32 v[108:109], v[180:181], v[78:79] op_sel:[0,0] op_sel_hi:[1,0]\n\t" \
  "v_pk_mul_f32 v[110:111], v[180:181], v[70:71] op_sel:[0,0] op_sel_hi:[1,0]\n\t" \
  "v_pk_fma_f32 v[72:73], v[186:187], v[70:71], v[106:107] op_sel:[0,1,0] op_sel_hi:[1,1,1]\n\t" \
  "ds_write_addtid_b32 v123 offset:8448\n\t" \
  "v_pk_fma_f32 v[78:79], v[188:189], v[78:79], v[108:109] op_sel:[0,1,0] op_sel_hi:[1,1,1]\n\t" \
  "v_pk_fma_f32 v[82:83], v[188:189], v[70:71], v[110:111] op_sel:[0,1,0] op_sel_hi:[1,1,1]\n\t" \
  "s_add_u32 m0, s40, 12288\n\t" \
  "s_add_u32 s43, s50, 294912\n\t" \
  "buffer_load_dwordx4 v19, %1, s43 offen offset:2048 lds\n\t" \
  "s_mov_b32 m0, s67\n\t" \
  "v_cvt_pk_bf16_f32 v124, v72, v73\n\t" \
  "v_cvt_pk_bf16_f32 v125, v78, v79\n\t" \
  "v_cvt_pk_bf16_f32 v126, v82, v83\n\t" \
  "v_pk_mul_f32 v[106:107], v[178:179], v[72:73] op_sel:[0,0] op_sel_hi:[1,0]\n\t" \
  "ds_write_addtid_b32 v124 offset:2640\n\t" \
  "v_pk_mul_f32 v[108:109], v[180:181], v[78:79] op_sel:[0,0] op_sel_hi:[1,0]\n\t" \
  "v_pk_mul_f32 v[110:111], v[180:181], v[82:83] op_sel:[0,0] op_sel_hi:[1,0]\n\t" \
  "ds_write_addtid_b32 v125 offset:8976\n\t" \
  "v_pk_fma_f32 v[74:75], v[186:187], v[72:73], v[106:107] op_sel:[0,1,0] op_sel_hi:[1,1,1]\n\t" \
  "v_pk_fma_f32 v[78:79], v[188:189], v[78:79], v[108:109] op_sel:[0,1,0] op_sel_hi:[1,1,1]\n\t" \
  "ds_write_addtid_b32 v126 offset:5280\n\t" \
  "v_pk_fma_f32 v[82:83], v[188:189], v[82:83], v[110:111] op_sel:[0,1,0] op_sel_hi:[1,1,1]\n\t" \
  "s_add_u32 m0, s41, 65536\n\t" \
  "s_add_u32 s44, s42, 128\n\t" \
  "buffer_load_dwordx4 v20, %0, s44 offen offset:0 lds\n\t" \
  "s_mov_b32 m0, s67\n\t" \
  "v_cvt_pk_bf16_f32 v127, v74, v75\n\t" \
  "v_cvt_pk_bf16_f32 v128, v78, v79\n\t" \
  "v_cvt_pk_bf16_f32 v129, v82, v83\n\t" \
  "v_pk_mul_f32 v[106:107], v[178:179], v[74:75] op_sel:[0,0] op_sel_hi:[1,0]\n\t" \
  "ds_write_addtid_b32 v127 offset:3168\n\t" \
  "v_pk_mul_f32 v[108:109], v[180:181], v[78:79] op_sel:[0,0] op_sel_hi:[1,0]\n\t" \
  "v_pk_mul_f32 v[110:111], v[180:181], v[82:83] op_sel:[0,0] op_sel_hi:[1,0]\n\t" \
  "ds_write_addtid_b32 v128 offset:9504\n\t" \
  "v_pk_mul_f32 v[112:113], v[180:181], v[74:75] op_sel:[0,0] op_sel_hi:[1,0]\n\t" \
  "v_pk_fma_f32 v[76:77], v[186:187], v[74:75], v[106:107] op_sel:[0,1,0] op_sel_hi:[1,1,1]\n\t" \
  "v_pk_fma_f32 v[78:79], v[188:189], v[78:79], v[108:109] op_sel:[0,1,0] op_sel_hi:[1,1,1]\n\t" \
  "ds_write_addtid_b32 v129 offset:6864\n\t" \
  "v_pk_fma_f32 v[82:83], v[188:189], v[82:83], v[110:111] op_sel:[0,1,0] op_sel_hi:[1,1,1]\n\t" \
  "v_pk_fma_f32 v[84:85], v[188:189], v[74:75], v[112:113] op_sel:[0,1,0] op_sel_hi:[1,1,1]\n\t" \
  "s_add_u32 m0, s41, 65536\n\t" \
  "s_add_u32 s44, s42, 128\n\t" \
  "buffer_load_dwordx4 v21, %0, s44 offen offset:1024 lds\n\t" \
  "s_mov_b32 m0, s67\n\t" \
  "v_cvt_pk_bf16_f32 v122, v76, v77\n\t" \
  "v_cvt_pk_bf16_f32 v123, v78, v79\n\t" \
  "v_cvt_pk_bf16_f32 v124, v82, v83\n\t" \
  "v_cvt_pk_bf16_f32 v125, v84, v85\n\t" \
  "v_pk_mul_f32 v[106:107], v[180:181], v[78:79] op_sel:[0,0] op_sel_hi:[1,0]\n\t" \
  "ds_write_addtid_b32 v122 offset:3696\n\t" \
  "ds_write_addtid_b32 v123 offset:10032\n\t" \
  "v_pk_fma_f32 v[78:79], v[188:189], v[78:79], v[106:107] op_sel:[0,1,0] op_sel_hi:[1,1,1]\n\t" \
  "ds_write_addtid_b32 v124 offset:7920\n\t" \
  "ds_write_addtid_b32 v125 offset:5808\n\t" \
  "s_add_u32 m0, s41, 65536\n\t" \
  "s_add_u32 s44, s42, 128\n\t" \
  "buffer_load_dwordx4 v22, %0, s44 offen offset:2048 lds\n\t" \
  "s_mov_b32 m0, s67\n\t" \
  "v_cvt_pk_bf16_f32 v126, v78, v79\n\t" \
  "s_nop 0\n\t" \
  "ds_write_addtid_b32 v126 offset:10560\n\t" \
  "s_add_u32 m0, s41, 65536\n\t" \
  "s_add_u32 s44, s42, 128\n\t" \
  "buffer_load_dwordx4 v23, %0, s44 offen offset:3072 lds\n\t" \
  "L_tab_%=:\n\t" \
  "v_lshrrev_b32 v33, 4, v16\n\t" \
  "v_and_b32 v61, 15, v16\n\t" \
  "v_mul_u32_u24 v242, 528, v61\n\t" \
  "v_lshrrev_b32 v62, 1, v33\n\t" \
  "v_lshl_add_u32 v242, v62, 7, v242\n\t" \
  "v_and_b32 v62, 1, v33\n\t" \
  "v_lshl_add_u32 v242, v62, 8, v242\n\t" \
  "s_and_b32 s54, %7, 1\n\t" \
  "s_mul_i32 s55, s54, 8448\n\t" \
  "s_add_u32 s55, s55, 47952\n\t" \
  "v_add_u32 v243, s55, v242\n\t" \
  "s_lshr_b32 s56, %7, 1\n\t" \
  "s_mul_i32 s55, s56, 8448\n\t" \
  "s_add_u32 s55, s55, 36864\n\t" \
  "v_add_u32 v242, s55, v242\n\t" \
  "s_waitcnt lgkmcnt(0)\n\t" \
  "s_barrier\n\t" \
  "ds_read_b128 v[64:67], v243 offset:0\n\t" \
  "v_lshrrev_b32 v62, 1, v61\n\t" \
  "v_xor_b32 v62, v62, v33\n\t" \
  "ds_read_b128 v[68:71], v242 offset:0\n\t" \
  "v_lshlrev_b32 v62, 4, v62\n\t" \
  "v_lshl_add_u32 v30, v61, 7, v62\n\t" \
  "ds_read_b128 v[72:75], v243 offset:16\n\t" \
  "s_add_u32 s55, s41, 32768\n\t" \
  "v_add_u32 v30, s55, v30\n\t" \
  "ds_read_b128 v[76:79], v242 offset:16\n\t" \
  "v_xor_b32 v31, 64, v30\n\t" \
  "v_lshlrev_b32 v35, 4, v33\n\t" \
  "ds_read_b128 v[80:83], v243 offset:32\n\t" \
  "v_lshlrev_b32 v60, 3, v61\n\t" \
  "v_lshlrev_b32 v32, 4, v16\n\t" \
  "ds_read_b128 v[84:87], v242 offset:32\n\t" \
  "v_lshrrev_b32 v33, 4, v16\n\t" \
  "v_bfe_u32 v61, v16, 2, 2\n\t" \
  "ds_read_b128 v[88:91], v243 offset:48\n\t" \
  "v_lshlrev_b32 v61, 6, v61\n\t" \
  "v_and_b32 v25, 3, v16\n\t" \
  "ds_read_b128 v[92:95], v242 offset:48\n\t" \
  "v_lshl_add_u32 v61, v25, 3, v61\n\t" \
  "v_lshrrev_b32 v25, 5, v16\n\t" \
  "s_waitcnt lgkmcnt(6)\n\t" \
  "v_mfma_f32_16x16x32_bf16 v[170:173], v[68:71], v[64:67], 0\n\t" \
  "ds_read_b128 v[96:99], v243 offset:64\n\t" \
  "v_mul_u32_u24 v25, 3072, v25\n\t" \
  "v_add_u32 v61, v61, v25\n\t" \
  "ds_read_b128 v[100:103], v242 offset:64\n\t" \
  "v_and_b32 v33, 1, v33\n\t" \
  "v_mul_u32_u24 v33, 544, v33\n\t" \
  "s_waitcnt lgkmcnt(6)\n\t" \
  "v_mfma_f32_16x16x32_bf16 v[170:173], v[76:79], v[72:75], v[170:173]\n\t" \
  "ds_read_b128 v[104:107], v243 offset:80\n\t" \
  "v_add_u32 v61, v61, v33\n\t" \
  "v_mov_b32 v24, v61\n\t" \
  "ds_read_b128 v[108:111], v242 offset:80\n\t" \
  "v_xor_b32 v25, 32, v61\n\t" \
  "s_lshl_b32 s73, s69, 2\n\t" \
  "s_waitcnt lgkmcnt(6)\n\t" \
  "v_mfma_f32_16x16x32_bf16 v[170:173], v[84:87], v[80:83], v[170:173]\n\t" \
  "ds_read_b128 v[112:115], v243 offset:96\n\t" \
  "s_add_u32 s73, s73, %7\n\t" \
  "s_lshl_b32 s73, s73, 6\n\t" \
  "ds_read_b128 v[116:119], v242 offset:96\n\t" \
  "s_add_u32 s73, s73, 917504\n\t" \
  "s_lshl_b32 s69, s69, 1\n\t" \
  "s_waitcnt lgkmcnt(6)\n\t" \
  "v_mfma_f32_16x16x32_bf16 v[170:173], v[92:95], v[88:91], v[170:173]\n\t" \
  "ds_read_b128 v[120:123], v243 offset:112\n\t" \
  "s_add_u32 s69, s69, s54\n\t" \
  "s_lshl_b32 s69, s69, 10\n\t" \
  "ds_read_b128 v[124:127], v242 offset:112\n\t" \
  "s_lshl_b32 s56, s56, 3\n\t" \
  "s_add_u32 s69, s69, s56\n\t" \
  "s_waitcnt lgkmcnt(6)\n\t" \
  "v_mfma_f32_16x16x32_bf16 v[170:173], v[100:103], v[96:99], v[170:173]\n\t" \
  "s_add_u32 s69, s69, 4194304\n\t" \
  "s_waitcnt lgkmcnt(4)\n\t" \
  "v_mfma_f32_16x16x32_bf16 v[170:173], v[108:111], v[104:107], v[170:173]\n\t" \
  "s_waitcnt lgkmcnt(2)\n\t" \
  "v_mfma_f32_16x16x32_bf16 v[170:173], v[116:119], v[112:115], v[170:173]\n\t" \
  "s_waitcnt lgkmcnt(0)\n\t" \
  "v_mfma_f32_16x16x32_bf16 v[170:173], v[124:127], v[120:123], v[170:173]\n\t" \
  "s_nop 7\n\t" \
  "v_cvt_pk_bf16_f32 v160, v170, v171\n\t" \
  "v_cvt_pk_bf16_f32 v161, v172, v173\n\t" \
  "buffer_store_dwordx2 v[160:161], v32, %3, s69 offen sc1\n\t" \
  "s_waitcnt vmcnt(8)\n\t" \
  "s_barrier\n\t" \
  "ds_read_b128 a[216:219], v30 offset:0\n\t" \
  "ds_read_b64_tr_b16 a[192:193], v24 offset:0\n\t" \
  "ds_read_b64_tr_b16 a[194:195], v24 offset:256\n\t" \
  "ds_read_b128 a[220:223], v30 offset:2048\n\t" \
  "ds_read_b64_tr_b16 a[196:197], v25 offset:0\n\t" \
  "ds_read_b64_tr_b16 a[198:199], v25 offset:256\n\t" \
  "ds_read_b128 a[224:227], v30 offset:4096\n\t" \
  "ds_read_b128 a[228:231], v30 offset:6144\n\t" \
  "ds_read_b64_tr_b16 a[200:201], v24 offset:6144\n\t" \
  "ds_read_b64_tr_b16 a[202:203], v24 offset:6400\n\t" \
  "ds_read_b64_tr_b16 a[204:205], v25 offset:6144\n\t" \
  "ds_read_b64_tr_b16 a[206:207], v25 offset:6400\n\t" \
  "s_waitcnt lgkmcnt(4)\n\t" \
  "v_mfma_f32_16x16x32_bf16 v[64:67], a[192:195], a[216:219], 0\n\t" \
  "ds_read_b64_tr_b16 a[208:209], v24 offset:1024\n\t" \
  "ds_read_b64_tr_b16 a[210:211], v24 offset:1280\n\t" \
  "v_and_b32 v57, 31, v16\n\t" \
  "v_mfma_f32_16x16x32_bf16 v[68:71], a[192:195], a[220:223], 0\n\t" \
  "s_add_u32 m0, s41, 69632\n\t" \
  "s_add_u32 s44, s42, 36992\n\t" \
  "v_lshrrev_b32 v38, 5, v16\n\t" \
  "v_mfma_f32_16x16x32_bf16 v[72:75], a[192:195], a[224:227], 0\n\t" \
  "buffer_load_dwordx4 v20, %0, s44 offen offset:0 lds\n\t" \
  "v_mul_u32_u24 v38, 82944, v38\n\t" \
  "v_mfma_f32_16x16x32_bf16 v[76:79], a[192:195], a[228:231], 0\n\t" \
  "v_lshl_add_u32 v56, v57, 4, v38\n\t" \
  "v_mov_b32 v39, 0x70000000\n\t" \
  "v_cmp_gt_u32 vcc, 24, v57\n\t" \
  "s_nop 1\n\t" \
  "v_cndmask_b32 v56, v39, v56, vcc\n\t" \
  "v_mfma_f32_16x16x32_bf16 v[80:83], a[196:199], a[216:219], 0\n\t" \
  "ds_read_b64_tr_b16 a[212:213], v25 offset:1024\n\t" \
  "ds_read_b64_tr_b16 a[214:215], v25 offset:1280\n\t" \
  "v_mfma_f32_16x16x32_bf16 v[84:87], a[196:199], a[220:223], 0\n\t" \
  "buffer_load_dwordx4 v21, %0, s44 offen offset:1024 lds\n\t" \
  "v_mfma_f32_16x16x32_bf16 v[88:91], a[196:199], a[224:227], 0\n\t" \
  "s_lshl_b32 s60, %4, 2\n\t" \
  "s_add_u32 s60, s60, %7\n\t" \
  "ds_read_b128 a[232:235], v31 offset:0\n\t" \
  "v_mfma_f32_16x16x32_bf16 v[92:95], a[196:199], a[228:231], 0\n\t" \
  "s_mul_i32 s60, s60, 165888\n\t" \
  "ds_read_b128 a[236:239], v31 offset:2048\n\t" \
  "s_waitcnt lgkmcnt(6)\n\t" \
  "v_mfma_f32_16x16x32_bf16 v[96:99], a[200:203], a[216:219], 0\n\t" \
  "ds_read_b64_tr_b16 a[192:193], v24 offset:7168\n\t" \
  "ds_read_b64_tr_b16 a[194:195], v24 offset:7424\n\t" \
  "ds_read_b128 a[240:243], v31 offset:4096\n\t" \
  "v_mfma_f32_16x16x32_bf16 v[100:103], a[200:203], a[220:223], 0\n\t" \
  "buffer_load_dwordx4 v22, %0, s44 offen offset:2048 lds\n\t" \
  "v_mfma_f32_16x16x32_bf16 v[104:107], a[200:203], a[224:227], 0\n\t" \
  "s_add_u32 s60, s60, s50\n\t" \
  "s_add_u32 s60, s60, 4096\n\t" \
  "v_mfma_f32_16x16x32_bf16 v[108:111], a[200:203], a[228:231], 0\n\t" \
  "buffer_load_dwordx4 v23, %0, s44 offen offset:3072 lds\n\t" \
  "v_mfma_f32_16x16x32_bf16 v[112:115], a[204:207], a[216:219], 0\n\t" \
  "ds_read_b64_tr_b16 a[196:197], v25 offset:7168\n\t" \
  "ds_read_b64_tr_b16 a[198:199], v25 offset:7424\n\t" \
  "v_mfma_f32_16x16x32_bf16 v[116:119], a[204:207], a[220:223], 0\n\t" \
  "s_add_u32 m0, s40, 24576\n\t" \
  "s_add_u32 s43, s50, 589824\n\t" \
  "v_mfma_f32_16x16x32_bf16 v[120:123], a[204:207], a[224:227], 0\n\t" \
  "buffer_load_dwordx4 v17, %1, s43 offen offset:0 lds\n\t" \
  "v_mfma_f32_16x16x32_bf16 v[124:127], a[204:207], a[228:231], 0\n\t" \
  "s_add_u32 s61, s60, 0\n\t" \
  "s_waitcnt lgkmcnt(7)\n\t" \
  "v_mfma_f32_16x16x32_bf16 v[128:131], a[208:211], a[216:219], 0\n\t" \
  "ds_read_b64_tr_b16 a[200:201], v24 offset:2048\n\t" \
  "ds_read_b64_tr_b16 a[202:203], v24 offset:2304\n\t" \
  "v_mfma_f32_16x16x32_bf16 v[132:135], a[208:211], a[220:223], 0\n\t" \
  "buffer_load_dwordx4 a[128:131], v56, %1, s61 offen\n\t" \
  "v_mfma_f32_16x16x32_bf16 v[136:139], a[208:211], a[224:227], 0\n\t" \
  "buffer_load_dwordx4 v18, %1, s43 offen offset:1024 lds\n\t" \
  "v_mfma_f32_16x16x32_bf16 v[140:143], a[208:211], a[228:231], 0\n\t" \
  "s_add_u32 s61, s60, 9216\n\t" \
  "ds_read_b128 a[244:247], v31 offset:6144\n\t" \
  "v_mfma_f32_16x16x32_bf16 v[144:147], a[212:215], a[216:219], 0\n\t" \
  "ds_read_b64_tr_b16 a[204:205], v25 offset:2048\n\t" \
  "ds_read_b64_tr_b16 a[206:207], v25 offset:2304\n\t" \
  "v_mfma_f32_16x16x32_bf16 v[148:151], a[212:215], a[220:223], 0\n\t" \
  "buffer_load_dwordx4 v19, %1, s43 offen offset:2048 lds\n\t" \
  "v_mfma_f32_16x16x32_bf16 v[152:155], a[212:215], a[224:227], 0\n\t" \
  "s_add_u32 m0, s40, 36864\n\t" \
  "s_add_u32 s43, s50, 884736\n\t" \
  "v_mfma_f32_16x16x32_bf16 v[156:159], a[212:215], a[228:231], 0\n\t" \
  "buffer_load_dwordx4 v17, %1, s43 offen offset:0 lds\n\t" \
  "s_waitcnt lgkmcnt(5)\n\t" \
  "v_mfma_f32_16x16x32_bf16 v[160:163], a[192:195], a[216:219], 0\n\t" \
  "ds_read_b64_tr_b16 a[208:209], v24 offset:8192\n\t" \
  "ds_read_b64_tr_b16 a[210:211], v24 offset:8448\n\t" \
  "v_mfma_f32_16x16x32_bf16 v[164:167], a[192:195], a[220:223], 0\n\t" \
  "buffer_load_dwordx4 a[132:135], v56, %1, s61 offen\n\t" \
  "s_add_u32 s61, s60, 18432\n\t" \
  "v_mfma_f32_16x16x32_bf16 v[168:171], a[192:195], a[224:227], 0\n\t" \
  "buffer_load_dwordx4 v18, %1, s43 offen offset:1024 lds\n\t" \
  "v_mfma_f32_16x16x32_bf16 v[172:175], a[192:195], a[228:231], 0\n\t" \
  "buffer_load_dwordx4 a[136:139], v56, %1, s61 offen\n\t" \
  "v_mfma_f32_16x16x32_bf16 v[176:179], a[196:199], a[216:219], 0\n\t" \
  "ds_read_b64_tr_b16 a[212:213], v25 offset:8192\n\t" \
  "ds_read_b64_tr_b16 a[214:215], v25 offset:8448\n\t" \
  "v_mfma_f32_16x16x32_bf16 v[180:183], a[196:199], a[220:223], 0\n\t" \
  "buffer_load_dwordx4 v19, %1, s43 offen offset:2048 lds\n\t" \
  "v_mfma_f32_16x16x32_bf16 v[184:187], a[196:199], a[224:227], 0\n\t" \
  "s_add_u32 m0, s41, 0\n\t" \
  "s_add_u32 s44, s42, 256\n\t" \
  "v_mfma_f32_16x16x32_bf16 v[188:191], a[196:199], a[228:231], 0\n\t" \
  "s_waitcnt vmcnt(18) lgkmcnt(4)\n\t" \
  "s_barrier\n\t" \
  "v_mfma_f32_16x16x32_bf16 v[192:195], a[200:203], a[216:219], 0\n\t" \
  "ds_read_b64_tr_b16 a[192:193], v24 offset:12288\n\t" \
  "ds_read_b64_tr_b16 a[194:195], v24 offset:12544\n\t" \
  "v_mfma_f32_16x16x32_bf16 v[196:199], a[200:203], a[220:223], 0\n\t" \
  "buffer_load_dwordx4 v20, %0, s44 offen offset:0 lds\n\t" \
  "v_mfma_f32_16x16x32_bf16 v[200:203], a[200:203], a[224:227], 0\n\t" \
  "s_add_u32 s61, s60, 27648\n\t" \
  "v_add_co_u32 v36, vcc, 1, v36\n\t" \
  "s_nop 1\n\t" \
  "v_addc_co_u32 v37, vcc, 0, v37, vcc\n\t" \
  "v_mfma_f32_16x16x32_bf16 v[204:207], a[200:203], a[228:231], 0\n\t" \
  "buffer_load_dwordx4 v21, %0, s44 offen offset:1024 lds\n\t" \
  "v_mfma_f32_16x16x32_bf16 v[208:211], a[204:207], a[216:219], 0\n\t" \
  "ds_read_b64_tr_b16 a[196:197], v25 offset:12288\n\t" \
  "ds_read_b64_tr_b16 a[198:199], v25 offset:12544\n\t" \
  "v_mfma_f32_16x16x32_bf16 v[212:215], a[204:207], a[220:223], 0\n\t" \
  "buffer_load_dwordx4 a[140:143], v56, %1, s61 offen\n\t" \
  "v_mfma_f32_16x16x32_bf16 v[216:219], a[204:207], a[224:227], 0\n\t" \
  "buffer_load_dwordx4 v22, %0, s44 offen offset:2048 lds\n\t" \
  "v_mfma_f32_16x16x32_bf16 v[220:223], a[204:207], a[228:231], 0\n\t" \
  "s_add_u32 s61, s60, 36864\n\t" \
  "s_waitcnt lgkmcnt(4)\n\t" \
  "v_mfma_f32_16x16x32_bf16 v[224:227], a[208:211], a[216:219], 0\n\t" \
  "ds_read_b64_tr_b16 a[200:201], v24 offset:18432\n\t" \
  "ds_read_b64_tr_b16 a[202:203], v24 offset:18688\n\t" \
  "v_mfma_f32_16x16x32_bf16 v[228:231], a[208:211], a[220:223], 0\n\t" \
  "buffer_load_dwordx4 v23, %0, s44 offen offset:3072 lds\n\t" \
  "v_mfma_f32_16x16x32_bf16 v[232:235], a[208:211], a[224:227], 0\n\t" \
  "s_add_u32 m0, s40, 49152\n\t" \
  "s_add_u32 s43, s50, 1179648\n\t" \
  "v_mfma_f32_16x16x32_bf16 v[236:239], a[208:211], a[228:231], 0\n\t" \
  "buffer_load_dwordx4 v17, %1, s43 offen offset:0 lds\n\t" \
  "v_mfma_f32_16x16x32_bf16 v[240:243], a[212:215], a[216:219], 0\n\t" \
  "ds_read_b64_tr_b16 a[204:205], v25 offset:18432\n\t" \
  "ds_read_b64_tr_b16 a[206:207], v25 offset:18688\n\t" \
  "v_mfma_f32_16x16x32_bf16 v[244:247], a[212:215], a[220:223], 0\n\t" \
  "buffer_load_dwordx4 a[144:147], v56, %1, s61 offen\n\t" \
  "v_mfma_f32_16x16x32_bf16 v[248:251], a[212:215], a[224:227], 0\n\t" \
  "buffer_load_dwordx4 v18, %1, s43 offen offset:1024 lds\n\t" \
  "v_mfma_f32_16x16x32_bf16 v[252:255], a[212:215], a[228:231], 0\n\t" \
  "s_add_u32 s61, s60, 46080\n\t" \
  "s_waitcnt lgkmcnt(4)\n\t" \
  "v_mfma_f32_16x16x32_bf16 v[64:67], a[192:195], a[232:235], v[64:67]\n\t" \
  "ds_read_b64_tr_b16 a[208:209], v24 offset:13312\n\t" \
  "ds_read_b64_tr_b16 a[210:211], v24 offset:13568\n\t" \
  "s_lshl_b32 s62, %4, 2\n\t" \
  "v_mfma_f32_16x16x32_bf16 v[68:71], a[192:195], a[236:239], v[68:71]\n\t" \
  "buffer_load_dwordx4 v19, %1, s43 offen offset:2048 lds\n\t" \
  "s_add_u32 s62, s62, %7\n\t" \
  "v_mfma_f32_16x16x32_bf16 v[72:75], a[192:195], a[240:243], v[72:75]\n\t" \
  "v_mov_b32 v33, 0x7ffffff0\n\t" \
  "v_cmp_eq_u32 vcc, 0, v16\n\t" \
  "s_nop 1\n\t" \
  "v_cndmask_b32 v34, v33, 0, vcc\n\t" \
  "v_mfma_f32_16x16x32_bf16 v[76:79], a[192:195], a[244:247], v[76:79]\n\t" \
  "buffer_load_dwordx4 a[148:151], v56, %1, s61 offen\n\t" \
  "s_lshl_b32 s62, s62, 9\n\t" \
  "v_mfma_f32_16x16x32_bf16 v[80:83], a[196:199], a[232:235], v[80:83]\n\t" \
  "ds_read_b64_tr_b16 a[212:213], v25 offset:13312\n\t" \
  "ds_read_b64_tr_b16 a[214:215], v25 offset:13568\n\t" \
  "s_add_u32 s62, s62, 327680\n\t" \
  "v_mfma_f32_16x16x32_bf16 v[84:87], a[196:199], a[236:239], v[84:87]\n\t" \
  "s_add_u32 m0, s41, 4096\n\t" \
  "s_add_u32 s44, s42, 37120\n\t" \
  "s_mul_i32 s63, s49, 768\n\t" \
  "v_mfma_f32_16x16x32_bf16 v[88:91], a[196:199], a[240:243], v[88:91]\n\t" \
  "buffer_load_dwordx4 v20, %0, s44 offen offset:0 lds\n\t" \
  "s_add_u32 s63, s63, 655360\n\t" \
  "v_mfma_f32_16x16x32_bf16 v[92:95], a[196:199], a[244:247], v[92:95]\n\t" \
  "s_add_u32 s61, s60, 55296\n\t" \
  "s_lshl_b32 s54, %7, 10\n\t" \
  "s_waitcnt lgkmcnt(4)\n\t" \
  "v_mfma_f32_16x16x32_bf16 v[96:99], a[200:203], a[232:235], v[96:99]\n\t" \
  "ds_read_b64_tr_b16 a[192:193], v24 offset:19456\n\t" \
  "ds_read_b64_tr_b16 a[194:195], v24 offset:19712\n\t" \
  "s_add_u32 s54, s54, 159744\n\t" \
  "v_mfma_f32_16x16x32_bf16 v[100:103], a[200:203], a[236:239], v[100:103]\n\t" \
  "buffer_load_dwordx4 v21, %0, s44 offen offset:1024 lds\n\t" \
  "v_add_u32 v35, s54, v35\n\t" \
  "v_mfma_f32_16x16x32_bf16 v[104:107], a[200:203], a[240:243], v[104:107]\n\t" \
  "buffer_load_dwordx4 a[152:155], v56, %1, s61 offen\n\t" \
  "v_add_u32 v62, s54, v32\n\t" \
  "v_mfma_f32_16x16x32_bf16 v[108:111], a[200:203], a[244:247], v[108:111]\n\t" \
  "buffer_load_dwordx4 v22, %0, s44 offen offset:2048 lds\n\t" \
  "s_lshl_b32 s54, s49, 3\n\t" \
  "v_mfma_f32_16x16x32_bf16 v[112:115], a[204:207], a[232:235], v[112:115]\n\t" \
  "ds_read_b64_tr_b16 a[196:197], v25 offset:19456\n\t" \
  "ds_read_b64_tr_b16 a[198:199], v25 offset:19712\n\t" \
  "s_add_u32 s54, s54, 6656\n\t" \
  "v_mfma_f32_16x16x32_bf16 v[116:119], a[204:207], a[236:239], v[116:119]\n\t" \
  "buffer_load_dwordx4 v23, %0, s44 offen offset:3072 lds\n\t" \
  "v_and_b32 v59, 7, v16\n\t" \
  "v_mfma_f32_16x16x32_bf16 v[120:123], a[204:207], a[240:243], v[120:123]\n\t" \
  "s_add_u32 s61, s60, 64512\n\t" \
  "v_add_u32 v30, 32768, v30\n\t" \
  "s_waitcnt vmcnt(29)\n\t" \
  "v_mfma_f32_16x16x32_bf16 v[124:127], a[204:207], a[244:247], v[124:127]\n\t" \
  "ds_read_b128 a[216:219], v30 offset:0\n\t" \
  "v_add_u32 v59, s54, v59\n\t" \
  "s_waitcnt lgkmcnt(5)\n\t" \
  "v_mfma_f32_16x16x32_bf16 v[128:131], a[208:211], a[232:235], v[128:131]\n\t" \
  "ds_read_b64_tr_b16 a[200:201], v24 offset:14336\n\t" \
  "ds_read_b64_tr_b16 a[202:203], v24 offset:14592\n\t" \
  "s_lshl_b32 s54, %4, 2\n\t" \
  "v_mfma_f32_16x16x32_bf16 v[132:135], a[208:211], a[236:239], v[132:135]\n\t" \
  "buffer_load_dwordx4 a[156:159], v56, %1, s61 offen\n\t" \
  "s_add_u32 s54, s54, %7\n\t" \
  "v_mfma_f32_16x16x32_bf16 v[136:139], a[208:211], a[240:243], v[136:139]\n\t" \
  "s_add_u32 s61, s60, 73728\n\t" \
  "ds_read_b128 a[220:223], v30 offset:2048\n\t" \
  "s_waitcnt vmcnt(25)\n\t" \
  "v_mfma_f32_16x16x32_bf16 v[140:143], a[208:211], a[244:247], v[140:143]\n\t" \
  "buffer_load_dwordx4 a[160:163], v56, %1, s61 offen\n\t" \
  "s_lshl_b32 s54, s54, 1\n\t" \
  "v_mfma_f32_16x16x32_bf16 v[144:147], a[212:215], a[232:235], v[144:147]\n\t" \
  "ds_read_b64_tr_b16 a[204:205], v25 offset:14336\n\t" \
  "ds_read_b64_tr_b16 a[206:207], v25 offset:14592\n\t" \
  "s_add_u32 s54, s54, 256\n\t" \
  "v_mfma_f32_16x16x32_bf16 v[148:151], a[212:215], a[236:239], v[148:151]\n\t" \
  "ds_read_b128 a[224:227], v30 offset:4096\n\t" \
  "v_and_b32 v58, 1, v16\n\t" \
  "v_add_u32 v58, s54, v58\n\t" \
  "v_mfma_f32_16x16x32_bf16 v[152:155], a[212:215], a[240:243], v[152:155]\n\t" \
  "s_waitcnt vmcnt(24)\n\t" \
  "v_accvgpr_read_b32 v52, a128\n\t" \
  "v_lshlrev_b32 v58, 7, v58\n\t" \
  "v_mfma_f32_16x16x32_bf16 v[156:159], a[212:215], a[244:247], v[156:159]\n\t" \
  "v_lshlrev_b32 v44, 16, v52\n\t" \
  "v_lshlrev_b32 v59, 7, v59\n\t" \
  "s_waitcnt lgkmcnt(7)\n\t" \
  "v_mfma_f32_16x16x32_bf16 v[160:163], a[192:195], a[232:235], v[160:163]\n\t" \
  "ds_read_b64_tr_b16 a[208:209], v24 offset:20480\n\t" \
  "ds_read_b64_tr_b16 a[210:211], v24 offset:20736\n\t" \
  "v_mfma_f32_16x16x32_bf16 v[164:167], a[192:195], a[236:239], v[164:167]\n\t" \
  "v_and_b32 v45, 0xffff0000, v52\n\t" \
  "v_accvgpr_read_b32 v53, a129\n\t" \
  "v_mfma_f32_16x16x32_bf16 v[168:171], a[192:195], a[240:243], v[168:171]\n\t" \
  "v_lshlrev_b32 v46, 16, v53\n\t" \
  "v_and_b32 v47, 0xffff0000, v53\n\t" \
  "v_mfma_f32_16x16x32_bf16 v[172:175], a[192:195], a[244:247], v[172:175]\n\t" \
  "v_accvgpr_read_b32 v52, a130\n\t" \
  "v_lshlrev_b32 v48, 16, v52\n\t" \
  "v_mfma_f32_16x16x32_bf16 v[176:179], a[196:199], a[232:235], v[176:179]\n\t" \
  "ds_read_b64_tr_b16 a[212:213], v25 offset:20480\n\t" \
  "ds_read_b64_tr_b16 a[214:215], v25 offset:20736\n\t" \
  "v_mfma_f32_16x16x32_bf16 v[180:183], a[196:199], a[236:239], v[180:183]\n\t" \
  "v_and_b32 v49, 0xffff0000, v52\n\t" \
  "v_accvgpr_read_b32 v53, a131\n\t" \
  "v_mfma_f32_16x16x32_bf16 v[184:187], a[196:199], a[240:243], v[184:187]\n\t" \
  "v_lshlrev_b32 v50, 16, v53\n\t" \
  "v_and_b32 v51, 0xffff0000, v53\n\t" \
  "v_mfma_f32_16x16x32_bf16 v[188:191], a[196:199], a[244:247], v[188:191]\n\t" \
  "s_waitcnt vmcnt(22) lgkmcnt(5)\n\t" \
  "s_barrier\n\t" \
  "v_mfma_f32_16x16x32_bf16 v[192:195], a[200:203], a[232:235], v[192:195]\n\t" \
  "ds_read_b64_tr_b16 a[192:193], v24 offset:24576\n\t" \
  "ds_read_b64_tr_b16 a[194:195], v24 offset:24832\n\t" \
  "v_mfma_f32_16x16x32_bf16 v[196:199], a[200:203], a[236:239], v[196:199]\n\t" \
  "s_add_u32 m0, s40, 0\n\t" \
  "s_add_u32 s43, s50, 1474560\n\t" \
  "v_mfma_f32_16x16x32_bf16 v[200:203], a[200:203], a[240:243], v[200:203]\n\t" \
  "buffer_load_dwordx4 v17, %1, s43 offen offset:0 lds\n\t" \
  "v_mfma_f32_16x16x32_bf16 v[204:207], a[200:203], a[244:247], v[204:207]\n\t" \
  "ds_read_b128 a[228:231], v30 offset:6144\n\t" \
  "v_mfma_f32_16x16x32_bf16 v[208:211], a[204:207], a[232:235], v[208:211]\n\t" \
  "ds_read_b64_tr_b16 a[196:197], v25 offset:24576\n\t" \
  "ds_read_b64_tr_b16 a[198:199], v25 offset:24832\n\t" \
  "v_mfma_f32_16x16x32_bf16 v[212:215], a[204:207], a[236:239], v[212:215]\n\t" \
  "buffer_load_dwordx4 v18, %1, s43 offen offset:1024 lds\n\t" \
  "v_mfma_f32_16x16x32_bf16 v[216:219], a[204:207], a[240:243], v[216:219]\n\t" \
  "s_waitcnt vmcnt(22)\n\t" \
  "v_accvgpr_read_b32 v52, a132\n\t" \
  "v_lshlrev_b32 v38, 16, v52\n\t" \
  "v_mfma_f32_16x16x32_bf16 v[220:223], a[204:207], a[244:247], v[220:223]\n\t" \
  "v_and_b32 v39, 0xffff0000, v52\n\t" \
  "v_add_f32 v44, v44, v38\n\t" \
  "s_waitcnt lgkmcnt(5)\n\t" \
  "v_mfma_f32_16x16x32_bf16 v[224:227], a[208:211], a[232:235], v[224:227]\n\t" \
  "ds_read_b64_tr_b16 a[200:201], v24 offset:30720\n\t" \
  "ds_read_b64_tr_b16 a[202:203], v24 offset:30976\n\t" \
  "v_add_f32 v45, v45, v39\n\t" \
  "v_mfma_f32_16x16x32_bf16 v[228:231], a[208:211], a[236:239], v[228:231]\n\t" \
  "buffer_load_dwordx4 v19, %1, s43 offen offset:2048 lds\n\t" \
  "v_accvgpr_read_b32 v53, a133\n\t" \
  "v_mfma_f32_16x16x32_bf16 v[232:235], a[208:211], a[240:243], v[232:235]\n\t" \
  "s_add_u32 m0, s41, 32768\n\t" \
  "s_add_u32 s44, s42, 384\n\t" \
  "v_lshlrev_b32 v38, 16, v53\n\t" \
  "v_mfma_f32_16x16x32_bf16 v[236:239], a[208:211], a[244:247], v[236:239]\n\t" \
  "buffer_load_dwordx4 v20, %0, s44 offen offset:0 lds\n\t" \
  "v_and_b32 v39, 0xffff0000, v53\n\t" \
  "v_mfma_f32_16x16x32_bf16 v[240:243], a[212:215], a[232:235], v[240:243]\n\t" \
  "ds_read_b64_tr_b16 a[204:205], v25 offset:30720\n\t" \
  "ds_read_b64_tr_b16 a[206:207], v25 offset:30976\n\t" \
  "v_add_f32 v46, v46, v38\n\t" \
  "v_mfma_f32_16x16x32_bf16 v[244:247], a[212:215], a[236:239], v[244:247]\n\t" \
  "buffer_load_dwordx4 v21, %0, s44 offen offset:1024 lds\n\t" \
  "v_add_f32 v47, v47, v39\n\t" \
  "v_mfma_f32_16x16x32_bf16 v[248:251], a[212:215], a[240:243], v[248:251]\n\t" \
  "v_accvgpr_read_b32 v52, a134\n\t" \
  "v_lshlrev_b32 v38, 16, v52\n\t" \
  "v_and_b32 v39, 0xffff0000, v52\n\t" \
  "v_mfma_f32_16x16x32_bf16 v[252:255], a[212:215], a[244:247], v[252:255]\n\t" \
  "v_add_f32 v48, v48, v38\n\t" \
  "s_waitcnt lgkmcnt(4)\n\t" \
  "v_mfma_f32_16x16x32_bf16 v[64:67], a[192:195], a[216:219], v[64:67]\n\t" \
  "ds_read_b64_tr_b16 a[208:209], v24 offset:25600\n\t" \
  "ds_read_b64_tr_b16 a[210:211], v24 offset:25856\n\t" \
  "v_mfma_f32_16x16x32_bf16 v[68:71], a[192:195], a[220:223], v[68:71]\n\t" \
  "buffer_load_dwordx4 v22, %0, s44 offen offset:2048 lds\n\t" \
  "v_mfma_f32_16x16x32_bf16 v[72:75], a[192:195], a[224:227], v[72:75]\n\t" \
  "v_add_f32 v49, v49, v39\n\t" \
  "v_accvgpr_read_b32 v53, a135\n\t" \
  "v_mfma_f32_16x16x32_bf16 v[76:79], a[192:195], a[228:231], v[76:79]\n\t" \
  "buffer_load_dwordx4 v23, %0, s44 offen offset:3072 lds\n\t" \
  "v_mfma_f32_16x16x32_bf16 v[80:83], a[196:199], a[216:219], v[80:83]\n\t" \
  "ds_read_b64_tr_b16 a[212:213], v25 offset:25600\n\t" \
  "ds_read_b64_tr_b16 a[214:215], v25 offset:25856\n\t" \
  "v_mfma_f32_16x16x32_bf16 v[84:87], a[196:199], a[220:223], v[84:87]\n\t" \
  "v_lshlrev_b32 v38, 16, v53\n\t" \
  "v_and_b32 v39, 0xffff0000, v53\n\t" \
  "s_add_u32 m0, s41, 36864\n\t" \
  "v_mfma_f32_16x16x32_bf16 v[88:91], a[196:199], a[224:227], v[88:91]\n\t" \
  "v_add_f32 v50, v50, v38\n\t" \
  "v_add_f32 v51, v51, v39\n\t" \
  "s_waitcnt vmcnt(25)\n\t" \
  "v_mfma_f32_16x16x32_bf16 v[92:95], a[196:199], a[228:231], v[92:95]\n\t" \
  "s_add_u32 s44, s42, 37248\n\t" \
  "v_accvgpr_read_b32 v52, a136\n\t" \
  "s_waitcnt lgkmcnt(4)\n\t" \
  "v_mfma_f32_16x16x32_bf16 v[96:99], a[200:203], a[216:219], v[96:99]\n\t" \
  "ds_read_b64_tr_b16 a[192:193], v24 offset:31744\n\t" \
  "ds_read_b64_tr_b16 a[194:195], v24 offset:32000\n\t" \
  "buffer_load_dwordx4 v20, %0, s44 offen offset:0 lds\n\t" \
  "v_mfma_f32_16x16x32_bf16 v[100:103], a[200:203], a[220:223], v[100:103]\n\t" \
  "v_lshlrev_b32 v38, 16, v52\n\t" \
  "v_and_b32 v39, 0xffff0000, v52\n\t" \
  "v_add_f32 v44, v44, v38\n\t" \
  "v_mfma_f32_16x16x32_bf16 v[104:107], a[200:203], a[224:227], v[104:107]\n\t" \
  "buffer_load_dwordx4 v21, %0, s44 offen offset:1024 lds\n\t" \
  "v_add_f32 v45, v45, v39\n\t" \
  "v_mfma_f32_16x16x32_bf16 v[108:111], a[200:203], a[228:231], v[108:111]\n\t" \
  "v_accvgpr_read_b32 v53, a137\n\t" \
  "v_lshlrev_b32 v38, 16, v53\n\t" \
  "v_and_b32 v39, 0xffff0000, v53\n\t" \
  "v_mfma_f32_16x16x32_bf16 v[112:115], a[204:207], a[216:219], v[112:115]\n\t" \
  "ds_read_b64_tr_b16 a[196:197], v25 offset:31744\n\t" \
  "ds_read_b64_tr_b16 a[198:199], v25 offset:32000\n\t" \
  "v_add_f32 v46, v46, v38\n\t" \
  "v_mfma_f32_16x16x32_bf16 v[116:119], a[204:207], a[220:223], v[116:119]\n\t" \
  "buffer_load_dwordx4 v22, %0, s44 offen offset:2048 lds\n\t" \
  "v_add_u32 v31, 32768, v31\n\t" \
  "v_mfma_f32_16x16x32_bf16 v[120:123], a[204:207], a[224:227], v[120:123]\n\t" \
  "v_add_f32 v47, v47, v39\n\t" \
  "v_accvgpr_read_b32 v52, a138\n\t" \
  "ds_read_b128 a[232:235], v31 offset:0\n\t" \
  "v_mfma_f32_16x16x32_bf16 v[124:127], a[204:207], a[228:231], v[124:127]\n\t" \
  "v_lshlrev_b32 v38, 16, v52\n\t" \
  "ds_read_b128 a[236:239], v31 offset:2048\n\t" \
  "s_waitcnt lgkmcnt(6)\n\t" \
  "v_mfma_f32_16x16x32_bf16 v[128:131], a[208:211], a[216:219], v[128:131]\n\t" \
  "ds_read_b64_tr_b16 a[200:201], v24 offset:26624\n\t" \
  "ds_read_b64_tr_b16 a[202:203], v24 offset:26880\n\t" \
  "ds_read_b128 a[240:243], v31 offset:4096\n\t" \
  "v_mfma_f32_16x16x32_bf16 v[132:135], a[208:211], a[220:223], v[132:135]\n\t" \
  "buffer_load_dwordx4 v23, %0, s44 offen offset:3072 lds\n\t" \
  "ds_read_b128 a[244:247], v31 offset:6144\n\t" \
  "v_mfma_f32_16x16x32_bf16 v[136:139], a[208:211], a[224:227], v[136:139]\n\t" \
  "v_and_b32 v39, 0xffff0000, v52\n\t" \
  "v_add_f32 v48, v48, v38\n\t" \
  "v_mfma_f32_16x16x32_bf16 v[140:143], a[208:211], a[228:231], v[140:143]\n\t" \
  "v_add_f32 v49, v49, v39\n\t" \
  "v_accvgpr_read_b32 v53, a139\n\t" \
  "v_mfma_f32_16x16x32_bf16 v[144:147], a[212:215], a[216:219], v[144:147]\n\t" \
  "ds_read_b64_tr_b16 a[204:205], v25 offset:26624\n\t" \
  "ds_read_b64_tr_b16 a[206:207], v25 offset:26880\n\t" \
  "v_mfma_f32_16x16x32_bf16 v[148:151], a[212:215], a[220:223], v[148:151]\n\t" \
  "v_lshlrev_b32 v38, 16, v53\n\t" \
  "v_and_b32 v39, 0xffff0000, v53\n\t" \
  "v_mfma_f32_16x16x32_bf16 v[152:155], a[212:215], a[224:227], v[152:155]\n\t" \
  "v_add_f32 v50, v50, v38\n\t" \
  "v_add_f32 v51, v51, v39\n\t" \
  "s_waitcnt vmcnt(25)\n\t" \
  "v_mfma_f32_16x16x32_bf16 v[156:159], a[212:215], a[228:231], v[156:159]\n\t" \
  "v_accvgpr_read_b32 v52, a140\n\t" \
  "v_lshlrev_b32 v38, 16, v52\n\t" \
  "s_waitcnt lgkmcnt(8)\n\t" \
  "v_mfma_f32_16x16x32_bf16 v[160:163], a[192:195], a[216:219], v[160:163]\n\t" \
  "ds_read_b64_tr_b16 a[208:209], v24 offset:32768\n\t" \
  "ds_read_b64_tr_b16 a[210:211], v24 offset:33024\n\t" \
  "v_and_b32 v39, 0xffff0000, v52\n\t" \
  "v_mfma_f32_16x16x32_bf16 v[164:167], a[192:195], a[220:223], v[164:167]\n\t" \
  "v_add_f32 v44, v44, v38\n\t" \
  "v_add_f32 v45, v45, v39\n\t" \
  "v_accvgpr_read_b32 v53, a141\n\t" \
  "v_mfma_f32_16x16x32_bf16 v[168:171], a[192:195], a[224:227], v[168:171]\n\t" \
  "v_lshlrev_b32 v38, 16, v53\n\t" \
  "v_and_b32 v39, 0xffff0000, v53\n\t" \
  "v_add_f32 v46, v46, v38\n\t" \
  "v_mfma_f32_16x16x32_bf16 v[172:175], a[192:195], a[228:231], v[172:175]\n\t" \
  "v_add_f32 v47, v47, v39\n\t" \
  "v_accvgpr_read_b32 v52, a142\n\t" \
  "v_lshlrev_b32 v38, 16, v52\n\t" \
  "v_mfma_f32_16x16x32_bf16 v[176:179], a[196:199], a[216:219], v[176:179]\n\t" \
  "ds_read_b64_tr_b16 a[212:213], v25 offset:32768\n\t" \
  "ds_read_b64_tr_b16 a[214:215], v25 offset:33024\n\t" \
  "v_mfma_f32_16x16x32_bf16 v[180:183], a[196:199], a[220:223], v[180:183]\n\t" \
  "v_and_b32 v39, 0xffff0000, v52\n\t" \
  "v_add_f32 v48, v48, v38\n\t" \
  "v_mfma_f32_16x16x32_bf16 v[184:187], a[196:199], a[224:227], v[184:187]\n\t" \
  "v_add_f32 v49, v49, v39\n\t" \
  "v_accvgpr_read_b32 v53, a143\n\t" \
  "v_mfma_f32_16x16x32_bf16 v[188:191], a[196:199], a[228:231], v[188:191]\n\t" \
  "s_waitcnt lgkmcnt(4)\n\t" \
  "s_barrier\n\t" \
  "v_mfma_f32_16x16x32_bf16 v[192:195], a[200:203], a[216:219], v[192:195]\n\t" \
  "ds_read_b64_tr_b16 a[192:193], v24 offset:36864\n\t" \
  "ds_read_b64_tr_b16 a[194:195], v24 offset:37120\n\t" \
  "v_mfma_f32_16x16x32_bf16 v[196:199], a[200:203], a[220:223], v[196:199]\n\t" \
  "s_add_u32 m0, s40, 12288\n\t" \
  "s_add_u32 s43, s50, 1769472\n\t" \
  "v_mfma_f32_16x16x32_bf16 v[200:203], a[200:203], a[224:227], v[200:203]\n\t" \
  "buffer_load_dwordx4 v17, %1, s43 offen offset:0 lds\n\t" \
  "v_mfma_f32_16x16x32_bf16 v[204:207], a[200:203], a[228:231], v[204:207]\n\t" \
  "v_lshlrev_b32 v38, 16, v53\n\t" \
  "v_and_b32 v39, 0xffff0000, v53\n\t" \
  "v_mfma_f32_16x16x32_bf16 v[208:211], a[204:207], a[216:219], v[208:211]\n\t" \
  "ds_read_b64_tr_b16 a[196:197], v25 offset:36864\n\t" \
  "ds_read_b64_tr_b16 a[198:199], v25 offset:37120\n\t" \
  "v_mfma_f32_16x16x32_bf16 v[212:215], a[204:207], a[220:223], v[212:215]\n\t" \
  "v_add_f32 v50, v50, v38\n\t" \
  "v_add_f32 v51, v51, v39\n\t" \
  "v_mfma_f32_16x16x32_bf16 v[216:219], a[204:207], a[224:227], v[216:219]\n\t" \
  "buffer_load_dwordx4 v18, %1, s43 offen offset:1024 lds\n\t" \
  "s_waitcnt vmcnt(23)\n\t" \
  "v_mfma_f32_16x16x32_bf16 v[220:223], a[204:207], a[228:231], v[220:223]\n\t" \
  "v_accvgpr_read_b32 v52, a144\n\t" \
  "v_lshlrev_b32 v38, 16, v52\n\t" \
  "s_waitcnt lgkmcnt(4)\n\t" \
  "v_mfma_f32_16x16x32_bf16 v[224:227], a[208:211], a[216:219], v[224:227]\n\t" \
  "ds_read_b64_tr_b16 a[200:201], v24 offset:43008\n\t" \
  "ds_read_b64_tr_b16 a[202:203], v24 offset:43264\n\t" \
  "v_and_b32 v39, 0xffff0000, v52\n\t" \
  "v_mfma_f32_16x16x32_bf16 v[228:231], a[208:211], a[220:223], v[228:231]\n\t" \
  "buffer_load_dwordx4 v19, %1, s43 offen offset:2048 lds\n\t" \
  "v_add_f32 v44, v44, v38\n\t" \
  "v_mfma_f32_16x16x32_bf16 v[232:235], a[208:211], a[224:227], v[232:235]\n\t" \
  "v_add_f32 v45, v45, v39\n\t" \
  "v_accvgpr_read_b32 v53, a145\n\t" \
  "v_lshlrev_b32 v38, 16, v53\n\t" \
  "v_mfma_f32_16x16x32_bf16 v[236:239], a[208:211], a[228:231], v[236:239]\n\t" \
  "v_and_b32 v39, 0xffff0000, v53\n\t" \
  "v_add_f32 v46, v46, v38\n\t" \
  "v_mfma_f32_16x16x32_bf16 v[240:243], a[212:215], a[216:219], v[240:243]\n\t" \
  "ds_read_b64_tr_b16 a[204:205], v25 offset:43008\n\t" \
  "ds_read_b64_tr_b16 a[206:207], v25 offset:43264\n\t" \
  "v_mfma_f32_16x16x32_bf16 v[244:247], a[212:215], a[220:223], v[244:247]\n\t" \
  "v_add_f32 v47, v47, v39\n\t" \
  "v_accvgpr_read_b32 v52, a146\n\t" \
  "v_mfma_f32_16x16x32_bf16 v[248:251], a[212:215], a[224:227], v[248:251]\n\t" \
  "v_lshlrev_b32 v38, 16, v52\n\t" \
  "v_and_b32 v39, 0xffff0000, v52\n\t" \
  "v_mfma_f32_16x16x32_bf16 v[252:255], a[212:215], a[228:231], v[252:255]\n\t" \
  "v_add_f32 v48, v48, v38\n\t" \
  "s_waitcnt lgkmcnt(4)\n\t" \
  "v_mfma_f32_16x16x32_bf16 v[64:67], a[192:195], a[232:235], v[64:67]\n\t" \
  "ds_read_b64_tr_b16 a[208:209], v24 offset:37888\n\t" \
  "ds_read_b64_tr_b16 a[210:211], v24 offset:38144\n\t" \
  "v_mfma_f32_16x16x32_bf16 v[68:71], a[192:195], a[236:239], v[68:71]\n\t" \
  "s_add_u32 m0, s41, 65536\n\t" \
  "s_add_u32 s44, s42, 512\n\t" \
  "v_mfma_f32_16x16x32_bf16 v[72:75], a[192:195], a[240:243], v[72:75]\n\t" \
  "buffer_load_dwordx4 v20, %0, s44 offen offset:0 lds\n\t" \
  "v_mfma_f32_16x16x32_bf16 v[76:79], a[192:195], a[244:247], v[76:79]\n\t" \
  "v_add_f32 v49, v49, v39\n\t" \
  "v_accvgpr_read_b32 v53, a147\n\t" \
  "v_mfma_f32_16x16x32_bf16 v[80:83], a[196:199], a[232:235], v[80:83]\n\t" \
  "ds_read_b64_tr_b16 a[212:213], v25 offset:37888\n\t" \
  "ds_read_b64_tr_b16 a[214:215], v25 offset:38144\n\t" \
  "v_mfma_f32_16x16x32_bf16 v[84:87], a[196:199], a[236:239], v[84:87]\n\t" \
  "v_lshlrev_b32 v38, 16, v53\n\t" \
  "v_and_b32 v39, 0xffff0000, v53\n\t" \
  "v_mfma_f32_16x16x32_bf16 v[88:91], a[196:199], a[240:243], v[88:91]\n\t" \
  "v_add_f32 v50, v50, v38\n\t" \
  "v_add_f32 v51, v51, v39\n\t" \
  "s_waitcnt vmcnt(22)\n\t" \
  "v_mfma_f32_16x16x32_bf16 v[92:95], a[196:199], a[244:247], v[92:95]\n\t" \
  "v_accvgpr_read_b32 v52, a148\n\t" \
  "v_lshlrev_b32 v38, 16, v52\n\t" \
  "s_waitcnt lgkmcnt(4)\n\t" \
  "v_mfma_f32_16x16x32_bf16 v[96:99], a[200:203], a[232:235], v[96:99]\n\t" \
  "ds_read_b64_tr_b16 a[192:193], v24 offset:44032\n\t" \
  "ds_read_b64_tr_b16 a[194:195], v24 offset:44288\n\t" \
  "v_and_b32 v39, 0xffff0000, v52\n\t" \
  "v_mfma_f32_16x16x32_bf16 v[100:103], a[200:203], a[236:239], v[100:103]\n\t" \
  "buffer_load_dwordx4 v21, %0, s44 offen offset:1024 lds\n\t" \
  "v_add_f32 v44, v44, v38\n\t" \
  "v_mfma_f32_16x16x32_bf16 v[104:107], a[200:203], a[240:243], v[104:107]\n\t" \
  "v_add_f32 v45, v45, v39\n\t" \
  "v_accvgpr_read_b32 v53, a149\n\t" \
  "v_lshlrev_b32 v38, 16, v53\n\t" \
  "v_mfma_f32_16x16x32_bf16 v[108:111], a[200:203], a[244:247], v[108:111]\n\t" \
  "buffer_load_dwordx4 v22, %0, s44 offen offset:2048 lds\n\t" \
  "v_and_b32 v39, 0xffff0000, v53\n\t" \
  "v_mfma_f32_16x16x32_bf16 v[112:115], a[204:207], a[232:235], v[112:115]\n\t" \
  "ds_read_b64_tr_b16 a[196:197], v25 offset:44032\n\t" \
  "ds_read_b64_tr_b16 a[198:199], v25 offset:44288\n\t" \
  "v_add_f32 v46, v46, v38\n\t" \
  "v_mfma_f32_16x16x32_bf16 v[116:119], a[204:207], a[236:239], v[116:119]\n\t" \
  "buffer_store_dwordx2 v[36:37], v34, %3, s73 offen offset:0 sc1\n\t" \
  "v_add_u32 v30, 4294901760, v30\n\t" \
  "v_mfma_f32_16x16x32_bf16 v[120:123], a[204:207], a[240:243], v[120:123]\n\t" \
  "v_add_f32 v47, v47, v39\n\t" \
  "v_accvgpr_read_b32 v52, a150\n\t" \
  "ds_read_b128 a[216:219], v30 offset:0\n\t" \
  "v_mfma_f32_16x16x32_bf16 v[124:127], a[204:207], a[244:247], v[124:127]\n\t" \
  "v_lshlrev_b32 v38, 16, v52\n\t" \
  "ds_read_b128 a[220:223], v30 offset:2048\n\t" \
  "s_waitcnt lgkmcnt(6)\n\t" \
  "v_mfma_f32_16x16x32_bf16 v[128:131], a[208:211], a[232:235], v[128:131]\n\t" \
  "ds_read_b64_tr_b16 a[200:201], v24 offset:38912\n\t" \
  "ds_read_b64_tr_b16 a[202:203], v24 offset:39168\n\t" \
  "s_waitcnt vmcnt(20)\n\t" \
  "v_mfma_f32_16x16x32_bf16 v[132:135], a[208:211], a[236:239], v[132:135]\n\t" \
  "buffer_load_dwordx4 v23, %0, s44 offen offset:3072 lds\n\t" \
  "ds_read_b128 a[224:227], v30 offset:4096\n\t" \
  "v_mfma_f32_16x16x32_bf16 v[136:139], a[208:211], a[240:243], v[136:139]\n\t" \
  "v_and_b32 v39, 0xffff0000, v52\n\t" \
  "v_add_f32 v48, v48, v38\n\t" \
  "ds_read_b128 a[228:231], v30 offset:6144\n\t" \
  "v_mfma_f32_16x16x32_bf16 v[140:143], a[208:211], a[244:247], v[140:143]\n\t" \
  "v_add_f32 v49, v49, v39\n\t" \
  "v_accvgpr_read_b32 v53, a151\n\t" \
  "v_mfma_f32_16x16x32_bf16 v[144:147], a[212:215], a[232:235], v[144:147]\n\t" \
  "ds_read_b64_tr_b16 a[204:205], v25 offset:38912\n\t" \
  "ds_read_b64_tr_b16 a[206:207], v25 offset:39168\n\t" \
  "v_mfma_f32_16x16x32_bf16 v[148:151], a[212:215], a[236:239], v[148:151]\n\t" \
  "v_lshlrev_b32 v38, 16, v53\n\t" \
  "v_and_b32 v39, 0xffff0000, v53\n\t" \
  "v_mfma_f32_16x16x32_bf16 v[152:155], a[212:215], a[240:243], v[152:155]\n\t" \
  "v_add_f32 v50, v50, v38\n\t" \
  "v_add_f32 v51, v51, v39\n\t" \
  "v_accvgpr_read_b32 v52, a152\n\t" \
  "v_mfma_f32_16x16x32_bf16 v[156:159], a[212:215], a[244:247], v[156:159]\n\t" \
  "v_lshlrev_b32 v38, 16, v52\n\t" \
  "v_and_b32 v39, 0xffff0000, v52\n\t" \
  "s_waitcnt lgkmcnt(8)\n\t" \
  "v_mfma_f32_16x16x32_bf16 v[160:163], a[192:195], a[232:235], v[160:163]\n\t" \
  "ds_read_b64_tr_b16 a[208:209], v24 offset:45056\n\t" \
  "ds_read_b64_tr_b16 a[210:211], v24 offset:45312\n\t" \
  "v_add_f32 v44, v44, v38\n\t" \
  "v_mfma_f32_16x16x32_bf16 v[164:167], a[192:195], a[236:239], v[164:167]\n\t" \
  "v_add_f32 v45, v45, v39\n\t" \
  "v_accvgpr_read_b32 v53, a153\n\t" \
  "v_mfma_f32_16x16x32_bf16 v[168:171], a[192:195], a[240:243], v[168:171]\n\t" \
  "v_lshlrev_b32 v38, 16, v53\n\t" \
  "v_and_b32 v39, 0xffff0000, v53\n\t" \
  "v_mfma_f32_16x16x32_bf16 v[172:175], a[192:195], a[244:247], v[172:175]\n\t" \
  "v_add_f32 v46, v46, v38\n\t" \
  "v_add_f32 v47, v47, v39\n\t" \
  "v_mfma_f32_16x16x32_bf16 v[176:179], a[196:199], a[232:235], v[176:179]\n\t" \
  "ds_read_b64_tr_b16 a[212:213], v25 offset:45056\n\t" \
  "ds_read_b64_tr_b16 a[214:215], v25 offset:45312\n\t" \
  "v_mfma_f32_16x16x32_bf16 v[180:183], a[196:199], a[236:239], v[180:183]\n\t" \
  "v_accvgpr_read_b32 v52, a154\n\t" \
  "v_lshlrev_b32 v38, 16, v52\n\t" \
  "v_mfma_f32_16x16x32_bf16 v[184:187], a[196:199], a[240:243], v[184:187]\n\t" \
  "v_and_b32 v39, 0xffff0000, v52\n\t" \
  "v_add_f32 v48, v48, v38\n\t" \
  "v_mfma_f32_16x16x32_bf16 v[188:191], a[196:199], a[244:247], v[188:191]\n\t" \
  "s_waitcnt lgkmcnt(4)\n\t" \
  "s_barrier\n\t" \
  "v_mfma_f32_16x16x32_bf16 v[192:195], a[200:203], a[232:235], v[192:195]\n\t" \
  "ds_read_b64_tr_b16 a[192:193], v24 offset:49152\n\t" \
  "ds_read_b64_tr_b16 a[194:195], v24 offset:49408\n\t" \
  "v_mfma_f32_16x16x32_bf16 v[196:199], a[200:203], a[236:239], v[196:199]\n\t" \
  "s_add_u32 m0, s40, 24576\n\t" \
  "s_add_u32 s43, s50, 2064384\n\t" \
  "v_mfma_f32_16x16x32_bf16 v[200:203], a[200:203], a[240:243], v[200:203]\n\t" \
  "buffer_load_dwordx4 v17, %1, s43 offen offset:0 lds\n\t" \
  "v_mfma_f32_16x16x32_bf16 v[204:207], a[200:203], a[244:247], v[204:207]\n\t" \
  "v_add_f32 v49, v49, v39\n\t" \
  "v_accvgpr_read_b32 v53, a155\n\t" \
  "v_mfma_f32_16x16x32_bf16 v[208:211], a[204:207], a[232:235], v[208:211]\n\t" \
  "ds_read_b64_tr_b16 a[196:197], v25 offset:49152\n\t" \
  "ds_read_b64_tr_b16 a[198:199], v25 offset:49408\n\t" \
  "v_mfma_f32_16x16x32_bf16 v[212:215], a[204:207], a[236:239], v[212:215]\n\t" \
  "v_lshlrev_b32 v38, 16, v53\n\t" \
  "v_and_b32 v39, 0xffff0000, v53\n\t" \
  "v_mfma_f32_16x16x32_bf16 v[216:219], a[204:207], a[240:243], v[216:219]\n\t" \
  "v_add_f32 v50, v50, v38\n\t" \
  "v_add_f32 v51, v51, v39\n\t" \
  "s_waitcnt vmcnt(21)\n\t" \
  "v_mfma_f32_16x16x32_bf16 v[220:223], a[204:207], a[244:247], v[220:223]\n\t" \
  "buffer_load_dwordx4 v18, %1, s43 offen offset:1024 lds\n\t" \
  "s_waitcnt lgkmcnt(4)\n\t" \
  "v_mfma_f32_16x16x32_bf16 v[224:227], a[208:211], a[232:235], v[224:227]\n\t" \
  "ds_read_b64_tr_b16 a[200:201], v24 offset:55296\n\t" \
  "ds_read_b64_tr_b16 a[202:203], v24 offset:55552\n\t" \
  "v_accvgpr_read_b32 v52, a156\n\t" \
  "v_mfma_f32_16x16x32_bf16 v[228:231], a[208:211], a[236:239], v[228:231]\n\t" \
  "v_lshlrev_b32 v38, 16, v52\n\t" \
  "v_and_b32 v39, 0xffff0000, v52\n\t" \
  "v_add_f32 v44, v44, v38\n\t" \
  "v_mfma_f32_16x16x32_bf16 v[232:235], a[208:211], a[240:243], v[232:235]\n\t" \
  "buffer_load_dwordx4 v19, %1, s43 offen offset:2048 lds\n\t" \
  "v_add_f32 v45, v45, v39\n\t" \
  "v_mfma_f32_16x16x32_bf16 v[236:239], a[208:211], a[244:247], v[236:239]\n\t" \
  "v_accvgpr_read_b32 v53, a157\n\t" \
  "v_lshlrev_b32 v38, 16, v53\n\t" \
  "v_and_b32 v39, 0xffff0000, v53\n\t" \
  "v_mfma_f32_16x16x32_bf16 v[240:243], a[212:215], a[232:235], v[240:243]\n\t" \
  "ds_read_b64_tr_b16 a[204:205], v25 offset:55296\n\t" \
  "ds_read_b64_tr_b16 a[206:207], v25 offset:55552\n\t" \
  "v_add_f32 v46, v46, v38\n\t" \
  "v_mfma_f32_16x16x32_bf16 v[244:247], a[212:215], a[236:239], v[244:247]\n\t" \
  "v_add_f32 v47, v47, v39\n\t" \
  "v_accvgpr_read_b32 v52, a158\n\t" \
  "v_mfma_f32_16x16x32_bf16 v[248:251], a[212:215], a[240:243], v[248:251]\n\t" \
  "v_lshlrev_b32 v38, 16, v52\n\t" \
  "v_and_b32 v39, 0xffff0000, v52\n\t" \
  "v_mfma_f32_16x16x32_bf16 v[252:255], a[212:215], a[244:247], v[252:255]\n\t" \
  "s_add_u32 m0, s41, 69632\n\t" \
  "s_waitcnt lgkmcnt(4)\n\t" \
  "v_mfma_f32_16x16x32_bf16 v[64:67], a[192:195], a[216:219], v[64:67]\n\t" \
  "ds_read_b64_tr_b16 a[208:209], v24 offset:50176\n\t" \
  "ds_read_b64_tr_b16 a[210:211], v24 offset:50432\n\t" \
  "v_mfma_f32_16x16x32_bf16 v[68:71], a[192:195], a[220:223], v[68:71]\n\t" \
  "s_add_u32 s44, s42, 37376\n\t" \
  "v_add_f32 v48, v48, v38\n\t" \
  "v_mfma_f32_16x16x32_bf16 v[72:75], a[192:195], a[224:227], v[72:75]\n\t" \
  "buffer_load_dwordx4 v20, %0, s44 offen offset:0 lds\n\t" \
  "v_mfma_f32_16x16x32_bf16 v[76:79], a[192:195], a[228:231], v[76:79]\n\t" \
  "v_add_f32 v49, v49, v39\n\t" \
  "v_accvgpr_read_b32 v53, a159\n\t" \
  "v_mfma_f32_16x16x32_bf16 v[80:83], a[196:199], a[216:219], v[80:83]\n\t" \
  "ds_read_b64_tr_b16 a[212:213], v25 offset:50176\n\t" \
  "ds_read_b64_tr_b16 a[214:215], v25 offset:50432\n\t" \
  "v_mfma_f32_16x16x32_bf16 v[84:87], a[196:199], a[220:223], v[84:87]\n\t" \
  "v_lshlrev_b32 v38, 16, v53\n\t" \
  "v_and_b32 v39, 0xffff0000, v53\n\t" \
  "v_mfma_f32_16x16x32_bf16 v[88:91], a[196:199], a[224:227], v[88:91]\n\t" \
  "v_add_f32 v50, v50, v38\n\t" \
  "v_add_f32 v51, v51, v39\n\t" \
  "s_waitcnt vmcnt(23)\n\t" \
  "v_mfma_f32_16x16x32_bf16 v[92:95], a[196:199], a[228:231], v[92:95]\n\t" \
  "buffer_load_dwordx4 v21, %0, s44 offen offset:1024 lds\n\t" \
  "s_waitcnt lgkmcnt(4)\n\t" \
  "v_mfma_f32_16x16x32_bf16 v[96:99], a[200:203], a[216:219], v[96:99]\n\t" \
  "ds_read_b64_tr_b16 a[192:193], v24 offset:56320\n\t" \
  "ds_read_b64_tr_b16 a[194:195], v24 offset:56576\n\t" \
  "v_accvgpr_read_b32 v52, a160\n\t" \
  "v_mfma_f32_16x16x32_bf16 v[100:103], a[200:203], a[220:223], v[100:103]\n\t" \
  "buffer_load_dwordx4 v22, %0, s44 offen offset:2048 lds\n\t" \
  "v_lshlrev_b32 v38, 16, v52\n\t" \
  "v_mfma_f32_16x16x32_bf16 v[104:107], a[200:203], a[224:227], v[104:107]\n\t" \
  "v_and_b32 v39, 0xffff0000, v52\n\t" \
  "v_add_f32 v44, v44, v38\n\t" \
  "v_add_f32 v45, v45, v39\n\t" \
  "v_mfma_f32_16x16x32_bf16 v[108:111], a[200:203], a[228:231], v[108:111]\n\t" \
  "v_accvgpr_read_b32 v53, a161\n\t" \
  "v_lshlrev_b32 v38, 16, v53\n\t" \
  "v_mfma_f32_16x16x32_bf16 v[112:115], a[204:207], a[216:219], v[112:115]\n\t" \
  "ds_read_b64_tr_b16 a[196:197], v25 offset:56320\n\t" \
  "ds_read_b64_tr_b16 a[198:199], v25 offset:56576\n\t" \
  "v_mfma_f32_16x16x32_bf16 v[116:119], a[204:207], a[220:223], v[116:119]\n\t" \
  "v_and_b32 v39, 0xffff0000, v53\n\t" \
  "v_add_f32 v46, v46, v38\n\t" \
  "v_mfma_f32_16x16x32_bf16 v[120:123], a[204:207], a[224:227], v[120:123]\n\t" \
  "buffer_load_dwordx4 v23, %0, s44 offen offset:3072 lds\n\t" \
  "v_mfma_f32_16x16x32_bf16 v[124:127], a[204:207], a[228:231], v[124:127]\n\t" \
  "v_add_f32 v47, v47, v39\n\t" \
  "s_waitcnt lgkmcnt(4)\n\t" \
  "v_mfma_f32_16x16x32_bf16 v[128:131], a[208:211], a[216:219], v[128:131]\n\t" \
  "ds_read_b64_tr_b16 a[200:201], v24 offset:51200\n\t" \
  "ds_read_b64_tr_b16 a[202:203], v24 offset:51456\n\t" \
  "v_mfma_f32_16x16x32_bf16 v[132:135], a[208:211], a[220:223], v[132:135]\n\t" \
  "v_accvgpr_read_b32 v52, a162\n\t" \
  "v_lshlrev_b32 v38, 16, v52\n\t" \
  "v_mfma_f32_16x16x32_bf16 v[136:139], a[208:211], a[224:227], v[136:139]\n\t" \
  "v_and_b32 v39, 0xffff0000, v52\n\t" \
  "v_add_f32 v48, v48, v38\n\t" \
  "v_mfma_f32_16x16x32_bf16 v[140:143], a[208:211], a[228:231], v[140:143]\n\t" \
  "v_add_f32 v49, v49, v39\n\t" \
  "v_accvgpr_read_b32 v53, a163\n\t" \
  "v_mfma_f32_16x16x32_bf16 v[144:147], a[212:215], a[216:219], v[144:147]\n\t" \
  "ds_read_b64_tr_b16 a[204:205], v25 offset:51200\n\t" \
  "ds_read_b64_tr_b16 a[206:207], v25 offset:51456\n\t" \
  "v_mfma_f32_16x16x32_bf16 v[148:151], a[212:215], a[220:223], v[148:151]\n\t" \
  "v_lshlrev_b32 v38, 16, v53\n\t" \
  "v_and_b32 v39, 0xffff0000, v53\n\t" \
  "v_mfma_f32_16x16x32_bf16 v[152:155], a[212:215], a[224:227], v[152:155]\n\t" \
  "v_add_f32 v50, v50, v38\n\t" \
  "v_add_f32 v51, v51, v39\n\t" \
  "v_mfma_f32_16x16x32_bf16 v[156:159], a[212:215], a[228:231], v[156:159]\n\t" \
  "v_add_u32 v31, 4294901760, v31\n\t" \
  "s_waitcnt lgkmcnt(4)\n\t" \
  "v_mfma_f32_16x16x32_bf16 v[160:163], a[192:195], a[216:219], v[160:163]\n\t" \
  "ds_read_b64_tr_b16 a[208:209], v24 offset:57344\n\t" \
  "ds_read_b64_tr_b16 a[210:211], v24 offset:57600\n\t" \
  "v_mfma_f32_16x16x32_bf16 v[164:167], a[192:195], a[220:223], v[164:167]\n\t" \
  "ds_read_b128 a[232:235], v31 offset:0\n\t" \
  "ds_read_b128 a[236:239], v31 offset:2048\n\t" \
  "v_mfma_f32_16x16x32_bf16 v[168:171], a[192:195], a[224:227], v[168:171]\n\t" \
  "ds_read_b128 a[240:243], v31 offset:4096\n\t" \
  "ds_read_b128 a[244:247], v31 offset:6144\n\t" \
  "v_mfma_f32_16x16x32_bf16 v[172:175], a[192:195], a[228:231], v[172:175]\n\t" \
  "s_nop 1\n\t" \
  "v_permlane32_swap_b32 v44, v48\n\t" \
  "v_permlane32_swap_b32 v45, v49\n\t" \
  "v_permlane32_swap_b32 v46, v50\n\t" \
  "v_permlane32_swap_b32 v47, v51\n\t" \
  "v_mfma_f32_16x16x32_bf16 v[176:179], a[196:199], a[216:219], v[176:179]\n\t" \
  "ds_read_b64_tr_b16 a[212:213], v25 offset:57344\n\t" \
  "ds_read_b64_tr_b16 a[214:215], v25 offset:57600\n\t" \
  "v_mfma_f32_16x16x32_bf16 v[180:183], a[196:199], a[220:223], v[180:183]\n\t" \
  "v_add_f32 v44, v44, v48\n\t" \
  "v_add_f32 v45, v45, v49\n\t" \
  "v_mfma_f32_16x16x32_bf16 v[184:187], a[196:199], a[224:227], v[184:187]\n\t" \
  "v_add_f32 v46, v46, v50\n\t" \
  "v_add_f32 v47, v47, v51\n\t" \
  "v_mfma_f32_16x16x32_bf16 v[188:191], a[196:199], a[228:231], v[188:191]\n\t" \
  "s_waitcnt vmcnt(23) lgkmcnt(8)\n\t" \
  "s_barrier\n\t" \
  "v_mfma_f32_16x16x32_bf16 v[192:195], a[200:203], a[216:219], v[192:195]\n\t" \
  "ds_read_b64_tr_b16 a[192:193], v24 offset:0\n\t" \
  "ds_read_b64_tr_b16 a[194:195], v24 offset:256\n\t" \
  "v_mfma_f32_16x16x32_bf16 v[196:199], a[200:203], a[220:223], v[196:199]\n\t" \
  "s_add_u32 m0, s40, 36864\n\t" \
  "s_add_u32 s43, s50, 2359296\n\t" \
  "v_mfma_f32_16x16x32_bf16 v[200:203], a[200:203], a[224:227], v[200:203]\n\t" \
  "buffer_load_dwordx4 v17, %1, s43 offen offset:0 lds\n\t" \
  "v_mfma_f32_16x16x32_bf16 v[204:207], a[200:203], a[228:231], v[204:207]\n\t" \
  "s_lshl_b32 s61, %7, 10\n\t" \
  "s_add_u32 s61, s61, 159744\n\t" \
  "v_mfma_f32_16x16x32_bf16 v[208:211], a[204:207], a[216:219], v[208:211]\n\t" \
  "ds_read_b64_tr_b16 a[196:197], v25 offset:0\n\t" \
  "ds_read_b64_tr_b16 a[198:199], v25 offset:256\n\t" \
  "v_mfma_f32_16x16x32_bf16 v[212:215], a[204:207], a[220:223], v[212:215]\n\t" \
  "buffer_load_dwordx4 v18, %1, s43 offen offset:1024 lds\n\t" \
  "v_mfma_f32_16x16x32_bf16 v[216:219], a[204:207], a[224:227], v[216:219]\n\t" \
  "v_add_u32 v57, s61, v32\n\t" \
  "v_mfma_f32_16x16x32_bf16 v[220:223], a[204:207], a[228:231], v[220:223]\n\t" \
  "s_waitcnt lgkmcnt(4)\n\t" \
  "v_mfma_f32_16x16x32_bf16 v[224:227], a[208:211], a[216:219], v[224:227]\n\t" \
  "ds_read_b64_tr_b16 a[200:201], v24 offset:6144\n\t" \
  "ds_read_b64_tr_b16 a[202:203], v24 offset:6400\n\t" \
  "v_mfma_f32_16x16x32_bf16 v[228:231], a[208:211], a[220:223], v[228:231]\n\t" \
  "ds_write_b128 v57, v[44:47]\n\t" \
  "v_add_u32 v57, 159744, v32\n\t" \
  "s_waitcnt lgkmcnt(0)\n\t" \
  "v_mfma_f32_16x16x32_bf16 v[232:235], a[208:211], a[224:227], v[232:235]\n\t" \
  "buffer_load_dwordx4 v19, %1, s43 offen offset:2048 lds\n\t" \
  "v_mfma_f32_16x16x32_bf16 v[236:239], a[208:211], a[228:231], v[236:239]\n\t" \
  "v_mfma_f32_16x16x32_bf16 v[240:243], a[212:215], a[216:219], v[240:243]\n\t" \
  "ds_read_b64_tr_b16 a[204:205], v25 offset:6144\n\t" \
  "ds_read_b64_tr_b16 a[206:207], v25 offset:6400\n\t" \
  "v_mfma_f32_16x16x32_bf16 v[244:247], a[212:215], a[220:223], v[244:247]\n\t" \
  "v_mfma_f32_16x16x32_bf16 v[248:251], a[212:215], a[224:227], v[248:251]\n\t" \
  "v_mfma_f32_16x16x32_bf16 v[252:255], a[212:215], a[228:231], v[252:255]\n\t" \
  "v_mfma_f32_16x16x32_bf16 v[64:67], a[192:195], a[232:235], v[64:67]\n\t" \
  "ds_read_b64_tr_b16 a[208:209], v24 offset:1024\n\t" \
  "ds_read_b64_tr_b16 a[210:211], v24 offset:1280\n\t" \
  "v_mfma_f32_16x16x32_bf16 v[68:71], a[192:195], a[236:239], v[68:71]\n\t" \
  "s_add_u32 m0, s41, 0\n\t" \
  "s_add_u32 s44, s42, 640\n\t" \
  "v_mfma_f32_16x16x32_bf16 v[72:75], a[192:195], a[240:243], v[72:75]\n\t" \
  "buffer_load_dwordx4 v20, %0, s44 offen offset:0 lds\n\t" \
  "v_mfma_f32_16x16x32_bf16 v[76:79], a[192:195], a[244:247], v[76:79]\n\t" \
  "v_mfma_f32_16x16x32_bf16 v[80:83], a[196:199], a[232:235], v[80:83]\n\t" \
  "ds_read_b64_tr_b16 a[212:213], v25 offset:1024\n\t" \
  "ds_read_b64_tr_b16 a[214:215], v25 offset:1280\n\t" \
  "v_mfma_f32_16x16x32_bf16 v[84:87], a[196:199], a[236:239], v[84:87]\n\t" \
  "v_mfma_f32_16x16x32_bf16 v[88:91], a[196:199], a[240:243], v[88:91]\n\t" \
  "buffer_load_dwordx4 v21, %0, s44 offen offset:1024 lds\n\t" \
  "v_mfma_f32_16x16x32_bf16 v[92:95], a[196:199], a[244:247], v[92:95]\n\t" \
  "v_add_u32 v30, 32768, v30\n\t" \
  "s_waitcnt vmcnt(24)\n\t" \
  "s_waitcnt lgkmcnt(4)\n\t" \
  "v_mfma_f32_16x16x32_bf16 v[96:99], a[200:203], a[232:235], v[96:99]\n\t" \
  "ds_read_b64_tr_b16 a[192:193], v24 offset:7168\n\t" \
  "ds_read_b64_tr_b16 a[194:195], v24 offset:7424\n\t" \
  "v_mfma_f32_16x16x32_bf16 v[100:103], a[200:203], a[236:239], v[100:103]\n\t" \
  "ds_read_b128 a[216:219], v30 offset:0\n\t" \
  "ds_read_b128 a[220:223], v30 offset:2048\n\t" \
  "s_waitcnt vmcnt(20)\n\t" \
  "v_mfma_f32_16x16x32_bf16 v[104:107], a[200:203], a[240:243], v[104:107]\n\t" \
  "ds_read_b128 a[224:227], v30 offset:4096\n\t" \
  "ds_read_b128 a[228:231], v30 offset:6144\n\t" \
  "v_mfma_f32_16x16x32_bf16 v[108:111], a[200:203], a[244:247], v[108:111]\n\t" \
  "buffer_load_dwordx4 v22, %0, s44 offen offset:2048 lds\n\t" \
  "v_mfma_f32_16x16x32_bf16 v[112:115], a[204:207], a[232:235], v[112:115]\n\t" \
  "ds_read_b64_tr_b16 a[196:197], v25 offset:7168\n\t" \
  "ds_read_b64_tr_b16 a[198:199], v25 offset:7424\n\t" \
  "v_mfma_f32_16x16x32_bf16 v[116:119], a[204:207], a[236:239], v[116:119]\n\t" \
  "v_mfma_f32_16x16x32_bf16 v[120:123], a[204:207], a[240:243], v[120:123]\n\t" \
  "v_mfma_f32_16x16x32_bf16 v[124:127], a[204:207], a[244:247], v[124:127]\n\t" \
  "s_waitcnt lgkmcnt(8)\n\t" \
  "v_mfma_f32_16x16x32_bf16 v[128:131], a[208:211], a[232:235], v[128:131]\n\t" \
  "ds_read_b64_tr_b16 a[200:201], v24 offset:2048\n\t" \
  "ds_read_b64_tr_b16 a[202:203], v24 offset:2304\n\t" \
  "v_mfma_f32_16x16x32_bf16 v[132:135], a[208:211], a[236:239], v[132:135]\n\t" \
  "buffer_load_dwordx4 v23, %0, s44 offen offset:3072 lds\n\t" \
  "v_mfma_f32_16x16x32_bf16 v[136:139], a[208:211], a[240:243], v[136:139]\n\t" \
  "v_mfma_f32_16x16x32_bf16 v[140:143], a[208:211], a[244:247], v[140:143]\n\t" \
  "v_mfma_f32_16x16x32_bf16 v[144:147], a[212:215], a[232:235], v[144:147]\n\t" \
  "ds_read_b64_tr_b16 a[204:205], v25 offset:2048\n\t" \
  "ds_read_b64_tr_b16 a[206:207], v25 offset:2304\n\t" \
  "v_mfma_f32_16x16x32_bf16 v[148:151], a[212:215], a[236:239], v[148:151]\n\t" \
  "v_mfma_f32_16x16x32_bf16 v[152:155], a[212:215], a[240:243], v[152:155]\n\t" \
  "v_mfma_f32_16x16x32_bf16 v[156:159], a[212:215], a[244:247], v[156:159]\n\t" \
  "s_waitcnt lgkmcnt(4)\n\t" \
  "v_mfma_f32_16x16x32_bf16 v[160:163], a[192:195], a[232:235], v[160:163]\n\t" \
  "ds_read_b64_tr_b16 a[208:209], v24 offset:8192\n\t" \
  "ds_read_b64_tr_b16 a[210:211], v24 offset:8448\n\t" \
  "v_mfma_f32_16x16x32_bf16 v[164:167], a[192:195], a[236:239], v[164:167]\n\t" \
  "v_mfma_f32_16x16x32_bf16 v[168:171], a[192:195], a[240:243], v[168:171]\n\t" \
  "v_mfma_f32_16x16x32_bf16 v[172:175], a[192:195], a[244:247], v[172:175]\n\t" \
  "v_mfma_f32_16x16x32_bf16 v[176:179], a[196:199], a[232:235], v[176:179]\n\t" \
  "ds_read_b64_tr_b16 a[212:213], v25 offset:8192\n\t" \
  "ds_read_b64_tr_b16 a[214:215], v25 offset:8448\n\t" \
  "v_mfma_f32_16x16x32_bf16 v[180:183], a[196:199], a[236:239], v[180:183]\n\t" \
  "v_mfma_f32_16x16x32_bf16 v[184:187], a[196:199], a[240:243], v[184:187]\n\t" \
  "v_mfma_f32_16x16x32_bf16 v[188:191], a[196:199], a[244:247], v[188:191]\n\t" \
  "s_waitcnt vmcnt(19) lgkmcnt(4)\n\t" \
  "s_barrier\n\t" \
  "v_mfma_f32_16x16x32_bf16 v[192:195], a[200:203], a[232:235], v[192:195]\n\t" \
  "ds_read_b64_tr_b16 a[192:193], v24 offset:12288\n\t" \
  "ds_read_b64_tr_b16 a[194:195], v24 offset:12544\n\t" \
  "v_mfma_f32_16x16x32_bf16 v[196:199], a[200:203], a[236:239], v[196:199]\n\t" \
  "s_add_u32 m0, s40, 49152\n\t" \
  "s_add_u32 s43, s50, 2654208\n\t" \
  "v_mfma_f32_16x16x32_bf16 v[200:203], a[200:203], a[240:243], v[200:203]\n\t" \
  "buffer_load_dwordx4 v17, %1, s43 offen offset:0 lds\n\t" \
  "v_mfma_f32_16x16x32_bf16 v[204:207], a[200:203], a[244:247], v[204:207]\n\t" \
  "ds_read_b128 v[44:47], v57 offset:0\n\t" \
  "ds_read_b128 v[48:51], v57 offset:1024\n\t" \
  "v_mfma_f32_16x16x32_bf16 v[208:211], a[204:207], a[232:235], v[208:211]\n\t" \
  "ds_read_b64_tr_b16 a[196:197], v25 offset:12288\n\t" \
  "ds_read_b64_tr_b16 a[198:199], v25 offset:12544\n\t" \
  "v_mfma_f32_16x16x32_bf16 v[212:215], a[204:207], a[236:239], v[212:215]\n\t" \
  "buffer_load_dwordx4 v18, %1, s43 offen offset:1024 lds\n\t" \
  "v_mfma_f32_16x16x32_bf16 v[216:219], a[204:207], a[240:243], v[216:219]\n\t" \
  "ds_read_b128 v[52:55], v57 offset:2048\n\t" \
  "v_mfma_f32_16x16x32_bf16 v[220:223], a[204:207], a[244:247], v[220:223]\n\t" \
  "s_waitcnt lgkmcnt(7)\n\t" \
  "v_mfma_f32_16x16x32_bf16 v[224:227], a[208:211], a[232:235], v[224:227]\n\t" \
  "ds_read_b64_tr_b16 a[200:201], v24 offset:18432\n\t" \
  "ds_read_b64_tr_b16 a[202:203], v24 offset:18688\n\t" \
  "ds_read_b128 v[40:43], v57 offset:3072\n\t" \
  "v_mfma_f32_16x16x32_bf16 v[228:231], a[208:211], a[236:239], v[228:231]\n\t" \
  "v_and_b32 v38, 31, v16\n\t" \
  "v_lshrrev_b32 v39, 5, v16\n\t" \
  "v_lshlrev_b32 v39, 4, v39\n\t" \
  "v_mfma_f32_16x16x32_bf16 v[232:235], a[208:211], a[240:243], v[232:235]\n\t" \
  "buffer_load_dwordx4 v19, %1, s43 offen offset:2048 lds\n\t" \
  "v_mfma_f32_16x16x32_bf16 v[236:239], a[208:211], a[244:247], v[236:239]\n\t" \
  "v_lshl_add_u32 v38, v38, 5, v39\n\t" \
  "s_cmp_eq_u32 %7, 0\n\t" \
  "s_cselect_b32 s60, 0, 0x70000000\n\t" \
  "v_mfma_f32_16x16x32_bf16 v[240:243], a[212:215], a[232:235], v[240:243]\n\t" \
  "ds_read_b64_tr_b16 a[204:205], v25 offset:18432\n\t" \
  "ds_read_b64_tr_b16 a[206:207], v25 offset:18688\n\t" \
  "v_mfma_f32_16x16x32_bf16 v[244:247], a[212:215], a[236:239], v[244:247]\n\t" \
  "v_and_b32 v56, 0x70000000, v56\n\t" \
  "v_or3_b32 v56, v56, v38, s60\n\t" \
  "v_mfma_f32_16x16x32_bf16 v[248:251], a[212:215], a[240:243], v[248:251]\n\t" \
  "v_or_b32 v57, s60, v34\n\t" \
  "s_mul_i32 s61, %4, 18432\n\t" \
  "v_mfma_f32_16x16x32_bf16 v[252:255], a[212:215], a[244:247], v[252:255]\n\t" \
  "s_add_u32 m0, s41, 4096\n\t" \
  "s_waitcnt lgkmcnt(6)\n\t" \
  "v_mfma_f32_16x16x32_bf16 v[64:67], a[192:195], a[216:219], v[64:67]\n\t" \
  "ds_read_b64_tr_b16 a[208:209], v24 offset:13312\n\t" \
  "ds_read_b64_tr_b16 a[210:211], v24 offset:13568\n\t" \
  "v_mfma_f32_16x16x32_bf16 v[68:71], a[192:195], a[220:223], v[68:71]\n\t" \
  "s_add_u32 s44, s42, 37504\n\t" \
  "s_add_u32 s61, s61, s63\n\t" \
  "s_waitcnt lgkmcnt(4)\n\t" \
  "v_mfma_f32_16x16x32_bf16 v[72:75], a[192:195], a[224:227], v[72:75]\n\t" \
  "buffer_load_dwordx4 v20, %0, s44 offen offset:0 lds\n\t" \
  "v_mfma_f32_16x16x32_bf16 v[76:79], a[192:195], a[228:231], v[76:79]\n\t" \
  "v_add_f32 v44, v44, v48\n\t" \
  "v_add_f32 v45, v45, v49\n\t" \
  "v_mfma_f32_16x16x32_bf16 v[80:83], a[196:199], a[216:219], v[80:83]\n\t" \
  "ds_read_b64_tr_b16 a[212:213], v25 offset:13312\n\t" \
  "ds_read_b64_tr_b16 a[214:215], v25 offset:13568\n\t" \
  "v_mfma_f32_16x16x32_bf16 v[84:87], a[196:199], a[220:223], v[84:87]\n\t" \
  "buffer_load_dwordx4 v21, %0, s44 offen offset:1024 lds\n\t" \
  "v_mfma_f32_16x16x32_bf16 v[88:91], a[196:199], a[224:227], v[88:91]\n\t" \
  "v_add_f32 v46, v46, v50\n\t" \
  "v_add_f32 v47, v47, v51\n\t" \
  "v_mfma_f32_16x16x32_bf16 v[92:95], a[196:199], a[228:231], v[92:95]\n\t" \
  "v_add_f32 v52, v52, v40\n\t" \
  "s_waitcnt lgkmcnt(4)\n\t" \
  "v_mfma_f32_16x16x32_bf16 v[96:99], a[200:203], a[216:219], v[96:99]\n\t" \
  "ds_read_b64_tr_b16 a[192:193], v24 offset:19456\n\t" \
  "ds_read_b64_tr_b16 a[194:195], v24 offset:19712\n\t" \
  "v_mfma_f32_16x16x32_bf16 v[100:103], a[200:203], a[220:223], v[100:103]\n\t" \
  "buffer_load_dwordx4 v22, %0, s44 offen offset:2048 lds\n\t" \
  "v_mfma_f32_16x16x32_bf16 v[104:107], a[200:203], a[224:227], v[104:107]\n\t" \
  "v_add_f32 v53, v53, v41\n\t" \
  "v_add_f32 v54, v54, v42\n\t" \
  "v_mfma_f32_16x16x32_bf16 v[108:111], a[200:203], a[228:231], v[108:111]\n\t" \
  "v_add_f32 v55, v55, v43\n\t" \
  "v_add_f32 v44, v44, v52\n\t" \
  "v_mfma_f32_16x16x32_bf16 v[112:115], a[204:207], a[216:219], v[112:115]\n\t" \
  "ds_read_b64_tr_b16 a[196:197], v25 offset:19456\n\t" \
  "ds_read_b64_tr_b16 a[198:199], v25 offset:19712\n\t" \
  "v_mfma_f32_16x16x32_bf16 v[116:119], a[204:207], a[220:223], v[116:119]\n\t" \
  "v_add_f32 v45, v45, v53\n\t" \
  "v_add_f32 v46, v46, v54\n\t" \
  "v_mfma_f32_16x16x32_bf16 v[120:123], a[204:207], a[224:227], v[120:123]\n\t" \
  "buffer_load_dwordx4 v23, %0, s44 offen offset:3072 lds\n\t" \
  "v_mfma_f32_16x16x32_bf16 v[124:127], a[204:207], a[228:231], v[124:127]\n\t" \
  "v_add_f32 v47, v47, v55\n\t" \
  "s_waitcnt lgkmcnt(4)\n\t" \
  "v_mfma_f32_16x16x32_bf16 v[128:131], a[208:211], a[216:219], v[128:131]\n\t" \
  "ds_read_b64_tr_b16 a[200:201], v24 offset:14336\n\t" \
  "ds_read_b64_tr_b16 a[202:203], v24 offset:14592\n\t" \
  "v_mfma_f32_16x16x32_bf16 v[132:135], a[208:211], a[220:223], v[132:135]\n\t" \
  "buffer_store_dwordx4 v[44:47], v56, %3, s61 offen sc1\n\t" \
  "v_mfma_f32_16x16x32_bf16 v[136:139], a[208:211], a[224:227], v[136:139]\n\t" \
  "s_lshl_b32 s61, s49, 3\n\t" \
  "s_add_u32 s61, s61, %4\n\t" \
  "v_mfma_f32_16x16x32_bf16 v[140:143], a[208:211], a[228:231], v[140:143]\n\t" \
  "s_lshl_b32 s61, s61, 7\n\t" \
  "s_add_u32 s61, s61, 851968\n\t" \
  "v_mfma_f32_16x16x32_bf16 v[144:147], a[212:215], a[216:219], v[144:147]\n\t" \
  "ds_read_b64_tr_b16 a[204:205], v25 offset:14336\n\t" \
  "ds_read_b64_tr_b16 a[206:207], v25 offset:14592\n\t" \
  "v_mfma_f32_16x16x32_bf16 v[148:151], a[212:215], a[220:223], v[148:151]\n\t" \
  "v_add_u32 v31, 32768, v31\n\t" \
  "ds_read_b128 a[232:235], v31 offset:0\n\t" \
  "v_mfma_f32_16x16x32_bf16 v[152:155], a[212:215], a[224:227], v[152:155]\n\t" \
  "ds_read_b128 a[236:239], v31 offset:2048\n\t" \
  "ds_read_b128 a[240:243], v31 offset:4096\n\t" \
  "v_mfma_f32_16x16x32_bf16 v[156:159], a[212:215], a[228:231], v[156:159]\n\t" \
  "s_lshl_b32 s54, %6, 3\n\t" \
  "s_waitcnt lgkmcnt(7)\n\t" \
  "v_mfma_f32_16x16x32_bf16 v[160:163], a[192:195], a[216:219], v[160:163]\n\t" \
  "ds_read_b64_tr_b16 a[208:209], v24 offset:20480\n\t" \
  "ds_read_b64_tr_b16 a[210:211], v24 offset:20736\n\t" \
  "v_mfma_f32_16x16x32_bf16 v[164:167], a[192:195], a[220:223], v[164:167]\n\t" \
  "ds_read_b128 a[244:247], v31 offset:6144\n\t" \
  "s_add_u32 s54, s54, %4\n\t" \
  "v_mfma_f32_16x16x32_bf16 v[168:171], a[192:195], a[224:227], v[168:171]\n\t" \
  "s_lshl_b32 s54, s54, 3\n\t" \
  "s_add_u32 s54, s54, %5\n\t" \
  "v_mfma_f32_16x16x32_bf16 v[172:175], a[192:195], a[228:231], v[172:175]\n\t" \
  "s_lshl_b32 s54, s54, 6\n\t" \
  "s_add_u32 s54, s54, 8192\n\t" \
  "v_mfma_f32_16x16x32_bf16 v[176:179], a[196:199], a[216:219], v[176:179]\n\t" \
  "ds_read_b64_tr_b16 a[212:213], v25 offset:20480\n\t" \
  "ds_read_b64_tr_b16 a[214:215], v25 offset:20736\n\t" \
  "v_mfma_f32_16x16x32_bf16 v[180:183], a[196:199], a[220:223], v[180:183]\n\t" \
  "buffer_store_dwordx2 v[36:37], v34, %3, s54 offen sc1\n\t" \
  "v_mfma_f32_16x16x32_bf16 v[184:187], a[196:199], a[224:227], v[184:187]\n\t" \
  "v_mfma_f32_16x16x32_bf16 v[188:191], a[196:199], a[228:231], v[188:191]\n\t" \
  "s_waitcnt vmcnt(20) lgkmcnt(8)\n\t" \
  "s_barrier\n\t" \
  "v_mfma_f32_16x16x32_bf16 v[192:195], a[200:203], a[216:219], v[192:195]\n\t" \
  "ds_read_b64_tr_b16 a[192:193], v24 offset:24576\n\t" \
  "ds_read_b64_tr_b16 a[194:195], v24 offset:24832\n\t" \
  "v_mfma_f32_16x16x32_bf16 v[196:199], a[200:203], a[220:223], v[196:199]\n\t" \
  "s_add_u32 m0, s40, 0\n\t" \
  "s_add_u32 s43, s50, 2949120\n\t" \
  "v_mfma_f32_16x16x32_bf16 v[200:203], a[200:203], a[224:227], v[200:203]\n\t" \
  "buffer_load_dwordx4 v17, %1, s43 offen offset:0 lds\n\t" \
  "v_mfma_f32_16x16x32_bf16 v[204:207], a[200:203], a[228:231], v[204:207]\n\t" \
  "v_mfma_f32_16x16x32_bf16 v[208:211], a[204:207], a[216:219], v[208:211]\n\t" \
  "ds_read_b64_tr_b16 a[196:197], v25 offset:24576\n\t" \
  "ds_read_b64_tr_b16 a[198:199], v25 offset:24832\n\t" \
  "v_mfma_f32_16x16x32_bf16 v[212:215], a[204:207], a[220:223], v[212:215]\n\t" \
  "buffer_load_dwordx4 v18, %1, s43 offen offset:1024 lds\n\t" \
  "v_mfma_f32_16x16x32_bf16 v[216:219], a[204:207], a[224:227], v[216:219]\n\t" \
  "v_mfma_f32_16x16x32_bf16 v[220:223], a[204:207], a[228:231], v[220:223]\n\t" \
  "s_waitcnt lgkmcnt(4)\n\t" \
  "v_mfma_f32_16x16x32_bf16 v[224:227], a[208:211], a[216:219], v[224:227]\n\t" \
  "ds_read_b64_tr_b16 a[200:201], v24 offset:30720\n\t" \
  "ds_read_b64_tr_b16 a[202:203], v24 offset:30976\n\t" \
  "v_mfma_f32_16x16x32_bf16 v[228:231], a[208:211], a[220:223], v[228:231]\n\t" \
  "v_mfma_f32_16x16x32_bf16 v[232:235], a[208:211], a[224:227], v[232:235]\n\t" \
  "buffer_load_dwordx4 v19, %1, s43 offen offset:2048 lds\n\t" \
  "v_mfma_f32_16x16x32_bf16 v[236:239], a[208:211], a[228:231], v[236:239]\n\t" \
  "v_mfma_f32_16x16x32_bf16 v[240:243], a[212:215], a[216:219], v[240:243]\n\t" \
  "ds_read_b64_tr_b16 a[204:205], v25 offset:30720\n\t" \
  "ds_read_b64_tr_b16 a[206:207], v25 offset:30976\n\t" \
  "v_mfma_f32_16x16x32_bf16 v[244:247], a[212:215], a[220:223], v[244:247]\n\t" \
  "v_mfma_f32_16x16x32_bf16 v[248:251], a[212:215], a[224:227], v[248:251]\n\t" \
  "v_mfma_f32_16x16x32_bf16 v[252:255], a[212:215], a[228:231], v[252:255]\n\t" \
  "s_waitcnt lgkmcnt(4)\n\t" \
  "v_mfma_f32_16x16x32_bf16 v[64:67], a[192:195], a[232:235], v[64:67]\n\t" \
  "ds_read_b64_tr_b16 a[208:209], v24 offset:25600\n\t" \
  "ds_read_b64_tr_b16 a[210:211], v24 offset:25856\n\t" \
  "v_mfma_f32_16x16x32_bf16 v[68:71], a[192:195], a[236:239], v[68:71]\n\t" \
  "s_add_u32 m0, s41, 32768\n\t" \
  "s_add_u32 s44, s42, 768\n\t" \
  "v_mfma_f32_16x16x32_bf16 v[72:75], a[192:195], a[240:243], v[72:75]\n\t" \
  "buffer_load_dwordx4 v20, %0, s44 offen offset:0 lds\n\t" \
  "v_mfma_f32_16x16x32_bf16 v[76:79], a[192:195], a[244:247], v[76:79]\n\t" \
  "s_mul_i32 s51, s49, 221184\n\t" \
  "s_mul_i32 s72, %6, 384\n\t" \
  "v_mfma_f32_16x16x32_bf16 v[80:83], a[196:199], a[232:235], v[80:83]\n\t" \
  "ds_read_b64_tr_b16 a[212:213], v25 offset:25600\n\t" \
  "ds_read_b64_tr_b16 a[214:215], v25 offset:25856\n\t" \
  "v_mfma_f32_16x16x32_bf16 v[84:87], a[196:199], a[236:239], v[84:87]\n\t" \
  "s_add_u32 s51, s51, s72\n\t" \
  "s_add_u32 s71, %6, 1\n\t" \
  "v_mfma_f32_16x16x32_bf16 v[88:91], a[196:199], a[240:243], v[88:91]\n\t" \
  "buffer_load_dwordx4 v21, %0, s44 offen offset:1024 lds\n\t" \
  "v_mfma_f32_16x16x32_bf16 v[92:95], a[196:199], a[244:247], v[92:95]\n\t" \
  "v_add_u32 v30, 32768, v30\n\t" \
  "s_waitcnt lgkmcnt(4)\n\t" \
  "v_mfma_f32_16x16x32_bf16 v[96:99], a[200:203], a[232:235], v[96:99]\n\t" \
  "ds_read_b64_tr_b16 a[192:193], v24 offset:31744\n\t" \
  "ds_read_b64_tr_b16 a[194:195], v24 offset:32000\n\t" \
  "v_mfma_f32_16x16x32_bf16 v[100:103], a[200:203], a[236:239], v[100:103]\n\t" \
  "ds_read_b128 a[216:219], v30 offset:0\n\t" \
  "ds_read_b128 a[220:223], v30 offset:2048\n\t" \
  "s_waitcnt vmcnt(21)\n\t" \
  "v_mfma_f32_16x16x32_bf16 v[104:107], a[200:203], a[240:243], v[104:107]\n\t" \
  "ds_read_b128 a[224:227], v30 offset:4096\n\t" \
  "ds_read_b128 a[228:231], v30 offset:6144\n\t" \
  "v_mfma_f32_16x16x32_bf16 v[108:111], a[200:203], a[244:247], v[108:111]\n\t" \
  "buffer_load_dwordx4 v22, %0, s44 offen offset:2048 lds\n\t" \
  "v_mfma_f32_16x16x32_bf16 v[112:115], a[204:207], a[232:235], v[112:115]\n\t" \
  "ds_read_b64_tr_b16 a[196:197], v25 offset:31744\n\t" \
  "ds_read_b64_tr_b16 a[198:199], v25 offset:32000\n\t" \
  "v_mfma_f32_16x16x32_bf16 v[116:119], a[204:207], a[236:239], v[116:119]\n\t" \
  "s_cmp_ge_u32 s71, 3\n\t" \
  "s_cselect_b32 s72, 3, 0\n\t" \
  "v_mfma_f32_16x16x32_bf16 v[120:123], a[204:207], a[240:243], v[120:123]\n\t" \
  "s_sub_u32 s71, s71, s72\n\t" \
  "s_mul_i32 s72, %5, 3\n\t" \
  "v_mfma_f32_16x16x32_bf16 v[124:127], a[204:207], a[244:247], v[124:127]\n\t" \
  "s_add_u32 s71, s71, s72\n\t" \
  "s_waitcnt lgkmcnt(8)\n\t" \
  "v_mfma_f32_16x16x32_bf16 v[128:131], a[208:211], a[232:235], v[128:131]\n\t" \
  "ds_read_b64_tr_b16 a[200:201], v24 offset:26624\n\t" \
  "ds_read_b64_tr_b16 a[202:203], v24 offset:26880\n\t" \
  "s_waitcnt vmcnt(7)\n\t" \
  "v_mfma_f32_16x16x32_bf16 v[132:135], a[208:211], a[236:239], v[132:135]\n\t" \
  "buffer_load_dwordx4 v23, %0, s44 offen offset:3072 lds\n\t" \
  "v_mfma_f32_16x16x32_bf16 v[136:139], a[208:211], a[240:243], v[136:139]\n\t" \
  "s_mul_i32 s72, %4, 24\n\t" \
  "s_add_u32 s72, s72, s71\n\t" \
  "v_mfma_f32_16x16x32_bf16 v[140:143], a[208:211], a[244:247], v[140:143]\n\t" \
  "buffer_store_dwordx2 v[36:37], v57, %3, s61 offen sc1\n\t" \
  "v_mfma_f32_16x16x32_bf16 v[144:147], a[212:215], a[232:235], v[144:147]\n\t" \
  "ds_read_b64_tr_b16 a[204:205], v25 offset:26624\n\t" \
  "ds_read_b64_tr_b16 a[206:207], v25 offset:26880\n\t" \
  "v_mfma_f32_16x16x32_bf16 v[148:151], a[212:215], a[236:239], v[148:151]\n\t" \
  "s_lshl_b32 s72, s72, 2\n\t" \
  "s_add_u32 s72, s72, %7\n\t" \
  "v_mfma_f32_16x16x32_bf16 v[152:155], a[212:215], a[240:243], v[152:155]\n\t" \
  "s_mul_i32 s46, s72, 24576\n\t" \
  "s_add_u32 s46, s46, 10485760\n\t" \
  "v_mfma_f32_16x16x32_bf16 v[156:159], a[212:215], a[244:247], v[156:159]\n\t" \
  "s_lshl_b32 s79, s72, 7\n\t" \
  "s_waitcnt lgkmcnt(4)\n\t" \
  "v_mfma_f32_16x16x32_bf16 v[160:163], a[192:195], a[232:235], v[160:163]\n\t" \
  "ds_read_b64_tr_b16 a[208:209], v24 offset:32768\n\t" \
  "ds_read_b64_tr_b16 a[210:211], v24 offset:33024\n\t" \
  "v_mfma_f32_16x16x32_bf16 v[164:167], a[192:195], a[236:239], v[164:167]\n\t" \
  "s_add_u32 s79, s79, 65536\n\t" \
  "s_mul_i32 s71, s71, 221184\n\t" \
  "v_mfma_f32_16x16x32_bf16 v[168:171], a[192:195], a[240:243], v[168:171]\n\t" \
  "s_mul_i32 s72, %6, 384\n\t" \
  "s_add_u32 s52, s71, s72\n\t" \
  "v_mfma_f32_16x16x32_bf16 v[172:175], a[192:195], a[244:247], v[172:175]\n\t" \
  "s_add_u32 s71, %6, 2\n\t" \
  "s_cmp_ge_u32 s71, 3\n\t" \
  "s_cselect_b32 s72, 3, 0\n\t" \
  "v_mfma_f32_16x16x32_bf16 v[176:179], a[196:199], a[232:235], v[176:179]\n\t" \
  "ds_read_b64_tr_b16 a[212:213], v25 offset:32768\n\t" \
  "ds_read_b64_tr_b16 a[214:215], v25 offset:33024\n\t" \
  "v_mfma_f32_16x16x32_bf16 v[180:183], a[196:199], a[236:239], v[180:183]\n\t" \
  "s_sub_u32 s71, s71, s72\n\t" \
  "s_mul_i32 s72, %5, 3\n\t" \
  "v_mfma_f32_16x16x32_bf16 v[184:187], a[196:199], a[240:243], v[184:187]\n\t" \
  "s_add_u32 s71, s71, s72\n\t" \
  "s_mul_i32 s72, %4, 24\n\t" \
  "v_mfma_f32_16x16x32_bf16 v[188:191], a[196:199], a[244:247], v[188:191]\n\t" \
  "s_waitcnt lgkmcnt(4)\n\t" \
  "s_barrier\n\t" \
  "v_mfma_f32_16x16x32_bf16 v[192:195], a[200:203], a[232:235], v[192:195]\n\t" \
  "ds_read_b64_tr_b16 a[192:193], v24 offset:36864\n\t" \
  "ds_read_b64_tr_b16 a[194:195], v24 offset:37120\n\t" \
  "v_mfma_f32_16x16x32_bf16 v[196:199], a[200:203], a[236:239], v[196:199]\n\t" \
  "s_add_u32 m0, s40, 12288\n\t" \
  "s_add_u32 s43, s50, 3244032\n\t" \
  "v_mfma_f32_16x16x32_bf16 v[200:203], a[200:203], a[240:243], v[200:203]\n\t" \
  "buffer_load_dwordx4 v17, %1, s43 offen offset:0 lds\n\t" \
  "v_mfma_f32_16x16x32_bf16 v[204:207], a[200:203], a[244:247], v[204:207]\n\t" \
  "s_add_u32 s72, s72, s71\n\t" \
  "s_lshl_b32 s72, s72, 2\n\t" \
  "v_mfma_f32_16x16x32_bf16 v[208:211], a[204:207], a[232:235], v[208:211]\n\t" \
  "ds_read_b64_tr_b16 a[196:197], v25 offset:36864\n\t" \
  "ds_read_b64_tr_b16 a[198:199], v25 offset:37120\n\t" \
  "v_mfma_f32_16x16x32_bf16 v[212:215], a[204:207], a[236:239], v[212:215]\n\t" \
  "buffer_load_dwordx4 v18, %1, s43 offen offset:1024 lds\n\t" \
  "v_mfma_f32_16x16x32_bf16 v[216:219], a[204:207], a[240:243], v[216:219]\n\t" \
  "s_add_u32 s72, s72, %7\n\t" \
  "s_mul_i32 s47, s72, 24576\n\t" \
  "v_mfma_f32_16x16x32_bf16 v[220:223], a[204:207], a[244:247], v[220:223]\n\t" \
  "s_add_u32 s47, s47, 10485760\n\t" \
  "s_waitcnt lgkmcnt(4)\n\t" \
  "v_mfma_f32_16x16x32_bf16 v[224:227], a[208:211], a[232:235], v[224:227]\n\t" \
  "ds_read_b64_tr_b16 a[200:201], v24 offset:43008\n\t" \
  "ds_read_b64_tr_b16 a[202:203], v24 offset:43264\n\t" \
  "v_mfma_f32_16x16x32_bf16 v[228:231], a[208:211], a[236:239], v[228:231]\n\t" \
  "s_lshl_b32 s95, s72, 7\n\t" \
  "s_add_u32 s95, s95, 65536\n\t" \
  "v_mfma_f32_16x16x32_bf16 v[232:235], a[208:211], a[240:243], v[232:235]\n\t" \
  "buffer_load_dwordx4 v19, %1, s43 offen offset:2048 lds\n\t" \
  "v_mfma_f32_16x16x32_bf16 v[236:239], a[208:211], a[244:247], v[236:239]\n\t" \
  "s_mul_i32 s71, s71, 221184\n\t" \
  "s_mul_i32 s72, %6, 384\n\t" \
  "v_mfma_f32_16x16x32_bf16 v[240:243], a[212:215], a[232:235], v[240:243]\n\t" \
  "ds_read_b64_tr_b16 a[204:205], v25 offset:43008\n\t" \
  "ds_read_b64_tr_b16 a[206:207], v25 offset:43264\n\t" \
  "v_mfma_f32_16x16x32_bf16 v[244:247], a[212:215], a[236:239], v[244:247]\n\t" \
  "s_add_u32 s53, s71, s72\n\t" \
  "s_mul_i32 s71, %4, 24\n\t" \
  "v_mfma_f32_16x16x32_bf16 v[248:251], a[212:215], a[240:243], v[248:251]\n\t" \
  "s_add_u32 s71, s71, s49\n\t" \
  "s_lshl_b32 s71, s71, 2\n\t" \
  "v_mfma_f32_16x16x32_bf16 v[252:255], a[212:215], a[244:247], v[252:255]\n\t" \
  "s_add_u32 m0, s41, 36864\n\t" \
  "s_waitcnt lgkmcnt(4)\n\t" \
  "v_mfma_f32_16x16x32_bf16 v[64:67], a[192:195], a[216:219], v[64:67]\n\t" \
  "ds_read_b64_tr_b16 a[208:209], v24 offset:37888\n\t" \
  "ds_read_b64_tr_b16 a[210:211], v24 offset:38144\n\t" \
  "v_mfma_f32_16x16x32_bf16 v[68:71], a[192:195], a[220:223], v[68:71]\n\t" \
  "s_add_u32 s44, s42, 37632\n\t" \
  "s_add_u32 s71, s71, %7\n\t" \
  "v_mfma_f32_16x16x32_bf16 v[72:75], a[192:195], a[224:227], v[72:75]\n\t" \
  "buffer_load_dwordx4 v20, %0, s44 offen offset:0 lds\n\t" \
  "v_mfma_f32_16x16x32_bf16 v[76:79], a[192:195], a[228:231], v[76:79]\n\t" \
  "buffer_load_dwordx2 v[38:39], v59, %3, 0 offen offset:0 sc1\n\t" \
  "v_mfma_f32_16x16x32_bf16 v[80:83], a[196:199], a[216:219], v[80:83]\n\t" \
  "ds_read_b64_tr_b16 a[212:213], v25 offset:37888\n\t" \
  "ds_read_b64_tr_b16 a[214:215], v25 offset:38144\n\t" \
  "v_mfma_f32_16x16x32_bf16 v[84:87], a[196:199], a[220:223], v[84:87]\n\t" \
  "buffer_load_dwordx4 v21, %0, s44 offen offset:1024 lds\n\t" \
  "v_mfma_f32_16x16x32_bf16 v[88:91], a[196:199], a[224:227], v[88:91]\n\t" \
  "v_add_u32 v31, 32768, v31\n\t" \
  "ds_read_b128 a[232:235], v31 offset:0\n\t" \
  "v_mfma_f32_16x16x32_bf16 v[92:95], a[196:199], a[228:231], v[92:95]\n\t" \
  "ds_read_b128 a[236:239], v31 offset:2048\n\t" \
  "s_waitcnt lgkmcnt(6)\n\t" \
  "v_mfma_f32_16x16x32_bf16 v[96:99], a[200:203], a[216:219], v[96:99]\n\t" \
  "ds_read_b64_tr_b16 a[192:193], v24 offset:44032\n\t" \
  "ds_read_b64_tr_b16 a[194:195], v24 offset:44288\n\t" \
  "v_mfma_f32_16x16x32_bf16 v[100:103], a[200:203], a[220:223], v[100:103]\n\t" \
  "buffer_load_dwordx4 v22, %0, s44 offen offset:2048 lds\n\t" \
  "v_mfma_f32_16x16x32_bf16 v[104:107], a[200:203], a[224:227], v[104:107]\n\t" \
  "ds_read_b128 a[240:243], v31 offset:4096\n\t" \
  "ds_read_b128 a[244:247], v31 offset:6144\n\t" \
  "v_mfma_f32_16x16x32_bf16 v[108:111], a[200:203], a[228:231], v[108:111]\n\t" \
  "s_mul_i32 s45, s71, 24576\n\t" \
  "s_add_u32 s45, s45, 10485760\n\t" \
  "v_mfma_f32_16x16x32_bf16 v[112:115], a[204:207], a[216:219], v[112:115]\n\t" \
  "ds_read_b64_tr_b16 a[196:197], v25 offset:44032\n\t" \
  "ds_read_b64_tr_b16 a[198:199], v25 offset:44288\n\t" \
  "v_mfma_f32_16x16x32_bf16 v[116:119], a[204:207], a[220:223], v[116:119]\n\t" \
  "s_lshl_b32 s64, s71, 7\n\t" \
  "s_add_u32 s64, s64, 65536\n\t" \
  "v_mfma_f32_16x16x32_bf16 v[120:123], a[204:207], a[224:227], v[120:123]\n\t" \
  "buffer_load_dwordx4 v23, %0, s44 offen offset:3072 lds\n\t" \
  "v_mfma_f32_16x16x32_bf16 v[124:127], a[204:207], a[228:231], v[124:127]\n\t" \
  "s_mul_i32 s71, %4, 3\n\t" \
  "s_waitcnt lgkmcnt(8)\n\t" \
  "v_mfma_f32_16x16x32_bf16 v[128:131], a[208:211], a[216:219], v[128:131]\n\t" \
  "ds_read_b64_tr_b16 a[200:201], v24 offset:38912\n\t" \
  "ds_read_b64_tr_b16 a[202:203], v24 offset:39168\n\t" \
  "v_mfma_f32_16x16x32_bf16 v[132:135], a[208:211], a[220:223], v[132:135]\n\t" \
  "s_add_u32 s71, s71, %6\n\t" \
  "s_lshl_b32 s72, s71, 2\n\t" \
  "v_mfma_f32_16x16x32_bf16 v[136:139], a[208:211], a[224:227], v[136:139]\n\t" \
  "s_add_u32 s72, s72, %7\n\t" \
  "s_mul_i32 s48, s72, 192\n\t" \
  "v_mfma_f32_16x16x32_bf16 v[140:143], a[208:211], a[228:231], v[140:143]\n\t" \
  "v_add_u32 v17, 4096, v63\n\t" \
  "v_add_u32 v18, 297984, v63\n\t" \
  "v_add_u32 v19, 591872, v63\n\t" \
  "v_mfma_f32_16x16x32_bf16 v[144:147], a[212:215], a[216:219], v[144:147]\n\t" \
  "ds_read_b64_tr_b16 a[204:205], v25 offset:38912\n\t" \
  "ds_read_b64_tr_b16 a[206:207], v25 offset:39168\n\t" \
  "v_lshrrev_b32 v63, 2, v16\n\t" \
  "v_mfma_f32_16x16x32_bf16 v[148:151], a[212:215], a[220:223], v[148:151]\n\t" \
  "v_mul_u32_u24 v63, 1152, v63\n\t" \
  "v_lshrrev_b32 v61, 3, v16\n\t" \
  "v_and_b32 v61, 2, v61\n\t" \
  "v_xor_b32 v61, v61, v16\n\t" \
  "v_and_b32 v61, 3, v61\n\t" \
  "v_lshl_add_u32 v63, v61, 4, v63\n\t" \
  "s_and_b32 s57, %7, 1\n\t" \
  "s_mul_i32 s57, s57, 18432\n\t" \
  "s_lshr_b32 s58, %7, 1\n\t" \
  "s_lshl_b32 s58, s58, 6\n\t" \
  "s_add_u32 s57, s57, s58\n\t" \
  "v_mfma_f32_16x16x32_bf16 v[152:155], a[212:215], a[224:227], v[152:155]\n\t" \
  "v_add_u32 v63, s57, v63\n\t" \
  "s_lshl_b32 s75, %7, 1\n\t" \
  "v_mfma_f32_16x16x32_bf16 v[156:159], a[212:215], a[228:231], v[156:159]\n\t" \
  "s_sub_u32 s75, %5, s75\n\t" \
  "s_waitcnt lgkmcnt(4)\n\t" \
  "v_mfma_f32_16x16x32_bf16 v[160:163], a[192:195], a[216:219], v[160:163]\n\t" \
  "ds_read_b64_tr_b16 a[208:209], v24 offset:45056\n\t" \
  "ds_read_b64_tr_b16 a[210:211], v24 offset:45312\n\t" \
  "v_mfma_f32_16x16x32_bf16 v[164:167], a[192:195], a[220:223], v[164:167]\n\t" \
  "s_sub_u32 s76, s75, 2\n\t" \
  "s_sub_u32 s75, s75, 1\n\t" \
  "v_mfma_f32_16x16x32_bf16 v[168:171], a[192:195], a[224:227], v[168:171]\n\t" \
  "s_and_b32 s75, s75, 7\n\t" \
  "s_and_b32 s76, s76, 7\n\t" \
  "v_mfma_f32_16x16x32_bf16 v[172:175], a[192:195], a[228:231], v[172:175]\n\t" \
  "s_add_u32 s74, s48, s76\n\t" \
  "s_add_u32 s48, s48, s75\n\t" \
  "v_mfma_f32_16x16x32_bf16 v[176:179], a[196:199], a[216:219], v[176:179]\n\t" \
  "ds_read_b64_tr_b16 a[212:213], v25 offset:45056\n\t" \
  "ds_read_b64_tr_b16 a[214:215], v25 offset:45312\n\t" \
  "v_mfma_f32_16x16x32_bf16 v[180:183], a[196:199], a[220:223], v[180:183]\n\t" \
  "s_lshl_b32 s48, s48, 10\n\t" \
  "s_add_u32 s48, s48, 29360128\n\t" \
  "v_mfma_f32_16x16x32_bf16 v[184:187], a[196:199], a[224:227], v[184:187]\n\t" \
  "s_lshl_b32 s74, s74, 10\n\t" \
  "s_add_u32 s74, s74, 29458432\n\t" \
  "v_mfma_f32_16x16x32_bf16 v[188:191], a[196:199], a[228:231], v[188:191]\n\t" \
  "s_waitcnt lgkmcnt(4)\n\t" \
  "s_barrier\n\t" \
  "v_mfma_f32_16x16x32_bf16 v[192:195], a[200:203], a[216:219], v[192:195]\n\t" \
  "ds_read_b64_tr_b16 a[192:193], v24 offset:49152\n\t" \
  "ds_read_b64_tr_b16 a[194:195], v24 offset:49408\n\t" \
  "v_mfma_f32_16x16x32_bf16 v[196:199], a[200:203], a[220:223], v[196:199]\n\t" \
  "s_add_u32 m0, s40, 24576\n\t" \
  "s_add_u32 s43, s50, 3538944\n\t" \
  "v_mfma_f32_16x16x32_bf16 v[200:203], a[200:203], a[224:227], v[200:203]\n\t" \
  "buffer_load_dwordx4 v17, %1, s43 offen offset:0 lds\n\t" \
  "v_mfma_f32_16x16x32_bf16 v[204:207], a[200:203], a[228:231], v[204:207]\n\t" \
  "s_mul_i32 s65, s72, 24\n\t" \
  "s_add_u32 s65, s65, %5\n\t" \
  "v_mfma_f32_16x16x32_bf16 v[208:211], a[204:207], a[216:219], v[208:211]\n\t" \
  "ds_read_b64_tr_b16 a[196:197], v25 offset:49152\n\t" \
  "ds_read_b64_tr_b16 a[198:199], v25 offset:49408\n\t" \
  "v_mfma_f32_16x16x32_bf16 v[212:215], a[204:207], a[220:223], v[212:215]\n\t" \
  "buffer_load_dwordx4 v18, %1, s43 offen offset:1024 lds\n\t" \
  "v_mfma_f32_16x16x32_bf16 v[216:219], a[204:207], a[224:227], v[216:219]\n\t" \
  "s_lshl_b32 s65, s65, 6\n\t" \
  "s_add_u32 s65, s65, 163840\n\t" \
  "v_mfma_f32_16x16x32_bf16 v[220:223], a[204:207], a[228:231], v[220:223]\n\t" \
  "s_waitcnt lgkmcnt(4)\n\t" \
  "v_mfma_f32_16x16x32_bf16 v[224:227], a[208:211], a[216:219], v[224:227]\n\t" \
  "ds_read_b64_tr_b16 a[200:201], v24 offset:55296\n\t" \
  "ds_read_b64_tr_b16 a[202:203], v24 offset:55552\n\t" \
  "v_mfma_f32_16x16x32_bf16 v[228:231], a[208:211], a[220:223], v[228:231]\n\t" \
  "v_mfma_f32_16x16x32_bf16 v[232:235], a[208:211], a[224:227], v[232:235]\n\t" \
  "buffer_load_dwordx4 v19, %1, s43 offen offset:2048 lds\n\t" \
  "v_mfma_f32_16x16x32_bf16 v[236:239], a[208:211], a[228:231], v[236:239]\n\t" \
  "v_mfma_f32_16x16x32_bf16 v[240:243], a[212:215], a[216:219], v[240:243]\n\t" \
  "ds_read_b64_tr_b16 a[204:205], v25 offset:55296\n\t" \
  "ds_read_b64_tr_b16 a[206:207], v25 offset:55552\n\t" \
  "v_mfma_f32_16x16x32_bf16 v[244:247], a[212:215], a[220:223], v[244:247]\n\t" \
  "v_mfma_f32_16x16x32_bf16 v[248:251], a[212:215], a[224:227], v[248:251]\n\t" \
  "v_mfma_f32_16x16x32_bf16 v[252:255], a[212:215], a[228:231], v[252:255]\n\t" \
  "s_waitcnt lgkmcnt(4)\n\t" \
  "v_mfma_f32_16x16x32_bf16 v[64:67], a[192:195], a[232:235], v[64:67]\n\t" \
  "ds_read_b64_tr_b16 a[208:209], v24 offset:50176\n\t" \
  "ds_read_b64_tr_b16 a[210:211], v24 offset:50432\n\t" \
  "v_mfma_f32_16x16x32_bf16 v[68:71], a[192:195], a[236:239], v[68:71]\n\t" \
  "s_add_u32 m0, s41, 65536\n\t" \
  "s_add_u32 s44, s42, 896\n\t" \
  "v_mfma_f32_16x16x32_bf16 v[72:75], a[192:195], a[240:243], v[72:75]\n\t" \
  "buffer_load_dwordx4 v20, %0, s44 offen offset:0 lds\n\t" \
  "v_mfma_f32_16x16x32_bf16 v[76:79], a[192:195], a[244:247], v[76:79]\n\t" \
  "v_mfma_f32_16x16x32_bf16 v[80:83], a[196:199], a[232:235], v[80:83]\n\t" \
  "ds_read_b64_tr_b16 a[212:213], v25 offset:50176\n\t" \
  "ds_read_b64_tr_b16 a[214:215], v25 offset:50432\n\t" \
  "v_mfma_f32_16x16x32_bf16 v[84:87], a[196:199], a[236:239], v[84:87]\n\t" \
  "v_mfma_f32_16x16x32_bf16 v[88:91], a[196:199], a[240:243], v[88:91]\n\t" \
  "buffer_load_dwordx4 v21, %0, s44 offen offset:1024 lds\n\t" \
  "v_mfma_f32_16x16x32_bf16 v[92:95], a[196:199], a[244:247], v[92:95]\n\t" \
  "v_add_u32 v30, 4294901760, v30\n\t" \
  "s_waitcnt lgkmcnt(4)\n\t" \
  "v_mfma_f32_16x16x32_bf16 v[96:99], a[200:203], a[232:235], v[96:99]\n\t" \
  "ds_read_b64_tr_b16 a[192:193], v24 offset:56320\n\t" \
  "ds_read_b64_tr_b16 a[194:195], v24 offset:56576\n\t" \
  "v_mfma_f32_16x16x32_bf16 v[100:103], a[200:203], a[236:239], v[100:103]\n\t" \
  "ds_read_b128 a[216:219], v30 offset:0\n\t" \
  "ds_read_b128 a[220:223], v30 offset:2048\n\t" \
  "v_mfma_f32_16x16x32_bf16 v[104:107], a[200:203], a[240:243], v[104:107]\n\t" \
  "ds_read_b128 a[224:227], v30 offset:4096\n\t" \
  "ds_read_b128 a[228:231], v30 offset:6144\n\t" \
  "v_mfma_f32_16x16x32_bf16 v[108:111], a[200:203], a[244:247], v[108:111]\n\t" \
  "buffer_load_dwordx4 v22, %0, s44 offen offset:2048 lds\n\t" \
  "v_mfma_f32_16x16x32_bf16 v[112:115], a[204:207], a[232:235], v[112:115]\n\t" \
  "ds_read_b64_tr_b16 a[196:197], v25 offset:56320\n\t" \
  "ds_read_b64_tr_b16 a[198:199], v25 offset:56576\n\t" \
  "v_mfma_f32_16x16x32_bf16 v[116:119], a[204:207], a[236:239], v[116:119]\n\t" \
  "v_mfma_f32_16x16x32_bf16 v[120:123], a[204:207], a[240:243], v[120:123]\n\t" \
  "v_mfma_f32_16x16x32_bf16 v[124:127], a[204:207], a[244:247], v[124:127]\n\t" \
  "s_waitcnt lgkmcnt(8)\n\t" \
  "v_mfma_f32_16x16x32_bf16 v[128:131], a[208:211], a[232:235], v[128:131]\n\t" \
  "ds_read_b64_tr_b16 a[200:201], v24 offset:51200\n\t" \
  "ds_read_b64_tr_b16 a[202:203], v24 offset:51456\n\t" \
  "v_mfma_f32_16x16x32_bf16 v[132:135], a[208:211], a[236:239], v[132:135]\n\t" \
  "buffer_load_dwordx4 v23, %0, s44 offen offset:3072 lds\n\t" \
  "v_mfma_f32_16x16x32_bf16 v[136:139], a[208:211], a[240:243], v[136:139]\n\t" \
  "v_mfma_f32_16x16x32_bf16 v[140:143], a[208:211], a[244:247], v[140:143]\n\t" \
  "buffer_load_dwordx2 v[40:41], v58, %3, 0 offen offset:0 sc1\n\t" \
  "v_mfma_f32_16x16x32_bf16 v[144:147], a[212:215], a[232:235], v[144:147]\n\t" \
  "ds_read_b64_tr_b16 a[204:205], v25 offset:51200\n\t" \
  "ds_read_b64_tr_b16 a[206:207], v25 offset:51456\n\t" \
  "v_mfma_f32_16x16x32_bf16 v[148:151], a[212:215], a[236:239], v[148:151]\n\t" \
  "v_mfma_f32_16x16x32_bf16 v[152:155], a[212:215], a[240:243], v[152:155]\n\t" \
  "v_mfma_f32_16x16x32_bf16 v[156:159], a[212:215], a[244:247], v[156:159]\n\t" \
  "s_waitcnt lgkmcnt(4)\n\t" \
  "v_mfma_f32_16x16x32_bf16 v[160:163], a[192:195], a[232:235], v[160:163]\n\t" \
  "ds_read_b64_tr_b16 a[208:209], v24 offset:57344\n\t" \
  "ds_read_b64_tr_b16 a[210:211], v24 offset:57600\n\t" \
  "v_mfma_f32_16x16x32_bf16 v[164:167], a[192:195], a[236:239], v[164:167]\n\t" \
  "v_lshrrev_b32 v33, 4, v16\n\t" \
  "v_bfe_u32 v61, v16, 2, 2\n\t" \
  "v_mfma_f32_16x16x32_bf16 v[168:171], a[192:195], a[240:243], v[168:171]\n\t" \
  "v_lshlrev_b32 v61, 6, v61\n\t" \
  "v_and_b32 v27, 3, v16\n\t" \
  "v_mfma_f32_16x16x32_bf16 v[172:175], a[192:195], a[244:247], v[172:175]\n\t" \
  "v_lshl_add_u32 v61, v27, 3, v61\n\t" \
  "v_lshl_add_u32 v61, v33, 8, v61\n\t" \
  "v_mfma_f32_16x16x32_bf16 v[176:179], a[196:199], a[232:235], v[176:179]\n\t" \
  "ds_read_b64_tr_b16 a[212:213], v25 offset:57344\n\t" \
  "ds_read_b64_tr_b16 a[214:215], v25 offset:57600\n\t" \
  "v_mfma_f32_16x16x32_bf16 v[180:183], a[196:199], a[236:239], v[180:183]\n\t" \
  "v_and_b32 v33, 1, v33\n\t" \
  "v_lshl_add_u32 v61, v33, 5, v61\n\t" \
  "v_mfma_f32_16x16x32_bf16 v[184:187], a[196:199], a[240:243], v[184:187]\n\t" \
  "v_add_u32 v26, 61440, v61\n\t" \
  "v_add_u32 v28, 122880, v61\n\t" \
  "v_mfma_f32_16x16x32_bf16 v[188:191], a[196:199], a[244:247], v[188:191]\n\t" \
  "s_waitcnt vmcnt(21) lgkmcnt(4)\n\t" \
  "s_barrier\n\t" \
  "v_mfma_f32_16x16x32_bf16 v[192:195], a[200:203], a[232:235], v[192:195]\n\t" \
  "ds_read_b64_tr_b16 a[192:193], v24 offset:0\n\t" \
  "ds_read_b64_tr_b16 a[194:195], v24 offset:256\n\t" \
  "v_mfma_f32_16x16x32_bf16 v[196:199], a[200:203], a[236:239], v[196:199]\n\t" \
  "s_add_u32 m0, s40, 36864\n\t" \
  "s_add_u32 s43, s50, 4423680\n\t" \
  "v_mfma_f32_16x16x32_bf16 v[200:203], a[200:203], a[240:243], v[200:203]\n\t" \
  "buffer_load_dwordx4 v17, %1, s43 offen offset:0 lds\n\t" \
  "v_mfma_f32_16x16x32_bf16 v[204:207], a[200:203], a[244:247], v[204:207]\n\t" \
  "v_xor_b32 v27, 32, v26\n\t" \
  "v_xor_b32 v29, 32, v28\n\t" \
  "v_mfma_f32_16x16x32_bf16 v[208:211], a[204:207], a[232:235], v[208:211]\n\t" \
  "ds_read_b64_tr_b16 a[196:197], v25 offset:0\n\t" \
  "ds_read_b64_tr_b16 a[198:199], v25 offset:256\n\t" \
  "v_mfma_f32_16x16x32_bf16 v[212:215], a[204:207], a[236:239], v[212:215]\n\t" \
  "buffer_load_dwordx4 v18, %1, s43 offen offset:1024 lds\n\t" \
  "v_mfma_f32_16x16x32_bf16 v[216:219], a[204:207], a[240:243], v[216:219]\n\t" \
  "v_mfma_f32_16x16x32_bf16 v[220:223], a[204:207], a[244:247], v[220:223]\n\t" \
  "s_waitcnt lgkmcnt(4)\n\t" \
  "v_mfma_f32_16x16x32_bf16 v[224:227], a[208:211], a[232:235], v[224:227]\n\t" \
  "ds_read_b64_tr_b16 a[200:201], v24 offset:6144\n\t" \
  "ds_read_b64_tr_b16 a[202:203], v24 offset:6400\n\t" \
  "v_mfma_f32_16x16x32_bf16 v[228:231], a[208:211], a[236:239], v[228:231]\n\t" \
  "v_mfma_f32_16x16x32_bf16 v[232:235], a[208:211], a[240:243], v[232:235]\n\t" \
  "buffer_load_dwordx4 v19, %1, s43 offen offset:2048 lds\n\t" \
  "v_mfma_f32_16x16x32_bf16 v[236:239], a[208:211], a[244:247], v[236:239]\n\t" \
  "v_mfma_f32_16x16x32_bf16 v[240:243], a[212:215], a[232:235], v[240:243]\n\t" \
  "ds_read_b64_tr_b16 a[204:205], v25 offset:6144\n\t" \
  "ds_read_b64_tr_b16 a[206:207], v25 offset:6400\n\t" \
  "v_mfma_f32_16x16x32_bf16 v[244:247], a[212:215], a[236:239], v[244:247]\n\t" \
  "v_mfma_f32_16x16x32_bf16 v[248:251], a[212:215], a[240:243], v[248:251]\n\t" \
  "v_mfma_f32_16x16x32_bf16 v[252:255], a[212:215], a[244:247], v[252:255]\n\t" \
  "s_add_u32 m0, s41, 69632\n\t" \
  "s_waitcnt lgkmcnt(4)\n\t" \
  "v_mfma_f32_16x16x32_bf16 v[64:67], a[192:195], a[216:219], v[64:67]\n\t" \
  "ds_read_b64_tr_b16 a[208:209], v24 offset:1024\n\t" \
  "ds_read_b64_tr_b16 a[210:211], v24 offset:1280\n\t" \
  "v_mfma_f32_16x16x32_bf16 v[68:71], a[192:195], a[220:223], v[68:71]\n\t" \
  "s_add_u32 s44, s42, 37760\n\t" \
  "v_mfma_f32_16x16x32_bf16 v[72:75], a[192:195], a[224:227], v[72:75]\n\t" \
  "buffer_load_dwordx4 v20, %0, s44 offen offset:0 lds\n\t" \
  "v_mfma_f32_16x16x32_bf16 v[76:79], a[192:195], a[228:231], v[76:79]\n\t" \
  "v_mfma_f32_16x16x32_bf16 v[80:83], a[196:199], a[216:219], v[80:83]\n\t" \
  "ds_read_b64_tr_b16 a[212:213], v25 offset:1024\n\t" \
  "ds_read_b64_tr_b16 a[214:215], v25 offset:1280\n\t" \
  "v_mfma_f32_16x16x32_bf16 v[84:87], a[196:199], a[220:223], v[84:87]\n\t" \
  "buffer_load_dwordx4 v21, %0, s44 offen offset:1024 lds\n\t" \
  "v_mfma_f32_16x16x32_bf16 v[88:91], a[196:199], a[224:227], v[88:91]\n\t" \
  "v_add_u32 v31, 4294901760, v31\n\t" \
  "ds_read_b128 a[232:235], v31 offset:0\n\t" \
  "v_mfma_f32_16x16x32_bf16 v[92:95], a[196:199], a[228:231], v[92:95]\n\t" \
  "ds_read_b128 a[236:239], v31 offset:2048\n\t" \
  "s_waitcnt lgkmcnt(6)\n\t" \
  "v_mfma_f32_16x16x32_bf16 v[96:99], a[200:203], a[216:219], v[96:99]\n\t" \
  "ds_read_b64_tr_b16 a[192:193], v24 offset:7168\n\t" \
  "ds_read_b64_tr_b16 a[194:195], v24 offset:7424\n\t" \
  "v_mfma_f32_16x16x32_bf16 v[100:103], a[200:203], a[220:223], v[100:103]\n\t" \
  "buffer_load_dwordx4 v22, %0, s44 offen offset:2048 lds\n\t" \
  "v_mfma_f32_16x16x32_bf16 v[104:107], a[200:203], a[224:227], v[104:107]\n\t" \
  "ds_read_b128 a[240:243], v31 offset:4096\n\t" \
  "ds_read_b128 a[244:247], v31 offset:6144\n\t" \
  "v_mfma_f32_16x16x32_bf16 v[108:111], a[200:203], a[228:231], v[108:111]\n\t" \
  "v_mfma_f32_16x16x32_bf16 v[112:115], a[204:207], a[216:219], v[112:115]\n\t" \
  "ds_read_b64_tr_b16 a[196:197], v25 offset:7168\n\t" \
  "ds_read_b64_tr_b16 a[198:199], v25 offset:7424\n\t" \
  "v_mfma_f32_16x16x32_bf16 v[116:119], a[204:207], a[220:223], v[116:119]\n\t" \
  "v_mfma_f32_16x16x32_bf16 v[120:123], a[204:207], a[224:227], v[120:123]\n\t" \
  "buffer_load_dwordx4 v23, %0, s44 offen offset:3072 lds\n\t" \
  "v_mfma_f32_16x16x32_bf16 v[124:127], a[204:207], a[228:231], v[124:127]\n\t" \
  "s_waitcnt lgkmcnt(8)\n\t" \
  "v_mfma_f32_16x16x32_bf16 v[128:131], a[208:211], a[216:219], v[128:131]\n\t" \
  "ds_read_b64_tr_b16 a[200:201], v24 offset:2048\n\t" \
  "ds_read_b64_tr_b16 a[202:203], v24 offset:2304\n\t" \
  "s_waitcnt vmcnt(18)\n\t" \
  "v_mfma_f32_16x16x32_bf16 v[132:135], a[208:211], a[220:223], v[132:135]\n\t" \
  "v_cmp_eq_u64 vcc, v[36:37], v[38:39]\n\t" \
  "s_nop 1\n\t" \
  "v_mfma_f32_16x16x32_bf16 v[136:139], a[208:211], a[224:227], v[136:139]\n\t" \
  "s_cmp_eq_u64 vcc, -1\n\t" \
  "s_cbranch_scc0 L_slow1_%=\n\t" \
  "L_back1_%=:\n\t" \
  "v_mfma_f32_16x16x32_bf16 v[140:143], a[208:211], a[228:231], v[140:143]\n\t" \
  "s_mul_i32 s54, %7, 36864\n\t" \
  "s_add_u32 s54, s54, s63\n\t" \
  "v_mfma_f32_16x16x32_bf16 v[144:147], a[212:215], a[216:219], v[144:147]\n\t" \
  "ds_read_b64_tr_b16 a[204:205], v25 offset:2048\n\t" \
  "ds_read_b64_tr_b16 a[206:207], v25 offset:2304\n\t" \
  "v_mfma_f32_16x16x32_bf16 v[148:151], a[212:215], a[220:223], v[148:151]\n\t" \
  "buffer_load_dwordx4 v[44:47], v32, %3, s54 offen sc1\n\t" \
  "v_mfma_f32_16x16x32_bf16 v[152:155], a[212:215], a[224:227], v[152:155]\n\t" \
  "s_add_u32 s54, s54, 18432\n\t" \
  "v_mfma_f32_16x16x32_bf16 v[156:159], a[212:215], a[228:231], v[156:159]\n\t" \
  "s_waitcnt lgkmcnt(4)\n\t" \
  "v_mfma_f32_16x16x32_bf16 v[160:163], a[192:195], a[216:219], v[160:163]\n\t" \
  "ds_read_b64_tr_b16 a[208:209], v24 offset:8192\n\t" \
  "ds_read_b64_tr_b16 a[210:211], v24 offset:8448\n\t" \
  "v_mfma_f32_16x16x32_bf16 v[164:167], a[192:195], a[220:223], v[164:167]\n\t" \
  "buffer_load_dwordx4 v[48:51], v32, %3, s54 offen sc1\n\t" \
  "v_mfma_f32_16x16x32_bf16 v[168:171], a[192:195], a[224:227], v[168:171]\n\t" \
  "v_mfma_f32_16x16x32_bf16 v[172:175], a[192:195], a[228:231], v[172:175]\n\t" \
  "v_mfma_f32_16x16x32_bf16 v[176:179], a[196:199], a[216:219], v[176:179]\n\t" \
  "ds_read_b64_tr_b16 a[212:213], v25 offset:8192\n\t" \
  "ds_read_b64_tr_b16 a[214:215], v25 offset:8448\n\t" \
  "v_mfma_f32_16x16x32_bf16 v[180:183], a[196:199], a[220:223], v[180:183]\n\t" \
  "v_mfma_f32_16x16x32_bf16 v[184:187], a[196:199], a[224:227], v[184:187]\n\t" \
  "v_mfma_f32_16x16x32_bf16 v[188:191], a[196:199], a[228:231], v[188:191]\n\t" \
  "s_waitcnt lgkmcnt(4)\n\t" \
  "s_barrier\n\t" \
  "v_mfma_f32_16x16x32_bf16 v[192:195], a[200:203], a[216:219], v[192:195]\n\t" \
  "ds_read_b64_tr_b16 a[192:193], v24 offset:12288\n\t" \
  "ds_read_b64_tr_b16 a[194:195], v24 offset:12544\n\t" \
  "v_mfma_f32_16x16x32_bf16 v[196:199], a[200:203], a[220:223], v[196:199]\n\t" \
  "s_add_u32 m0, s40, 49152\n\t" \
  "s_add_u32 s43, s50, 3539072\n\t" \
  "v_mfma_f32_16x16x32_bf16 v[200:203], a[200:203], a[224:227], v[200:203]\n\t" \
  "buffer_load_dwordx4 v17, %1, s43 offen offset:0 lds\n\t" \
  "v_mfma_f32_16x16x32_bf16 v[204:207], a[200:203], a[228:231], v[204:207]\n\t" \
  "v_mfma_f32_16x16x32_bf16 v[208:211], a[204:207], a[216:219], v[208:211]\n\t" \
  "ds_read_b64_tr_b16 a[196:197], v25 offset:12288\n\t" \
  "ds_read_b64_tr_b16 a[198:199], v25 offset:12544\n\t" \
  "v_mfma_f32_16x16x32_bf16 v[212:215], a[204:207], a[220:223], v[212:215]\n\t" \
  "buffer_load_dwordx4 v18, %1, s43 offen offset:1024 lds\n\t" \
  "v_mfma_f32_16x16x32_bf16 v[216:219], a[204:207], a[224:227], v[216:219]\n\t" \
  "v_mfma_f32_16x16x32_bf16 v[220:223], a[204:207], a[228:231], v[220:223]\n\t" \
  "s_waitcnt lgkmcnt(4)\n\t" \
  "v_mfma_f32_16x16x32_bf16 v[224:227], a[208:211], a[216:219], v[224:227]\n\t" \
  "ds_read_b64_tr_b16 a[200:201], v24 offset:18432\n\t" \
  "ds_read_b64_tr_b16 a[202:203], v24 offset:18688\n\t" \
  "v_mfma_f32_16x16x32_bf16 v[228:231], a[208:211], a[220:223], v[228:231]\n\t" \
  "v_mfma_f32_16x16x32_bf16 v[232:235], a[208:211], a[224:227], v[232:235]\n\t" \
  "buffer_load_dwordx4 v19, %1, s43 offen offset:2048 lds\n\t" \
  "v_mfma_f32_16x16x32_bf16 v[236:239], a[208:211], a[228:231], v[236:239]\n\t" \
  "v_mfma_f32_16x16x32_bf16 v[240:243], a[212:215], a[216:219], v[240:243]\n\t" \
  "ds_read_b64_tr_b16 a[204:205], v25 offset:18432\n\t" \
  "ds_read_b64_tr_b16 a[206:207], v25 offset:18688\n\t" \
  "v_mfma_f32_16x16x32_bf16 v[244:247], a[212:215], a[220:223], v[244:247]\n\t" \
  "v_mfma_f32_16x16x32_bf16 v[248:251], a[212:215], a[224:227], v[248:251]\n\t" \
  "v_mfma_f32_16x16x32_bf16 v[252:255], a[212:215], a[228:231], v[252:255]\n\t" \
  "s_waitcnt lgkmcnt(4)\n\t" \
  "v_mfma_f32_16x16x32_bf16 v[64:67], a[192:195], a[232:235], v[64:67]\n\t" \
  "ds_read_b64_tr_b16 a[208:209], v24 offset:13312\n\t" \
  "ds_read_b64_tr_b16 a[210:211], v24 offset:13568\n\t" \
  "v_mfma_f32_16x16x32_bf16 v[68:71], a[192:195], a[236:239], v[68:71]\n\t" \
  "s_add_u32 m0, s41, 0\n\t" \
  "s_add_u32 s44, s42, 1024\n\t" \
  "v_mfma_f32_16x16x32_bf16 v[72:75], a[192:195], a[240:243], v[72:75]\n\t" \
  "buffer_load_dwordx4 v20, %0, s44 offen offset:0 lds\n\t" \
  "v_mfma_f32_16x16x32_bf16 v[76:79], a[192:195], a[244:247], v[76:79]\n\t" \
  "v_add_u32 v30, 32768, v30\n\t" \
  "ds_read_b128 a[0:3], v30 offset:0\n\t" \
  "v_mfma_f32_16x16x32_bf16 v[80:83], a[196:199], a[232:235], v[80:83]\n\t" \
  "ds_read_b64_tr_b16 a[212:213], v25 offset:13312\n\t" \
  "ds_read_b64_tr_b16 a[214:215], v25 offset:13568\n\t" \
  "v_mfma_f32_16x16x32_bf16 v[84:87], a[196:199], a[236:239], v[84:87]\n\t" \
  "ds_read_b128 a[4:7], v30 offset:2048\n\t" \
  "s_waitcnt vmcnt(21)\n\t" \
  "v_mfma_f32_16x16x32_bf16 v[88:91], a[196:199], a[240:243], v[88:91]\n\t" \
  "buffer_load_dwordx4 v21, %0, s44 offen offset:1024 lds\n\t" \
  "v_mfma_f32_16x16x32_bf16 v[92:95], a[196:199], a[244:247], v[92:95]\n\t" \
  "ds_read_b128 a[8:11], v30 offset:4096\n\t" \
  "s_waitcnt lgkmcnt(7)\n\t" \
  "v_mfma_f32_16x16x32_bf16 v[96:99], a[200:203], a[232:235], v[96:99]\n\t" \
  "ds_read_b64_tr_b16 a[192:193], v24 offset:19456\n\t" \
  "ds_read_b64_tr_b16 a[194:195], v24 offset:19712\n\t" \
  "v_mfma_f32_16x16x32_bf16 v[100:103], a[200:203], a[236:239], v[100:103]\n\t" \
  "ds_read_b128 a[12:15], v30 offset:6144\n\t" \
  "v_mfma_f32_16x16x32_bf16 v[104:107], a[200:203], a[240:243], v[104:107]\n\t" \
  "v_mfma_f32_16x16x32_bf16 v[108:111], a[200:203], a[244:247], v[108:111]\n\t" \
  "buffer_load_dwordx4 v22, %0, s44 offen offset:2048 lds\n\t" \
  "v_mfma_f32_16x16x32_bf16 v[112:115], a[204:207], a[232:235], v[112:115]\n\t" \
  "ds_read_b64_tr_b16 a[196:197], v25 offset:19456\n\t" \
  "ds_read_b64_tr_b16 a[198:199], v25 offset:19712\n\t" \
  "v_mfma_f32_16x16x32_bf16 v[116:119], a[204:207], a[236:239], v[116:119]\n\t" \
  "v_mfma_f32_16x16x32_bf16 v[120:123], a[204:207], a[240:243], v[120:123]\n\t" \
  "v_mfma_f32_16x16x32_bf16 v[124:127], a[204:207], a[244:247], v[124:127]\n\t" \
  "s_waitcnt lgkmcnt(7)\n\t" \
  "v_mfma_f32_16x16x32_bf16 v[128:131], a[208:211], a[232:235], v[128:131]\n\t" \
  "ds_read_b64_tr_b16 a[200:201], v24 offset:14336\n\t" \
  "ds_read_b64_tr_b16 a[202:203], v24 offset:14592\n\t" \
  "v_mfma_f32_16x16x32_bf16 v[132:135], a[208:211], a[236:239], v[132:135]\n\t" \
  "buffer_load_dwordx4 v23, %0, s44 offen offset:3072 lds\n\t" \
  "v_mfma_f32_16x16x32_bf16 v[136:139], a[208:211], a[240:243], v[136:139]\n\t" \
  "v_add_u32 v31, 32768, v31\n\t" \
  "ds_read_b128 a[16:19], v31 offset:0\n\t" \
  "v_mfma_f32_16x16x32_bf16 v[140:143], a[208:211], a[244:247], v[140:143]\n\t" \
  "ds_read_b128 a[20:23], v31 offset:2048\n\t" \
  "v_mfma_f32_16x16x32_bf16 v[144:147], a[212:215], a[232:235], v[144:147]\n\t" \
  "ds_read_b64_tr_b16 a[204:205], v25 offset:14336\n\t" \
  "ds_read_b64_tr_b16 a[206:207], v25 offset:14592\n\t" \
  "v_mfma_f32_16x16x32_bf16 v[148:151], a[212:215], a[236:239], v[148:151]\n\t" \
  "s_add_u32 m0, s41, 4096\n\t" \
  "s_add_u32 s44, s42, 37888\n\t" \
  "v_mfma_f32_16x16x32_bf16 v[152:155], a[212:215], a[240:243], v[152:155]\n\t" \
  "buffer_load_dwordx4 v20, %0, s44 offen offset:0 lds\n\t" \
  "v_mfma_f32_16x16x32_bf16 v[156:159], a[212:215], a[244:247], v[156:159]\n\t" \
  "s_waitcnt lgkmcnt(6)\n\t" \
  "v_mfma_f32_16x16x32_bf16 v[160:163], a[192:195], a[232:235], v[160:163]\n\t" \
  "ds_read_b64_tr_b16 a[208:209], v24 offset:20480\n\t" \
  "ds_read_b64_tr_b16 a[210:211], v24 offset:20736\n\t" \
  "v_mfma_f32_16x16x32_bf16 v[164:167], a[192:195], a[236:239], v[164:167]\n\t" \
  "ds_read_b128 a[24:27], v31 offset:4096\n\t" \
  "ds_read_b128 a[28:31], v31 offset:6144\n\t" \
  "v_mfma_f32_16x16x32_bf16 v[168:171], a[192:195], a[240:243], v[168:171]\n\t" \
  "buffer_load_dwordx4 v21, %0, s44 offen offset:1024 lds\n\t" \
  "v_mfma_f32_16x16x32_bf16 v[172:175], a[192:195], a[244:247], v[172:175]\n\t" \
  "v_mfma_f32_16x16x32_bf16 v[176:179], a[196:199], a[232:235], v[176:179]\n\t" \
  "ds_read_b64_tr_b16 a[212:213], v25 offset:20480\n\t" \
  "ds_read_b64_tr_b16 a[214:215], v25 offset:20736\n\t" \
  "v_mfma_f32_16x16x32_bf16 v[180:183], a[196:199], a[236:239], v[180:183]\n\t" \
  "v_mfma_f32_16x16x32_bf16 v[184:187], a[196:199], a[240:243], v[184:187]\n\t" \
  "s_waitcnt vmcnt(18)\n\t" \
  "v_cmp_eq_u64 vcc, v[36:37], v[40:41]\n\t" \
  "v_mfma_f32_16x16x32_bf16 v[188:191], a[196:199], a[244:247], v[188:191]\n\t" \
  "s_waitcnt lgkmcnt(6)\n\t" \
  "s_barrier\n\t" \
  "v_mfma_f32_16x16x32_bf16 v[192:195], a[200:203], a[232:235], v[192:195]\n\t" \
  "ds_read_b64_tr_b16 a[192:193], v24 offset:24576\n\t" \
  "ds_read_b64_tr_b16 a[194:195], v24 offset:24832\n\t" \
  "v_mfma_f32_16x16x32_bf16 v[196:199], a[200:203], a[236:239], v[196:199]\n\t" \
  "buffer_load_dwordx4 v22, %0, s44 offen offset:2048 lds\n\t" \
  "v_mfma_f32_16x16x32_bf16 v[200:203], a[200:203], a[240:243], v[200:203]\n\t" \
  "s_nop 1\n\t" \
  "s_cmp_eq_u64 vcc, -1\n\t" \
  "s_cbranch_scc0 L_slow0_%=\n\t" \
  "L_back0_%=:\n\t" \
  "v_mfma_f32_16x16x32_bf16 v[204:207], a[200:203], a[244:247], v[204:207]\n\t" \
  "buffer_load_dword v40, v60, %3, s62 offen offset:0 sc1\n\t" \
  "v_mfma_f32_16x16x32_bf16 v[208:211], a[204:207], a[232:235], v[208:211]\n\t" \
  "ds_read_b64_tr_b16 a[196:197], v25 offset:24576\n\t" \
  "ds_read_b64_tr_b16 a[198:199], v25 offset:24832\n\t" \
  "v_mfma_f32_16x16x32_bf16 v[212:215], a[204:207], a[236:239], v[212:215]\n\t" \
  "buffer_load_dwordx4 v23, %0, s44 offen offset:3072 lds\n\t" \
  "v_mfma_f32_16x16x32_bf16 v[216:219], a[204:207], a[240:243], v[216:219]\n\t" \
  "v_mfma_f32_16x16x32_bf16 v[220:223], a[204:207], a[244:247], v[220:223]\n\t" \
  "s_waitcnt lgkmcnt(4)\n\t" \
  "v_mfma_f32_16x16x32_bf16 v[224:227], a[208:211], a[232:235], v[224:227]\n\t" \
  "ds_read_b64_tr_b16 a[200:201], v24 offset:30720\n\t" \
  "ds_read_b64_tr_b16 a[202:203], v24 offset:30976\n\t" \
  "v_mfma_f32_16x16x32_bf16 v[228:231], a[208:211], a[236:239], v[228:231]\n\t" \
  "s_add_u32 m0, s40, 0\n\t" \
  "s_add_u32 s43, s50, 4423808\n\t" \
  "v_mfma_f32_16x16x32_bf16 v[232:235], a[208:211], a[240:243], v[232:235]\n\t" \
  "buffer_load_dwordx4 v17, %1, s43 offen offset:0 lds\n\t" \
  "v_mfma_f32_16x16x32_bf16 v[236:239], a[208:211], a[244:247], v[236:239]\n\t" \
  "buffer_load_dword v41, v60, %3, s62 offen offset:128 sc1\n\t" \
  "v_mfma_f32_16x16x32_bf16 v[240:243], a[212:215], a[232:235], v[240:243]\n\t" \
  "ds_read_b64_tr_b16 a[204:205], v25 offset:30720\n\t" \
  "ds_read_b64_tr_b16 a[206:207], v25 offset:30976\n\t" \
  "v_mfma_f32_16x16x32_bf16 v[244:247], a[212:215], a[236:239], v[244:247]\n\t" \
  "buffer_load_dword v42, v60, %3, s62 offen offset:256 sc1\n\t" \
  "v_mfma_f32_16x16x32_bf16 v[248:251], a[212:215], a[240:243], v[248:251]\n\t" \
  "buffer_load_dword v43, v60, %3, s62 offen offset:384 sc1\n\t" \
  "v_mfma_f32_16x16x32_bf16 v[252:255], a[212:215], a[244:247], v[252:255]\n\t" \
  "v_add_u32 v30, 32768, v30\n\t" \
  "s_waitcnt lgkmcnt(4)\n\t" \
  "v_mfma_f32_16x16x32_bf16 v[64:67], a[192:195], a[0:3], v[64:67]\n\t" \
  "ds_read_b64_tr_b16 a[208:209], v24 offset:25600\n\t" \
  "ds_read_b64_tr_b16 a[210:211], v24 offset:25856\n\t" \
  "v_mfma_f32_16x16x32_bf16 v[68:71], a[192:195], a[4:7], v[68:71]\n\t" \
  "buffer_load_dwordx4 v18, %1, s43 offen offset:1024 lds\n\t" \
  "v_mfma_f32_16x16x32_bf16 v[72:75], a[192:195], a[8:11], v[72:75]\n\t" \
  "ds_read_b128 a[32:35], v30 offset:0\n\t" \
  "ds_read_b128 a[36:39], v30 offset:2048\n\t" \
  "s_waitcnt vmcnt(19)\n\t" \
  "v_mfma_f32_16x16x32_bf16 v[76:79], a[192:195], a[12:15], v[76:79]\n\t" \
  "buffer_load_dwordx4 v19, %1, s43 offen offset:2048 lds\n\t" \
  "v_mfma_f32_16x16x32_bf16 v[80:83], a[196:199], a[0:3], v[80:83]\n\t" \
  "ds_read_b64_tr_b16 a[212:213], v25 offset:25600\n\t" \
  "ds_read_b64_tr_b16 a[214:215], v25 offset:25856\n\t" \
  "v_mfma_f32_16x16x32_bf16 v[84:87], a[196:199], a[4:7], v[84:87]\n\t" \
  "ds_read_b128 a[40:43], v30 offset:4096\n\t" \
  "v_mfma_f32_16x16x32_bf16 v[88:91], a[196:199], a[8:11], v[88:91]\n\t" \
  "v_mfma_f32_16x16x32_bf16 v[92:95], a[196:199], a[12:15], v[92:95]\n\t" \
  "s_waitcnt lgkmcnt(7)\n\t" \
  "v_mfma_f32_16x16x32_bf16 v[96:99], a[200:203], a[0:3], v[96:99]\n\t" \
  "ds_read_b64_tr_b16 a[192:193], v24 offset:31744\n\t" \
  "ds_read_b64_tr_b16 a[194:195], v24 offset:32000\n\t" \
  "v_mfma_f32_16x16x32_bf16 v[100:103], a[200:203], a[4:7], v[100:103]\n\t" \
  "ds_read_b128 a[44:47], v30 offset:6144\n\t" \
  "v_mfma_f32_16x16x32_bf16 v[104:107], a[200:203], a[8:11], v[104:107]\n\t" \
  "v_add_u32 v31, 32768, v31\n\t" \
  "ds_read_b128 a[48:51], v31 offset:0\n\t" \
  "v_mfma_f32_16x16x32_bf16 v[108:111], a[200:203], a[12:15], v[108:111]\n\t" \
  "v_mfma_f32_16x16x32_bf16 v[112:115], a[204:207], a[0:3], v[112:115]\n\t" \
  "ds_read_b64_tr_b16 a[196:197], v25 offset:31744\n\t" \
  "ds_read_b64_tr_b16 a[198:199], v25 offset:32000\n\t" \
  "v_mfma_f32_16x16x32_bf16 v[116:119], a[204:207], a[4:7], v[116:119]\n\t" \
  "v_mfma_f32_16x16x32_bf16 v[120:123], a[204:207], a[8:11], v[120:123]\n\t" \
  "v_mfma_f32_16x16x32_bf16 v[124:127], a[204:207], a[12:15], v[124:127]\n\t" \
  "s_waitcnt lgkmcnt(7)\n\t" \
  "v_mfma_f32_16x16x32_bf16 v[64:67], a[208:211], a[16:19], v[64:67]\n\t" \
  "ds_read_b64_tr_b16 a[200:201], v24 offset:26624\n\t" \
  "ds_read_b64_tr_b16 a[202:203], v24 offset:26880\n\t" \
  "s_waitcnt vmcnt(18)\n\t" \
  "v_mfma_f32_16x16x32_bf16 v[68:71], a[208:211], a[20:23], v[68:71]\n\t" \
  "v_add_f32 v44, v44, v48\n\t" \
  "v_add_f32 v45, v45, v49\n\t" \
  "v_mfma_f32_16x16x32_bf16 v[72:75], a[208:211], a[24:27], v[72:75]\n\t" \
  "v_add_f32 v46, v46, v50\n\t" \
  "v_add_f32 v47, v47, v51\n\t" \
  "v_mfma_f32_16x16x32_bf16 v[76:79], a[208:211], a[28:31], v[76:79]\n\t" \
  "ds_write_b128 v62, v[44:47]\n\t" \
  "v_add_u32 v56, 159744, v32\n\t" \
  "s_waitcnt lgkmcnt(0)\n\t" \
  "v_mfma_f32_16x16x32_bf16 v[80:83], a[212:215], a[16:19], v[80:83]\n\t" \
  "ds_read_b64_tr_b16 a[204:205], v25 offset:26624\n\t" \
  "ds_read_b64_tr_b16 a[206:207], v25 offset:26880\n\t" \
  "v_mfma_f32_16x16x32_bf16 v[84:87], a[212:215], a[20:23], v[84:87]\n\t" \
  "ds_read_b128 a[52:55], v31 offset:2048\n\t" \
  "ds_read_b128 a[56:59], v31 offset:4096\n\t" \
  "v_mfma_f32_16x16x32_bf16 v[88:91], a[212:215], a[24:27], v[88:91]\n\t" \
  "ds_read_b128 a[60:63], v31 offset:6144\n\t" \
  "v_mfma_f32_16x16x32_bf16 v[92:95], a[212:215], a[28:31], v[92:95]\n\t" \
  "v_mfma_f32_16x16x32_bf16 v[96:99], a[192:195], a[16:19], v[96:99]\n\t" \
  "ds_read_b64_tr_b16 a[208:209], v24 offset:32768\n\t" \
  "ds_read_b64_tr_b16 a[210:211], v24 offset:33024\n\t" \
  "v_mfma_f32_16x16x32_bf16 v[100:103], a[192:195], a[20:23], v[100:103]\n\t" \
  "v_mfma_f32_16x16x32_bf16 v[104:107], a[192:195], a[24:27], v[104:107]\n\t" \
  "v_mfma_f32_16x16x32_bf16 v[108:111], a[192:195], a[28:31], v[108:111]\n\t" \
  "v_mfma_f32_16x16x32_bf16 v[112:115], a[196:199], a[16:19], v[112:115]\n\t" \
  "ds_read_b64_tr_b16 a[212:213], v25 offset:32768\n\t" \
  "ds_read_b64_tr_b16 a[214:215], v25 offset:33024\n\t" \
  "v_mfma_f32_16x16x32_bf16 v[116:119], a[196:199], a[20:23], v[116:119]\n\t" \
  "v_mfma_f32_16x16x32_bf16 v[120:123], a[196:199], a[24:27], v[120:123]\n\t" \
  "v_mfma_f32_16x16x32_bf16 v[124:127], a[196:199], a[28:31], v[124:127]\n\t" \
  "s_waitcnt lgkmcnt(7)\n\t" \
  "s_barrier\n\t" \
  "v_mfma_f32_16x16x32_bf16 v[64:67], a[200:203], a[32:35], v[64:67]\n\t" \
  "ds_read_b64_tr_b16 a[192:193], v24 offset:36864\n\t" \
  "ds_read_b64_tr_b16 a[194:195], v24 offset:37120\n\t" \
  "v_mfma_f32_16x16x32_bf16 v[68:71], a[200:203], a[36:39], v[68:71]\n\t" \
  "s_add_u32 m0, s40, 12288\n\t" \
  "s_add_u32 s43, s50, 3539200\n\t" \
  "v_mfma_f32_16x16x32_bf16 v[72:75], a[200:203], a[40:43], v[72:75]\n\t" \
  "buffer_load_dwordx4 v17, %1, s43 offen offset:0 lds\n\t" \
  "v_mfma_f32_16x16x32_bf16 v[76:79], a[200:203], a[44:47], v[76:79]\n\t" \
  "ds_read_b128 v[44:47], v56 offset:0\n\t" \
  "ds_read_b128 v[48:51], v56 offset:1024\n\t" \
  "v_mfma_f32_16x16x32_bf16 v[80:83], a[204:207], a[32:35], v[80:83]\n\t" \
  "ds_read_b64_tr_b16 a[196:197], v25 offset:36864\n\t" \
  "ds_read_b64_tr_b16 a[198:199], v25 offset:37120\n\t" \
  "v_mfma_f32_16x16x32_bf16 v[84:87], a[204:207], a[36:39], v[84:87]\n\t" \
  "buffer_load_dwordx4 v18, %1, s43 offen offset:1024 lds\n\t" \
  "v_mfma_f32_16x16x32_bf16 v[88:91], a[204:207], a[40:43], v[88:91]\n\t" \
  "v_add_u32 v30, 4294901760, v30\n\t" \
  "s_waitcnt vmcnt(13)\n\t" \
  "v_mfma_f32_16x16x32_bf16 v[92:95], a[204:207], a[44:47], v[92:95]\n\t" \
  "s_waitcnt lgkmcnt(6)\n\t" \
  "v_mfma_f32_16x16x32_bf16 v[96:99], a[208:211], a[32:35], v[96:99]\n\t" \
  "ds_read_b64_tr_b16 a[200:201], v24 offset:43008\n\t" \
  "ds_read_b64_tr_b16 a[202:203], v24 offset:43264\n\t" \
  "v_mfma_f32_16x16x32_bf16 v[100:103], a[208:211], a[36:39], v[100:103]\n\t" \
  "ds_read_b128 a[64:67], v30 offset:0\n\t" \
  "ds_read_b128 a[68:71], v30 offset:2048\n\t" \
  "s_waitcnt vmcnt(8)\n\t" \
  "v_mfma_f32_16x16x32_bf16 v[104:107], a[208:211], a[40:43], v[104:107]\n\t" \
  "buffer_load_dwordx4 v19, %1, s43 offen offset:2048 lds\n\t" \
  "v_mfma_f32_16x16x32_bf16 v[108:111], a[208:211], a[44:47], v[108:111]\n\t" \
  "ds_read_b128 a[72:75], v30 offset:4096\n\t" \
  "v_mfma_f32_16x16x32_bf16 v[112:115], a[212:215], a[32:35], v[112:115]\n\t" \
  "ds_read_b64_tr_b16 a[204:205], v25 offset:43008\n\t" \
  "ds_read_b64_tr_b16 a[206:207], v25 offset:43264\n\t" \
  "v_mfma_f32_16x16x32_bf16 v[116:119], a[212:215], a[36:39], v[116:119]\n\t" \
  "v_mfma_f32_16x16x32_bf16 v[120:123], a[212:215], a[40:43], v[120:123]\n\t" \
  "v_mfma_f32_16x16x32_bf16 v[124:127], a[212:215], a[44:47], v[124:127]\n\t" \
  "s_waitcnt lgkmcnt(7)\n\t" \
  "v_mfma_f32_16x16x32_bf16 v[64:67], a[192:195], a[48:51], v[64:67]\n\t" \
  "ds_read_b64_tr_b16 a[208:209], v24 offset:37888\n\t" \
  "ds_read_b64_tr_b16 a[210:211], v24 offset:38144\n\t" \
  "v_mfma_f32_16x16x32_bf16 v[68:71], a[192:195], a[52:55], v[68:71]\n\t" \
  "ds_read_b128 a[76:79], v30 offset:6144\n\t" \
  "ds_read_b128 v[52:55], v56 offset:2048\n\t" \
  "v_mfma_f32_16x16x32_bf16 v[72:75], a[192:195], a[56:59], v[72:75]\n\t" \
  "v_add_u32 v31, 4294901760, v31\n\t" \
  "v_mfma_f32_16x16x32_bf16 v[76:79], a[192:195], a[60:63], v[76:79]\n\t" \
  "v_mfma_f32_16x16x32_bf16 v[80:83], a[196:199], a[48:51], v[80:83]\n\t" \
  "ds_read_b64_tr_b16 a[212:213], v25 offset:37888\n\t" \
  "ds_read_b64_tr_b16 a[214:215], v25 offset:38144\n\t" \
  "v_mfma_f32_16x16x32_bf16 v[84:87], a[196:199], a[52:55], v[84:87]\n\t" \
  "v_mfma_f32_16x16x32_bf16 v[88:91], a[196:199], a[56:59], v[88:91]\n\t" \
  "v_mfma_f32_16x16x32_bf16 v[92:95], a[196:199], a[60:63], v[92:95]\n\t" \
  "s_waitcnt lgkmcnt(6)\n\t" \
  "v_mfma_f32_16x16x32_bf16 v[96:99], a[200:203], a[48:51], v[96:99]\n\t" \
  "ds_read_b64_tr_b16 a[192:193], v24 offset:44032\n\t" \
  "ds_read_b64_tr_b16 a[194:195], v24 offset:44288\n\t" \
  "v_mfma_f32_16x16x32_bf16 v[100:103], a[200:203], a[52:55], v[100:103]\n\t" \
  "ds_read_b128 v[20:23], v56 offset:3072\n\t" \
  "s_waitcnt lgkmcnt(0)\n\t" \
  "v_mfma_f32_16x16x32_bf16 v[104:107], a[200:203], a[56:59], v[104:107]\n\t" \
  "ds_read_b128 a[80:83], v31 offset:0\n\t" \
  "ds_read_b128 a[84:87], v31 offset:2048\n\t" \
  "v_mfma_f32_16x16x32_bf16 v[108:111], a[200:203], a[60:63], v[108:111]\n\t" \
  "ds_read_b128 a[88:91], v31 offset:4096\n\t" \
  "ds_read_b128 a[92:95], v31 offset:6144\n\t" \
  "v_mfma_f32_16x16x32_bf16 v[112:115], a[204:207], a[48:51], v[112:115]\n\t" \
  "ds_read_b64_tr_b16 a[196:197], v25 offset:44032\n\t" \
  "ds_read_b64_tr_b16 a[198:199], v25 offset:44288\n\t" \
  "v_mfma_f32_16x16x32_bf16 v[116:119], a[204:207], a[52:55], v[116:119]\n\t" \
  "v_add_f32 v44, v44, v48\n\t" \
  "v_add_f32 v45, v45, v49\n\t" \
  "v_mfma_f32_16x16x32_bf16 v[120:123], a[204:207], a[56:59], v[120:123]\n\t" \
  "v_add_f32 v46, v46, v50\n\t" \
  "v_add_f32 v47, v47, v51\n\t" \
  "v_mfma_f32_16x16x32_bf16 v[124:127], a[204:207], a[60:63], v[124:127]\n\t" \
  "v_add_f32 v52, v52, v20\n\t" \
  "v_mfma_f32_16x16x32_bf16 v[64:67], a[208:211], a[64:67], v[64:67]\n\t" \
  "ds_read_b64_tr_b16 a[200:201], v24 offset:38912\n\t" \
  "ds_read_b64_tr_b16 a[202:203], v24 offset:39168\n\t" \
  "v_mfma_f32_16x16x32_bf16 v[68:71], a[208:211], a[68:71], v[68:71]\n\t" \
  "v_add_f32 v53, v53, v21\n\t" \
  "v_add_f32 v54, v54, v22\n\t" \
  "v_mfma_f32_16x16x32_bf16 v[72:75], a[208:211], a[72:75], v[72:75]\n\t" \
  "v_add_f32 v55, v55, v23\n\t" \
  "v_add_f32 v52, v52, v44\n\t" \
  "v_mfma_f32_16x16x32_bf16 v[76:79], a[208:211], a[76:79], v[76:79]\n\t" \
  "v_add_f32 v53, v53, v45\n\t" \
  "v_add_f32 v54, v54, v46\n\t" \
  "v_mfma_f32_16x16x32_bf16 v[80:83], a[212:215], a[64:67], v[80:83]\n\t" \
  "ds_read_b64_tr_b16 a[204:205], v25 offset:38912\n\t" \
  "ds_read_b64_tr_b16 a[206:207], v25 offset:39168\n\t" \
  "v_mfma_f32_16x16x32_bf16 v[84:87], a[212:215], a[68:71], v[84:87]\n\t" \
  "v_add_f32 v55, v55, v47\n\t" \
  "v_mfma_f32_16x16x32_bf16 v[88:91], a[212:215], a[72:75], v[88:91]\n\t" \
  "v_mfma_f32_16x16x32_bf16 v[92:95], a[212:215], a[76:79], v[92:95]\n\t" \
  "s_waitcnt lgkmcnt(4)\n\t" \
  "v_mfma_f32_16x16x32_bf16 v[96:99], a[192:195], a[64:67], v[96:99]\n\t" \
  "ds_read_b64_tr_b16 a[208:209], v24 offset:45056\n\t" \
  "ds_read_b64_tr_b16 a[210:211], v24 offset:45312\n\t" \
  "v_mfma_f32_16x16x32_bf16 v[100:103], a[192:195], a[68:71], v[100:103]\n\t" \
  "v_mfma_f32_16x16x32_bf16 v[104:107], a[192:195], a[72:75], v[104:107]\n\t" \
  "v_mfma_f32_16x16x32_bf16 v[108:111], a[192:195], a[76:79], v[108:111]\n\t" \
  "v_mfma_f32_16x16x32_bf16 v[112:115], a[196:199], a[64:67], v[112:115]\n\t" \
  "ds_read_b64_tr_b16 a[212:213], v25 offset:45056\n\t" \
  "ds_read_b64_tr_b16 a[214:215], v25 offset:45312\n\t" \
  "v_mfma_f32_16x16x32_bf16 v[116:119], a[196:199], a[68:71], v[116:119]\n\t" \
  "v_mfma_f32_16x16x32_bf16 v[120:123], a[196:199], a[72:75], v[120:123]\n\t" \
  "v_mfma_f32_16x16x32_bf16 v[124:127], a[196:199], a[76:79], v[124:127]\n\t" \
  "s_waitcnt lgkmcnt(4)\n\t" \
  "s_barrier\n\t" \
  "v_mfma_f32_16x16x32_bf16 v[64:67], a[200:203], a[80:83], v[64:67]\n\t" \
  "ds_read_b64_tr_b16 a[192:193], v24 offset:49152\n\t" \
  "ds_read_b64_tr_b16 a[194:195], v24 offset:49408\n\t" \
  "v_mfma_f32_16x16x32_bf16 v[68:71], a[200:203], a[84:87], v[68:71]\n\t" \
  "s_add_u32 m0, s40, 24576\n\t" \
  "s_add_u32 s43, s50, 4423936\n\t" \
  "v_mfma_f32_16x16x32_bf16 v[72:75], a[200:203], a[88:91], v[72:75]\n\t" \
  "buffer_load_dwordx4 v17, %1, s43 offen offset:0 lds\n\t" \
  "v_mfma_f32_16x16x32_bf16 v[76:79], a[200:203], a[92:95], v[76:79]\n\t" \
  "ds_write_b128 v62, v[52:55]\n\t" \
  "ds_read_b128 v[44:47], v35 offset:0\n\t" \
  "v_mfma_f32_16x16x32_bf16 v[80:83], a[204:207], a[80:83], v[80:83]\n\t" \
  "ds_read_b64_tr_b16 a[196:197], v25 offset:49152\n\t" \
  "ds_read_b64_tr_b16 a[198:199], v25 offset:49408\n\t" \
  "v_mfma_f32_16x16x32_bf16 v[84:87], a[204:207], a[84:87], v[84:87]\n\t" \
  "buffer_load_dwordx4 v18, %1, s43 offen offset:1024 lds\n\t" \
  "v_mfma_f32_16x16x32_bf16 v[88:91], a[204:207], a[88:91], v[88:91]\n\t" \
  "ds_read_b128 v[48:51], v35 offset:64\n\t" \
  "s_waitcnt lgkmcnt(4)\n\t" \
  "v_mfma_f32_16x16x32_bf16 v[92:95], a[204:207], a[92:95], v[92:95]\n\t" \
  "ds_read_b128 v[52:55], v35 offset:128\n\t" \
  "v_mfma_f32_16x16x32_bf16 v[96:99], a[208:211], a[80:83], v[96:99]\n\t" \
  "ds_read_b64_tr_b16 a[200:201], v24 offset:55296\n\t" \
  "ds_read_b64_tr_b16 a[202:203], v24 offset:55552\n\t" \
  "v_mfma_f32_16x16x32_bf16 v[100:103], a[208:211], a[84:87], v[100:103]\n\t" \
  "ds_read_b128 v[56:59], v35 offset:192\n\t" \
  "v_mov_b32 v30, s79\n\t" \
  "v_mov_b32 v31, s95\n\t" \
  "v_mfma_f32_16x16x32_bf16 v[104:107], a[208:211], a[88:91], v[104:107]\n\t" \
  "buffer_load_dwordx4 v19, %1, s43 offen offset:2048 lds\n\t" \
  "v_mfma_f32_16x16x32_bf16 v[108:111], a[208:211], a[92:95], v[108:111]\n\t" \
  "v_and_b32 v33, 1, v16\n\t" \
  "v_cmp_eq_u32 vcc, 1, v33\n\t" \
  "s_nop 1\n\t" \
  "v_cndmask_b32 v30, v30, v31, vcc\n\t" \
  "v_mfma_f32_16x16x32_bf16 v[112:115], a[212:215], a[80:83], v[112:115]\n\t" \
  "ds_read_b64_tr_b16 a[204:205], v25 offset:55296\n\t" \
  "ds_read_b64_tr_b16 a[206:207], v25 offset:55552\n\t" \
  "v_mfma_f32_16x16x32_bf16 v[116:119], a[212:215], a[84:87], v[116:119]\n\t" \
  "v_mfma_f32_16x16x32_bf16 v[120:123], a[212:215], a[88:91], v[120:123]\n\t" \
  "v_mfma_f32_16x16x32_bf16 v[124:127], a[212:215], a[92:95], v[124:127]\n\t" \
  "v_add_u32 v17, 4096, v63\n\t" \
  "v_add_u32 v18, 3200, v63\n\t" \
  "s_waitcnt lgkmcnt(7)\n\t" \
  "v_mfma_f32_16x16x32_bf16 v[128:131], a[192:195], a[0:3], v[128:131]\n\t" \
  "ds_read_b64_tr_b16 a[208:209], v24 offset:50176\n\t" \
  "ds_read_b64_tr_b16 a[210:211], v24 offset:50432\n\t" \
  "v_add_u32 v19, 2304, v63\n\t" \
  "v_mfma_f32_16x16x32_bf16 v[132:135], a[192:195], a[4:7], v[132:135]\n\t" \
  "s_add_u32 m0, s40, 61440\n\t" \
  "s_add_u32 s43, s51, 0\n\t" \
  "v_mfma_f32_16x16x32_bf16 v[136:139], a[192:195], a[8:11], v[136:139]\n\t" \
  "buffer_load_dwordx4 v17, %2, s43 offen offset:0 lds\n\t" \
  "v_mfma_f32_16x16x32_bf16 v[140:143], a[192:195], a[12:15], v[140:143]\n\t" \
  "v_mfma_f32_16x16x32_bf16 v[144:147], a[196:199], a[0:3], v[144:147]\n\t" \
  "ds_read_b64_tr_b16 a[212:213], v25 offset:50176\n\t" \
  "ds_read_b64_tr_b16 a[214:215], v25 offset:50432\n\t" \
  "s_waitcnt lgkmcnt(10)\n\t" \
  "v_mfma_f32_16x16x32_bf16 v[148:151], a[196:199], a[4:7], v[148:151]\n\t" \
  "buffer_load_dwordx4 v18, %2, s43 offen offset:1024 lds\n\t" \
  "s_waitcnt vmcnt(10)\n\t" \
  "v_mfma_f32_16x16x32_bf16 v[152:155], a[196:199], a[8:11], v[152:155]\n\t" \
  "s_add_u32 s67, s45, 0\n\t" \
  "v_fmac_f32 v64, v40, v44\n\t" \
  "v_fmac_f32 v65, v40, v45\n\t" \
  "v_mfma_f32_16x16x32_bf16 v[156:159], a[196:199], a[12:15], v[156:159]\n\t" \
  "v_fmac_f32 v66, v40, v46\n\t" \
  "v_fmac_f32 v67, v40, v47\n\t" \
  "s_waitcnt lgkmcnt(4)\n\t" \
  "v_mfma_f32_16x16x32_bf16 v[160:163], a[200:203], a[0:3], v[160:163]\n\t" \
  "ds_read_b64_tr_b16 a[192:193], v24 offset:56320\n\t" \
  "ds_read_b64_tr_b16 a[194:195], v24 offset:56576\n\t" \
  "v_fmac_f32 v80, v40, v48\n\t" \
  "v_mfma_f32_16x16x32_bf16 v[164:167], a[200:203], a[4:7], v[164:167]\n\t" \
  "buffer_load_dwordx4 v19, %2, s43 offen offset:2048 lds\n\t" \
  "v_fmac_f32 v81, v40, v49\n\t" \
  "v_mfma_f32_16x16x32_bf16 v[168:171], a[200:203], a[8:11], v[168:171]\n\t" \
  "v_fmac_f32 v82, v40, v50\n\t" \
  "v_fmac_f32 v83, v40, v51\n\t" \
  "v_cvt_pk_bf16_f32 v64, v64, v65\n\t" \
  "v_mfma_f32_16x16x32_bf16 v[172:175], a[200:203], a[12:15], v[172:175]\n\t" \
  "v_cvt_pk_bf16_f32 v65, v66, v67\n\t" \
  "v_cvt_pk_bf16_f32 v66, v80, v81\n\t" \
  "v_cvt_pk_bf16_f32 v67, v82, v83\n\t" \
  "v_mfma_f32_16x16x32_bf16 v[176:179], a[204:207], a[0:3], v[176:179]\n\t" \
  "ds_read_b64_tr_b16 a[196:197], v25 offset:56320\n\t" \
  "ds_read_b64_tr_b16 a[198:199], v25 offset:56576\n\t" \
  "v_pk_max_i16 v64, v64, 0\n\t" \
  "v_mfma_f32_16x16x32_bf16 v[180:183], a[204:207], a[4:7], v[180:183]\n\t" \
  "v_pk_max_i16 v65, v65, 0\n\t" \
  "v_pk_max_i16 v66, v66, 0\n\t" \
  "v_pk_max_i16 v67, v67, 0\n\t" \
  "v_mfma_f32_16x16x32_bf16 v[184:187], a[204:207], a[8:11], v[184:187]\n\t" \
  "s_add_u32 m0, s40, 73728\n\t" \
  "s_add_u32 s43, s51, 36864\n\t" \
  "v_mfma_f32_16x16x32_bf16 v[188:191], a[204:207], a[12:15], v[188:191]\n\t" \
  "buffer_store_dwordx4 v[64:67], v32, %3, s67 offen offset:0 sc1\n\t" \
  "s_waitcnt lgkmcnt(4)\n\t" \
  "v_mfma_f32_16x16x32_bf16 v[128:131], a[208:211], a[16:19], v[128:131]\n\t" \
  "ds_read_b64_tr_b16 a[200:201], v24 offset:51200\n\t" \
  "ds_read_b64_tr_b16 a[202:203], v24 offset:51456\n\t" \
  "v_fmac_f32 v68, v41, v44\n\t" \
  "v_mfma_f32_16x16x32_bf16 v[132:135], a[208:211], a[20:23], v[132:135]\n\t" \
  "buffer_load_dwordx4 v17, %2, s43 offen offset:0 lds\n\t" \
  "v_fmac_f32 v69, v41, v45\n\t" \
  "v_mfma_f32_16x16x32_bf16 v[136:139], a[208:211], a[24:27], v[136:139]\n\t" \
  "v_fmac_f32 v70, v41, v46\n\t" \
  "v_fmac_f32 v71, v41, v47\n\t" \
  "v_fmac_f32 v84, v41, v48\n\t" \
  "v_mfma_f32_16x16x32_bf16 v[140:143], a[208:211], a[28:31], v[140:143]\n\t" \
  "buffer_load_dwordx4 v18, %2, s43 offen offset:1024 lds\n\t" \
  "v_fmac_f32 v85, v41, v49\n\t" \
  "v_mfma_f32_16x16x32_bf16 v[144:147], a[212:215], a[16:19], v[144:147]\n\t" \
  "ds_read_b64_tr_b16 a[204:205], v25 offset:51200\n\t" \
  "ds_read_b64_tr_b16 a[206:207], v25 offset:51456\n\t" \
  "v_fmac_f32 v86, v41, v50\n\t" \
  "v_mfma_f32_16x16x32_bf16 v[148:151], a[212:215], a[20:23], v[148:151]\n\t" \
  "v_fmac_f32 v87, v41, v51\n\t" \
  "v_cvt_pk_bf16_f32 v68, v68, v69\n\t" \
  "v_cvt_pk_bf16_f32 v69, v70, v71\n\t" \
  "v_mfma_f32_16x16x32_bf16 v[152:155], a[212:215], a[24:27], v[152:155]\n\t" \
  "v_cvt_pk_bf16_f32 v70, v84, v85\n\t" \
  "v_cvt_pk_bf16_f32 v71, v86, v87\n\t" \
  "v_mfma_f32_16x16x32_bf16 v[156:159], a[212:215], a[28:31], v[156:159]\n\t" \
  "v_pk_max_i16 v68, v68, 0\n\t" \
  "s_waitcnt lgkmcnt(4)\n\t" \
  "v_mfma_f32_16x16x32_bf16 v[160:163], a[192:195], a[16:19], v[160:163]\n\t" \
  "ds_read_b64_tr_b16 a[208:209], v24 offset:57344\n\t" \
  "ds_read_b64_tr_b16 a[210:211], v24 offset:57600\n\t" \
  "v_mfma_f32_16x16x32_bf16 v[164:167], a[192:195], a[20:23], v[164:167]\n\t" \
  "buffer_load_dwordx4 v19, %2, s43 offen offset:2048 lds\n\t" \
  "v_mfma_f32_16x16x32_bf16 v[168:171], a[192:195], a[24:27], v[168:171]\n\t" \
  "v_pk_max_i16 v69, v69, 0\n\t" \
  "v_pk_max_i16 v70, v70, 0\n\t" \
  "v_mfma_f32_16x16x32_bf16 v[172:175], a[192:195], a[28:31], v[172:175]\n\t" \
  "v_pk_max_i16 v71, v71, 0\n\t" \
  "v_mfma_f32_16x16x32_bf16 v[176:179], a[196:199], a[16:19], v[176:179]\n\t" \
  "ds_read_b64_tr_b16 a[212:213], v25 offset:57344\n\t" \
  "ds_read_b64_tr_b16 a[214:215], v25 offset:57600\n\t" \
  "v_mfma_f32_16x16x32_bf16 v[180:183], a[196:199], a[20:23], v[180:183]\n\t" \
  "s_add_u32 m0, s40, 86016\n\t" \
  "s_add_u32 s43, s51, 73728\n\t" \
  "v_mfma_f32_16x16x32_bf16 v[184:187], a[196:199], a[24:27], v[184:187]\n\t" \
  "buffer_load_dwordx4 v17, %2, s43 offen offset:0 lds\n\t" \
  "v_mfma_f32_16x16x32_bf16 v[188:191], a[196:199], a[28:31], v[188:191]\n\t" \
  "buffer_store_dwordx4 v[68:71], v32, %3, s67 offen offset:1024 sc1\n\t" \
  "s_waitcnt vmcnt(15) lgkmcnt(4)\n\t" \
  "s_barrier\n\t" \
  "v_mfma_f32_16x16x32_bf16 v[128:131], a[200:203], a[32:35], v[128:131]\n\t" \
  "ds_read_b64_tr_b16 a[192:193], v24 offset:0\n\t" \
  "ds_read_b64_tr_b16 a[194:195], v24 offset:256\n\t" \
  "v_mfma_f32_16x16x32_bf16 v[132:135], a[200:203], a[36:39], v[132:135]\n\t" \
  "v_fmac_f32 v72, v42, v44\n\t" \
  "v_fmac_f32 v73, v42, v45\n\t" \
  "v_mfma_f32_16x16x32_bf16 v[136:139], a[200:203], a[40:43], v[136:139]\n\t" \
  "buffer_load_dwordx4 v18, %2, s43 offen offset:1024 lds\n\t" \
  "v_mfma_f32_16x16x32_bf16 v[140:143], a[200:203], a[44:47], v[140:143]\n\t" \
  "v_fmac_f32 v74, v42, v46\n\t" \
  "v_fmac_f32 v75, v42, v47\n\t" \
  "v_mfma_f32_16x16x32_bf16 v[144:147], a[204:207], a[32:35], v[144:147]\n\t" \
  "ds_read_b64_tr_b16 a[196:197], v25 offset:0\n\t" \
  "ds_read_b64_tr_b16 a[198:199], v25 offset:256\n\t" \
  "v_mfma_f32_16x16x32_bf16 v[148:151], a[204:207], a[36:39], v[148:151]\n\t" \
  "v_fmac_f32 v88, v42, v48\n\t" \
  "v_fmac_f32 v89, v42, v49\n\t" \
  "v_mfma_f32_16x16x32_bf16 v[152:155], a[204:207], a[40:43], v[152:155]\n\t" \
  "v_fmac_f32 v90, v42, v50\n\t" \
  "v_fmac_f32 v91, v42, v51\n\t" \
  "v_mfma_f32_16x16x32_bf16 v[156:159], a[204:207], a[44:47], v[156:159]\n\t" \
  "v_cvt_pk_bf16_f32 v72, v72, v73\n\t" \
  "s_waitcnt lgkmcnt(4)\n\t" \
  "v_mfma_f32_16x16x32_bf16 v[160:163], a[208:211], a[32:35], v[160:163]\n\t" \
  "ds_read_b64_tr_b16 a[200:201], v24 offset:6144\n\t" \
  "ds_read_b64_tr_b16 a[202:203], v24 offset:6400\n\t" \
  "v_mfma_f32_16x16x32_bf16 v[164:167], a[208:211], a[36:39], v[164:167]\n\t" \
  "buffer_load_dwordx4 v19, %2, s43 offen offset:2048 lds\n\t" \
  "v_mfma_f32_16x16x32_bf16 v[168:171], a[208:211], a[40:43], v[168:171]\n\t" \
  "v_cvt_pk_bf16_f32 v73, v74, v75\n\t" \
  "v_cvt_pk_bf16_f32 v74, v88, v89\n\t" \
  "v_mfma_f32_16x16x32_bf16 v[172:175], a[208:211], a[44:47], v[172:175]\n\t" \
  "v_cvt_pk_bf16_f32 v75, v90, v91\n\t" \
  "v_pk_max_i16 v72, v72, 0\n\t" \
  "v_mfma_f32_16x16x32_bf16 v[176:179], a[212:215], a[32:35], v[176:179]\n\t" \
  "ds_read_b64_tr_b16 a[204:205], v25 offset:6144\n\t" \
  "ds_read_b64_tr_b16 a[206:207], v25 offset:6400\n\t" \
  "v_mfma_f32_16x16x32_bf16 v[180:183], a[212:215], a[36:39], v[180:183]\n\t" \
  "v_pk_max_i16 v73, v73, 0\n\t" \
  "v_pk_max_i16 v74, v74, 0\n\t" \
  "v_mfma_f32_16x16x32_bf16 v[184:187], a[212:215], a[40:43], v[184:187]\n\t" \
  "v_pk_max_i16 v75, v75, 0\n\t" \
  "v_mfma_f32_16x16x32_bf16 v[188:191], a[212:215], a[44:47], v[188:191]\n\t" \
  "buffer_store_dwordx4 v[72:75], v32, %3, s67 offen offset:2048 sc1\n\t" \
  "s_waitcnt lgkmcnt(4)\n\t" \
  "v_mfma_f32_16x16x32_bf16 v[128:131], a[192:195], a[48:51], v[128:131]\n\t" \
  "ds_read_b64_tr_b16 a[208:209], v24 offset:1024\n\t" \
  "ds_read_b64_tr_b16 a[210:211], v24 offset:1280\n\t" \
  "v_mfma_f32_16x16x32_bf16 v[132:135], a[192:195], a[52:55], v[132:135]\n\t" \
  "v_fmac_f32 v76, v43, v44\n\t" \
  "v_fmac_f32 v77, v43, v45\n\t" \
  "v_mfma_f32_16x16x32_bf16 v[136:139], a[192:195], a[56:59], v[136:139]\n\t" \
  "v_fmac_f32 v78, v43, v46\n\t" \
  "v_fmac_f32 v79, v43, v47\n\t" \
  "v_mfma_f32_16x16x32_bf16 v[140:143], a[192:195], a[60:63], v[140:143]\n\t" \
  "v_fmac_f32 v92, v43, v48\n\t" \
  "v_fmac_f32 v93, v43, v49\n\t" \
  "v_mfma_f32_16x16x32_bf16 v[144:147], a[196:199], a[48:51], v[144:147]\n\t" \
  "ds_read_b64_tr_b16 a[212:213], v25 offset:1024\n\t" \
  "ds_read_b64_tr_b16 a[214:215], v25 offset:1280\n\t" \
  "v_mfma_f32_16x16x32_bf16 v[148:151], a[196:199], a[52:55], v[148:151]\n\t" \
  "v_fmac_f32 v94, v43, v50\n\t" \
  "v_fmac_f32 v95, v43, v51\n\t" \
  "v_mfma_f32_16x16x32_bf16 v[152:155], a[196:199], a[56:59], v[152:155]\n\t" \
  "v_cvt_pk_bf16_f32 v76, v76, v77\n\t" \
  "v_cvt_pk_bf16_f32 v77, v78, v79\n\t" \
  "v_mfma_f32_16x16x32_bf16 v[156:159], a[196:199], a[60:63], v[156:159]\n\t" \
  "v_cvt_pk_bf16_f32 v78, v92, v93\n\t" \
  "s_waitcnt lgkmcnt(4)\n\t" \
  "v_mfma_f32_16x16x32_bf16 v[160:163], a[200:203], a[48:51], v[160:163]\n\t" \
  "ds_read_b64_tr_b16 a[192:193], v24 offset:7168\n\t" \
  "ds_read_b64_tr_b16 a[194:195], v24 offset:7424\n\t" \
  "v_mfma_f32_16x16x32_bf16 v[164:167], a[200:203], a[52:55], v[164:167]\n\t" \
  "v_cvt_pk_bf16_f32 v79, v94, v95\n\t" \
  "v_pk_max_i16 v76, v76, 0\n\t" \
  "v_mfma_f32_16x16x32_bf16 v[168:171], a[200:203], a[56:59], v[168:171]\n\t" \
  "v_pk_max_i16 v77, v77, 0\n\t" \
  "v_pk_max_i16 v78, v78, 0\n\t" \
  "v_mfma_f32_16x16x32_bf16 v[172:175], a[200:203], a[60:63], v[172:175]\n\t" \
  "v_pk_max_i16 v79, v79, 0\n\t" \
  "v_mfma_f32_16x16x32_bf16 v[176:179], a[204:207], a[48:51], v[176:179]\n\t" \
  "ds_read_b64_tr_b16 a[196:197], v25 offset:7168\n\t" \
  "ds_read_b64_tr_b16 a[198:199], v25 offset:7424\n\t" \
  "v_mfma_f32_16x16x32_bf16 v[180:183], a[204:207], a[52:55], v[180:183]\n\t" \
  "buffer_store_dwordx4 v[76:79], v32, %3, s67 offen offset:3072 sc1\n\t" \
  "v_mfma_f32_16x16x32_bf16 v[184:187], a[204:207], a[56:59], v[184:187]\n\t" \
  "ds_read_b128 v[44:47], v35 offset:256\n\t" \
  "ds_read_b128 v[48:51], v35 offset:320\n\t" \
  "s_add_u32 s67, s45, 4096\n\t" \
  "v_mfma_f32_16x16x32_bf16 v[188:191], a[204:207], a[60:63], v[188:191]\n\t" \
  "v_fmac_f32 v96, v40, v52\n\t" \
  "v_fmac_f32 v97, v40, v53\n\t" \
  "s_waitcnt lgkmcnt(6)\n\t" \
  "v_mfma_f32_16x16x32_bf16 v[128:131], a[208:211], a[64:67], v[128:131]\n\t" \
  "ds_read_b64_tr_b16 a[200:201], v24 offset:2048\n\t" \
  "ds_read_b64_tr_b16 a[202:203], v24 offset:2304\n\t" \
  "v_fmac_f32 v98, v40, v54\n\t" \
  "v_mfma_f32_16x16x32_bf16 v[132:135], a[208:211], a[68:71], v[132:135]\n\t" \
  "v_fmac_f32 v99, v40, v55\n\t" \
  "v_fmac_f32 v112, v40, v56\n\t" \
  "v_fmac_f32 v113, v40, v57\n\t" \
  "v_mfma_f32_16x16x32_bf16 v[136:139], a[208:211], a[72:75], v[136:139]\n\t" \
  "v_fmac_f32 v114, v40, v58\n\t" \
  "v_fmac_f32 v115, v40, v59\n\t" \
  "v_cvt_pk_bf16_f32 v96, v96, v97\n\t" \
  "v_mfma_f32_16x16x32_bf16 v[140:143], a[208:211], a[76:79], v[140:143]\n\t" \
  "v_cvt_pk_bf16_f32 v97, v98, v99\n\t" \
  "v_cvt_pk_bf16_f32 v98, v112, v113\n\t" \
  "v_cvt_pk_bf16_f32 v99, v114, v115\n\t" \
  "v_mfma_f32_16x16x32_bf16 v[144:147], a[212:215], a[64:67], v[144:147]\n\t" \
  "ds_read_b64_tr_b16 a[204:205], v25 offset:2048\n\t" \
  "ds_read_b64_tr_b16 a[206:207], v25 offset:2304\n\t" \
  "v_mfma_f32_16x16x32_bf16 v[148:151], a[212:215], a[68:71], v[148:151]\n\t" \
  "v_pk_max_i16 v96, v96, 0\n\t" \
  "v_pk_max_i16 v97, v97, 0\n\t" \
  "v_mfma_f32_16x16x32_bf16 v[152:155], a[212:215], a[72:75], v[152:155]\n\t" \
  "v_pk_max_i16 v98, v98, 0\n\t" \
  "v_pk_max_i16 v99, v99, 0\n\t" \
  "v_mfma_f32_16x16x32_bf16 v[156:159], a[212:215], a[76:79], v[156:159]\n\t" \
  "buffer_store_dwordx4 v[96:99], v32, %3, s67 offen offset:0 sc1\n\t" \
  "s_waitcnt lgkmcnt(6)\n\t" \
  "v_mfma_f32_16x16x32_bf16 v[160:163], a[192:195], a[64:67], v[160:163]\n\t" \
  "ds_read_b64_tr_b16 a[208:209], v24 offset:8192\n\t" \
  "ds_read_b64_tr_b16 a[210:211], v24 offset:8448\n\t" \
  "v_mfma_f32_16x16x32_bf16 v[164:167], a[192:195], a[68:71], v[164:167]\n\t" \
  "s_add_u32 m0, s40, 98304\n\t" \
  "s_add_u32 s43, s51, 110592\n\t" \
  "v_mfma_f32_16x16x32_bf16 v[168:171], a[192:195], a[72:75], v[168:171]\n\t" \
  "buffer_load_dwordx4 v17, %2, s43 offen offset:0 lds\n\t" \
  "v_mfma_f32_16x16x32_bf16 v[172:175], a[192:195], a[76:79], v[172:175]\n\t" \
  "v_fmac_f32 v100, v41, v52\n\t" \
  "v_fmac_f32 v101, v41, v53\n\t" \
  "v_mfma_f32_16x16x32_bf16 v[176:179], a[196:199], a[64:67], v[176:179]\n\t" \
  "ds_read_b64_tr_b16 a[212:213], v25 offset:8192\n\t" \
  "ds_read_b64_tr_b16 a[214:215], v25 offset:8448\n\t" \
  "v_mfma_f32_16x16x32_bf16 v[180:183], a[196:199], a[68:71], v[180:183]\n\t" \
  "buffer_load_dwordx4 v18, %2, s43 offen offset:1024 lds\n\t" \
  "v_mfma_f32_16x16x32_bf16 v[184:187], a[196:199], a[72:75], v[184:187]\n\t" \
  "v_fmac_f32 v102, v41, v54\n\t" \
  "v_fmac_f32 v103, v41, v55\n\t" \
  "v_mfma_f32_16x16x32_bf16 v[188:191], a[196:199], a[76:79], v[188:191]\n\t" \
  "s_waitcnt vmcnt(19) lgkmcnt(4)\n\t" \
  "s_barrier\n\t" \
  "v_mfma_f32_16x16x32_bf16 v[128:131], a[200:203], a[80:83], v[128:131]\n\t" \
  "ds_read_b64_tr_b16 a[192:193], v24 offset:12288\n\t" \
  "ds_read_b64_tr_b16 a[194:195], v24 offset:12544\n\t" \
  "v_mfma_f32_16x16x32_bf16 v[132:135], a[200:203], a[84:87], v[132:135]\n\t" \
  "v_fmac_f32 v116, v41, v56\n\t" \
  "v_fmac_f32 v117, v41, v57\n\t" \
  "v_mfma_f32_16x16x32_bf16 v[136:139], a[200:203], a[88:91], v[136:139]\n\t" \
  "buffer_load_dwordx4 v19, %2, s43 offen offset:2048 lds\n\t" \
  "v_mfma_f32_16x16x32_bf16 v[140:143], a[200:203], a[92:95], v[140:143]\n\t" \
  "v_fmac_f32 v118, v41, v58\n\t" \
  "v_fmac_f32 v119, v41, v59\n\t" \
  "v_mfma_f32_16x16x32_bf16 v[144:147], a[204:207], a[80:83], v[144:147]\n\t" \
  "ds_read_b64_tr_b16 a[196:197], v25 offset:12288\n\t" \
  "ds_read_b64_tr_b16 a[198:199], v25 offset:12544\n\t" \
  "v_mfma_f32_16x16x32_bf16 v[148:151], a[204:207], a[84:87], v[148:151]\n\t" \
  "v_cvt_pk_bf16_f32 v100, v100, v101\n\t" \
  "v_cvt_pk_bf16_f32 v101, v102, v103\n\t" \
  "v_mfma_f32_16x16x32_bf16 v[152:155], a[204:207], a[88:91], v[152:155]\n\t" \
  "v_cvt_pk_bf16_f32 v102, v116, v117\n\t" \
  "v_cvt_pk_bf16_f32 v103, v118, v119\n\t" \
  "v_mfma_f32_16x16x32_bf16 v[156:159], a[204:207], a[92:95], v[156:159]\n\t" \
  "v_pk_max_i16 v100, v100, 0\n\t" \
  "s_waitcnt lgkmcnt(4)\n\t" \
  "v_mfma_f32_16x16x32_bf16 v[160:163], a[208:211], a[80:83], v[160:163]\n\t" \
  "ds_read_b64_tr_b16 a[200:201], v24 offset:18432\n\t" \
  "ds_read_b64_tr_b16 a[202:203], v24 offset:18688\n\t" \
  "v_mfma_f32_16x16x32_bf16 v[164:167], a[208:211], a[84:87], v[164:167]\n\t" \
  "v_pk_max_i16 v101, v101, 0\n\t" \
  "v_pk_max_i16 v102, v102, 0\n\t" \
  "v_mfma_f32_16x16x32_bf16 v[168:171], a[208:211], a[88:91], v[168:171]\n\t" \
  "v_pk_max_i16 v103, v103, 0\n\t" \
  "v_mfma_f32_16x16x32_bf16 v[172:175], a[208:211], a[92:95], v[172:175]\n\t" \
  "buffer_store_dwordx4 v[100:103], v32, %3, s67 offen offset:1024 sc1\n\t" \
  "v_mfma_f32_16x16x32_bf16 v[176:179], a[212:215], a[80:83], v[176:179]\n\t" \
  "ds_read_b64_tr_b16 a[204:205], v25 offset:18432\n\t" \
  "ds_read_b64_tr_b16 a[206:207], v25 offset:18688\n\t" \
  "v_mfma_f32_16x16x32_bf16 v[180:183], a[212:215], a[84:87], v[180:183]\n\t" \
  "v_fmac_f32 v104, v42, v52\n\t" \
  "v_fmac_f32 v105, v42, v53\n\t" \
  "v_mfma_f32_16x16x32_bf16 v[184:187], a[212:215], a[88:91], v[184:187]\n\t" \
  "v_fmac_f32 v106, v42, v54\n\t" \
  "v_fmac_f32 v107, v42, v55\n\t" \
  "v_mfma_f32_16x16x32_bf16 v[188:191], a[212:215], a[92:95], v[188:191]\n\t" \
  "v_fmac_f32 v120, v42, v56\n\t" \
  "s_waitcnt lgkmcnt(4)\n\t" \
  "v_mfma_f32_16x16x32_bf16 v[192:195], a[192:195], a[0:3], v[192:195]\n\t" \
  "ds_read_b64_tr_b16 a[208:209], v24 offset:13312\n\t" \
  "ds_read_b64_tr_b16 a[210:211], v24 offset:13568\n\t" \
  "v_mfma_f32_16x16x32_bf16 v[196:199], a[192:195], a[4:7], v[196:199]\n\t" \
  "v_fmac_f32 v121, v42, v57\n\t" \
  "v_fmac_f32 v122, v42, v58\n\t" \
  "v_mfma_f32_16x16x32_bf16 v[200:203], a[192:195], a[8:11], v[200:203]\n\t" \
  "v_fmac_f32 v123, v42, v59\n\t" \
  "v_cvt_pk_bf16_f32 v104, v104, v105\n\t" \
  "v_mfma_f32_16x16x32_bf16 v[204:207], a[192:195], a[12:15], v[204:207]\n\t" \
  "v_cvt_pk_bf16_f32 v105, v106, v107\n\t" \
  "v_cvt_pk_bf16_f32 v106, v120, v121\n\t" \
  "v_mfma_f32_16x16x32_bf16 v[208:211], a[196:199], a[0:3], v[208:211]\n\t" \
  "ds_read_b64_tr_b16 a[212:213], v25 offset:13312\n\t" \
  "ds_read_b64_tr_b16 a[214:215], v25 offset:13568\n\t" \
  "v_mfma_f32_16x16x32_bf16 v[212:215], a[196:199], a[4:7], v[212:215]\n\t" \
  "v_cvt_pk_bf16_f32 v107, v122, v123\n\t" \
  "v_pk_max_i16 v104, v104, 0\n\t" \
  "v_mfma_f32_16x16x32_bf16 v[216:219], a[196:199], a[8:11], v[216:219]\n\t" \
  "v_pk_max_i16 v105, v105, 0\n\t" \
  "v_pk_max_i16 v106, v106, 0\n\t" \
  "v_mfma_f32_16x16x32_bf16 v[220:223], a[196:199], a[12:15], v[220:223]\n\t" \
  "v_pk_max_i16 v107, v107, 0\n\t" \
  "s_waitcnt lgkmcnt(4)\n\t" \
  "v_mfma_f32_16x16x32_bf16 v[224:227], a[200:203], a[0:3], v[224:227]\n\t" \
  "ds_read_b64_tr_b16 a[192:193], v24 offset:19456\n\t" \
  "ds_read_b64_tr_b16 a[194:195], v24 offset:19712\n\t" \
  "v_mfma_f32_16x16x32_bf16 v[228:231], a[200:203], a[4:7], v[228:231]\n\t" \
  "buffer_store_dwordx4 v[104:107], v32, %3, s67 offen offset:2048 sc1\n\t" \
  "v_mfma_f32_16x16x32_bf16 v[232:235], a[200:203], a[8:11], v[232:235]\n\t" \
  "v_fmac_f32 v108, v43, v52\n\t" \
  "v_fmac_f32 v109, v43, v53\n\t" \
  "v_mfma_f32_16x16x32_bf16 v[236:239], a[200:203], a[12:15], v[236:239]\n\t" \
  "v_fmac_f32 v110, v43, v54\n\t" \
  "v_fmac_f32 v111, v43, v55\n\t" \
  "v_mfma_f32_16x16x32_bf16 v[240:243], a[204:207], a[0:3], v[240:243]\n\t" \
  "ds_read_b64_tr_b16 a[196:197], v25 offset:19456\n\t" \
  "ds_read_b64_tr_b16 a[198:199], v25 offset:19712\n\t" \
  "v_mfma_f32_16x16x32_bf16 v[244:247], a[204:207], a[4:7], v[244:247]\n\t" \
  "v_fmac_f32 v124, v43, v56\n\t" \
  "v_fmac_f32 v125, v43, v57\n\t" \
  "v_mfma_f32_16x16x32_bf16 v[248:251], a[204:207], a[8:11], v[248:251]\n\t" \
  "v_fmac_f32 v126, v43, v58\n\t" \
  "v_fmac_f32 v127, v43, v59\n\t" \
  "v_mfma_f32_16x16x32_bf16 v[252:255], a[204:207], a[12:15], v[252:255]\n\t" \
  "v_cvt_pk_bf16_f32 v108, v108, v109\n\t" \
  "s_waitcnt lgkmcnt(4)\n\t" \
  "v_mfma_f32_16x16x32_bf16 v[192:195], a[208:211], a[16:19], v[192:195]\n\t" \
  "ds_read_b64_tr_b16 a[200:201], v24 offset:14336\n\t" \
  "ds_read_b64_tr_b16 a[202:203], v24 offset:14592\n\t" \
  "v_mfma_f32_16x16x32_bf16 v[196:199], a[208:211], a[20:23], v[196:199]\n\t" \
  "v_cvt_pk_bf16_f32 v109, v110, v111\n\t" \
  "v_cvt_pk_bf16_f32 v110, v124, v125\n\t" \
  "v_mfma_f32_16x16x32_bf16 v[200:203], a[208:211], a[24:27], v[200:203]\n\t" \
  "v_cvt_pk_bf16_f32 v111, v126, v127\n\t" \
  "v_pk_max_i16 v108, v108, 0\n\t" \
  "v_mfma_f32_16x16x32_bf16 v[204:207], a[208:211], a[28:31], v[204:207]\n\t" \
  "v_pk_max_i16 v109, v109, 0\n\t" \
  "v_pk_max_i16 v110, v110, 0\n\t" \
  "v_mfma_f32_16x16x32_bf16 v[208:211], a[212:215], a[16:19], v[208:211]\n\t" \
  "ds_read_b64_tr_b16 a[204:205], v25 offset:14336\n\t" \
  "ds_read_b64_tr_b16 a[206:207], v25 offset:14592\n\t" \
  "v_mfma_f32_16x16x32_bf16 v[212:215], a[212:215], a[20:23], v[212:215]\n\t" \
  "v_pk_max_i16 v111, v111, 0\n\t" \
  "v_mfma_f32_16x16x32_bf16 v[216:219], a[212:215], a[24:27], v[216:219]\n\t" \
  "buffer_store_dwordx4 v[108:111], v32, %3, s67 offen offset:3072 sc1\n\t" \
  "ds_read_b128 v[52:55], v35 offset:384\n\t" \
  "v_mfma_f32_16x16x32_bf16 v[220:223], a[212:215], a[28:31], v[220:223]\n\t" \
  "ds_read_b128 v[56:59], v35 offset:448\n\t" \
  "s_add_u32 s67, s45, 8192\n\t" \
  "s_waitcnt lgkmcnt(6)\n\t" \
  "v_mfma_f32_16x16x32_bf16 v[224:227], a[192:195], a[16:19], v[224:227]\n\t" \
  "ds_read_b64_tr_b16 a[208:209], v24 offset:20480\n\t" \
  "ds_read_b64_tr_b16 a[210:211], v24 offset:20736\n\t" \
  "v_fmac_f32 v128, v40, v44\n\t" \
  "v_mfma_f32_16x16x32_bf16 v[228:231], a[192:195], a[20:23], v[228:231]\n\t" \
  "s_add_u32 m0, s40, 110592\n\t" \
  "s_add_u32 s43, s51, 147456\n\t" \
  "v_fmac_f32 v129, v40, v45\n\t" \
  "v_mfma_f32_16x16x32_bf16 v[232:235], a[192:195], a[24:27], v[232:235]\n\t" \
  "buffer_load_dwordx4 v17, %2, s43 offen offset:0 lds\n\t" \
  "v_fmac_f32 v130, v40, v46\n\t" \
  "v_mfma_f32_16x16x32_bf16 v[236:239], a[192:195], a[28:31], v[236:239]\n\t" \
  "v_fmac_f32 v131, v40, v47\n\t" \
  "v_fmac_f32 v144, v40, v48\n\t" \
  "v_fmac_f32 v145, v40, v49\n\t" \
  "v_mfma_f32_16x16x32_bf16 v[240:243], a[196:199], a[16:19], v[240:243]\n\t" \
  "ds_read_b64_tr_b16 a[212:213], v25 offset:20480\n\t" \
  "ds_read_b64_tr_b16 a[214:215], v25 offset:20736\n\t" \
  "v_fmac_f32 v146, v40, v50\n\t" \
  "v_mfma_f32_16x16x32_bf16 v[244:247], a[196:199], a[20:23], v[244:247]\n\t" \
  "buffer_load_dwordx4 v18, %2, s43 offen offset:1024 lds\n\t" \
  "v_fmac_f32 v147, v40, v51\n\t" \
  "v_mfma_f32_16x16x32_bf16 v[248:251], a[196:199], a[24:27], v[248:251]\n\t" \
  "v_cvt_pk_bf16_f32 v128, v128, v129\n\t" \
  "v_cvt_pk_bf16_f32 v129, v130, v131\n\t" \
  "v_cvt_pk_bf16_f32 v130, v144, v145\n\t" \
  "v_mfma_f32_16x16x32_bf16 v[252:255], a[196:199], a[28:31], v[252:255]\n\t" \
  "v_cvt_pk_bf16_f32 v131, v146, v147\n\t" \
  "s_waitcnt vmcnt(22) lgkmcnt(6)\n\t" \
  "s_barrier\n\t" \
  "v_mfma_f32_16x16x32_bf16 v[192:195], a[200:203], a[32:35], v[192:195]\n\t" \
  "ds_read_b64_tr_b16 a[192:193], v24 offset:24576\n\t" \
  "ds_read_b64_tr_b16 a[194:195], v24 offset:24832\n\t" \
  "v_pk_max_i16 v128, v128, 0\n\t" \
  "v_mfma_f32_16x16x32_bf16 v[196:199], a[200:203], a[36:39], v[196:199]\n\t" \
  "v_pk_max_i16 v129, v129, 0\n\t" \
  "v_pk_max_i16 v130, v130, 0\n\t" \
  "v_mfma_f32_16x16x32_bf16 v[200:203], a[200:203], a[40:43], v[200:203]\n\t" \
  "buffer_load_dwordx4 v19, %2, s43 offen offset:2048 lds\n\t" \
  "v_mfma_f32_16x16x32_bf16 v[204:207], a[200:203], a[44:47], v[204:207]\n\t" \
  "v_pk_max_i16 v131, v131, 0\n\t" \
  "v_mfma_f32_16x16x32_bf16 v[208:211], a[204:207], a[32:35], v[208:211]\n\t" \
  "ds_read_b64_tr_b16 a[196:197], v25 offset:24576\n\t" \
  "ds_read_b64_tr_b16 a[198:199], v25 offset:24832\n\t" \
  "v_mfma_f32_16x16x32_bf16 v[212:215], a[204:207], a[36:39], v[212:215]\n\t" \
  "buffer_store_dwordx4 v[128:131], v32, %3, s67 offen offset:0 sc1\n\t" \
  "v_fmac_f32 v132, v41, v44\n\t" \
  "v_mfma_f32_16x16x32_bf16 v[216:219], a[204:207], a[40:43], v[216:219]\n\t" \
  "v_fmac_f32 v133, v41, v45\n\t" \
  "v_fmac_f32 v134, v41, v46\n\t" \
  "v_mfma_f32_16x16x32_bf16 v[220:223], a[204:207], a[44:47], v[220:223]\n\t" \
  "v_fmac_f32 v135, v41, v47\n\t" \
  "s_waitcnt lgkmcnt(4)\n\t" \
  "v_mfma_f32_16x16x32_bf16 v[224:227], a[208:211], a[32:35], v[224:227]\n\t" \
  "ds_read_b64_tr_b16 a[200:201], v24 offset:30720\n\t" \
  "ds_read_b64_tr_b16 a[202:203], v24 offset:30976\n\t" \
  "v_mfma_f32_16x16x32_bf16 v[228:231], a[208:211], a[36:39], v[228:231]\n\t" \
  "v_fmac_f32 v148, v41, v48\n\t" \
  "v_fmac_f32 v149, v41, v49\n\t" \
  "v_mfma_f32_16x16x32_bf16 v[232:235], a[208:211], a[40:43], v[232:235]\n\t" \
  "v_fmac_f32 v150, v41, v50\n\t" \
  "v_fmac_f32 v151, v41, v51\n\t" \
  "v_mfma_f32_16x16x32_bf16 v[236:239], a[208:211], a[44:47], v[236:239]\n\t" \
  "v_cvt_pk_bf16_f32 v132, v132, v133\n\t" \
  "v_cvt_pk_bf16_f32 v133, v134, v135\n\t" \
  "v_mfma_f32_16x16x32_bf16 v[240:243], a[212:215], a[32:35], v[240:243]\n\t" \
  "ds_read_b64_tr_b16 a[204:205], v25 offset:30720\n\t" \
  "ds_read_b64_tr_b16 a[206:207], v25 offset:30976\n\t" \
  "v_mfma_f32_16x16x32_bf16 v[244:247], a[212:215], a[36:39], v[244:247]\n\t" \
  "v_cvt_pk_bf16_f32 v134, v148, v149\n\t" \
  "v_cvt_pk_bf16_f32 v135, v150, v151\n\t" \
  "v_mfma_f32_16x16x32_bf16 v[248:251], a[212:215], a[40:43], v[248:251]\n\t" \
  "v_pk_max_i16 v132, v132, 0\n\t" \
  "v_pk_max_i16 v133, v133, 0\n\t" \
  "v_mfma_f32_16x16x32_bf16 v[252:255], a[212:215], a[44:47], v[252:255]\n\t" \
  "v_pk_max_i16 v134, v134, 0\n\t" \
  "s_waitcnt lgkmcnt(4)\n\t" \
  "v_mfma_f32_16x16x32_bf16 v[192:195], a[192:195], a[48:51], v[192:195]\n\t" \
  "ds_read_b64_tr_b16 a[208:209], v24 offset:25600\n\t" \
  "ds_read_b64_tr_b16 a[210:211], v24 offset:25856\n\t" \
  "v_mfma_f32_16x16x32_bf16 v[196:199], a[192:195], a[52:55], v[196:199]\n\t" \
  "v_pk_max_i16 v135, v135, 0\n\t" \
  "v_mfma_f32_16x16x32_bf16 v[200:203], a[192:195], a[56:59], v[200:203]\n\t" \
  "buffer_store_dwordx4 v[132:135], v32, %3, s67 offen offset:1024 sc1\n\t" \
  "v_fmac_f32 v136, v42, v44\n\t" \
  "v_mfma_f32_16x16x32_bf16 v[204:207], a[192:195], a[60:63], v[204:207]\n\t" \
  "v_fmac_f32 v137, v42, v45\n\t" \
  "v_fmac_f32 v138, v42, v46\n\t" \
  "v_mfma_f32_16x16x32_bf16 v[208:211], a[196:199], a[48:51], v[208:211]\n\t" \
  "ds_read_b64_tr_b16 a[212:213], v25 offset:25600\n\t" \
  "ds_read_b64_tr_b16 a[214:215], v25 offset:25856\n\t" \
  "v_mfma_f32_16x16x32_bf16 v[212:215], a[196:199], a[52:55], v[212:215]\n\t" \
  "v_fmac_f32 v139, v42, v47\n\t" \
  "v_fmac_f32 v152, v42, v48\n\t" \
  "v_mfma_f32_16x16x32_bf16 v[216:219], a[196:199], a[56:59], v[216:219]\n\t" \
  "v_fmac_f32 v153, v42, v49\n\t" \
  "v_fmac_f32 v154, v42, v50\n\t" \
  "v_mfma_f32_16x16x32_bf16 v[220:223], a[196:199], a[60:63], v[220:223]\n\t" \
  "v_fmac_f32 v155, v42, v51\n\t" \
  "s_waitcnt lgkmcnt(4)\n\t" \
  "v_mfma_f32_16x16x32_bf16 v[224:227], a[200:203], a[48:51], v[224:227]\n\t" \
  "ds_read_b64_tr_b16 a[192:193], v24 offset:31744\n\t" \
  "ds_read_b64_tr_b16 a[194:195], v24 offset:32000\n\t" \
  "v_mfma_f32_16x16x32_bf16 v[228:231], a[200:203], a[52:55], v[228:231]\n\t" \
  "v_cvt_pk_bf16_f32 v136, v136, v137\n\t" \
  "v_cvt_pk_bf16_f32 v137, v138, v139\n\t" \
  "v_mfma_f32_16x16x32_bf16 v[232:235], a[200:203], a[56:59], v[232:235]\n\t" \
  "v_cvt_pk_bf16_f32 v138, v152, v153\n\t" \
  "v_cvt_pk_bf16_f32 v139, v154, v155\n\t" \
  "v_mfma_f32_16x16x32_bf16 v[236:239], a[200:203], a[60:63], v[236:239]\n\t" \
  "v_pk_max_i16 v136, v136, 0\n\t" \
  "v_pk_max_i16 v137, v137, 0\n\t" \
  "v_mfma_f32_16x16x32_bf16 v[240:243], a[204:207], a[48:51], v[240:243]\n\t" \
  "ds_read_b64_tr_b16 a[196:197], v25 offset:31744\n\t" \
  "ds_read_b64_tr_b16 a[198:199], v25 offset:32000\n\t" \
  "v_mfma_f32_16x16x32_bf16 v[244:247], a[204:207], a[52:55], v[244:247]\n\t" \
  "v_pk_max_i16 v138, v138, 0\n\t" \
  "v_pk_max_i16 v139, v139, 0\n\t" \
  "v_mfma_f32_16x16x32_bf16 v[248:251], a[204:207], a[56:59], v[248:251]\n\t" \
  "buffer_store_dwordx4 v[136:139], v32, %3, s67 offen offset:2048 sc1\n\t" \
  "v_mfma_f32_16x16x32_bf16 v[252:255], a[204:207], a[60:63], v[252:255]\n\t" \
  "v_fmac_f32 v140, v43, v44\n\t" \
  "s_waitcnt lgkmcnt(4)\n\t" \
  "v_mfma_f32_16x16x32_bf16 v[192:195], a[208:211], a[64:67], v[192:195]\n\t" \
  "ds_read_b64_tr_b16 a[200:201], v24 offset:26624\n\t" \
  "ds_read_b64_tr_b16 a[202:203], v24 offset:26880\n\t" \
  "v_mfma_f32_16x16x32_bf16 v[196:199], a[208:211], a[68:71], v[196:199]\n\t" \
  "v_fmac_f32 v141, v43, v45\n\t" \
  "v_fmac_f32 v142, v43, v46\n\t" \
  "v_mfma_f32_16x16x32_bf16 v[200:203], a[208:211], a[72:75], v[200:203]\n\t" \
  "v_fmac_f32 v143, v43, v47\n\t" \
  "v_fmac_f32 v156, v43, v48\n\t" \
  "v_mfma_f32_16x16x32_bf16 v[204:207], a[208:211], a[76:79], v[204:207]\n\t" \
  "v_fmac_f32 v157, v43, v49\n\t" \
  "v_fmac_f32 v158, v43, v50\n\t" \
  "v_mfma_f32_16x16x32_bf16 v[208:211], a[212:215], a[64:67], v[208:211]\n\t" \
  "ds_read_b64_tr_b16 a[204:205], v25 offset:26624\n\t" \
  "ds_read_b64_tr_b16 a[206:207], v25 offset:26880\n\t" \
  "v_mfma_f32_16x16x32_bf16 v[212:215], a[212:215], a[68:71], v[212:215]\n\t" \
  "v_fmac_f32 v159, v43, v51\n\t" \
  "v_cvt_pk_bf16_f32 v140, v140, v141\n\t" \
  "v_mfma_f32_16x16x32_bf16 v[216:219], a[212:215], a[72:75], v[216:219]\n\t" \
  "v_cvt_pk_bf16_f32 v141, v142, v143\n\t" \
  "v_cvt_pk_bf16_f32 v142, v156, v157\n\t" \
  "v_mfma_f32_16x16x32_bf16 v[220:223], a[212:215], a[76:79], v[220:223]\n\t" \
  "v_cvt_pk_bf16_f32 v143, v158, v159\n\t" \
  "s_waitcnt lgkmcnt(4)\n\t" \
  "v_mfma_f32_16x16x32_bf16 v[224:227], a[192:195], a[64:67], v[224:227]\n\t" \
  "ds_read_b64_tr_b16 a[208:209], v24 offset:32768\n\t" \
  "ds_read_b64_tr_b16 a[210:211], v24 offset:33024\n\t" \
  "v_mfma_f32_16x16x32_bf16 v[228:231], a[192:195], a[68:71], v[228:231]\n\t" \
  "s_add_u32 m0, s40, 122880\n\t" \
  "s_add_u32 s43, s51, 184320\n\t" \
  "v_mfma_f32_16x16x32_bf16 v[232:235], a[192:195], a[72:75], v[232:235]\n\t" \
  "buffer_load_dwordx4 v17, %2, s43 offen offset:0 lds\n\t" \
  "v_mfma_f32_16x16x32_bf16 v[236:239], a[192:195], a[76:79], v[236:239]\n\t" \
  "v_pk_max_i16 v140, v140, 0\n\t" \
  "v_pk_max_i16 v141, v141, 0\n\t" \
  "v_mfma_f32_16x16x32_bf16 v[240:243], a[196:199], a[64:67], v[240:243]\n\t" \
  "ds_read_b64_tr_b16 a[212:213], v25 offset:32768\n\t" \
  "ds_read_b64_tr_b16 a[214:215], v25 offset:33024\n\t" \
  "v_mfma_f32_16x16x32_bf16 v[244:247], a[196:199], a[68:71], v[244:247]\n\t" \
  "buffer_load_dwordx4 v18, %2, s43 offen offset:1024 lds\n\t" \
  "v_mfma_f32_16x16x32_bf16 v[248:251], a[196:199], a[72:75], v[248:251]\n\t" \
  "v_pk_max_i16 v142, v142, 0\n\t" \
  "v_pk_max_i16 v143, v143, 0\n\t" \
  "v_mfma_f32_16x16x32_bf16 v[252:255], a[196:199], a[76:79], v[252:255]\n\t" \
  "s_waitcnt vmcnt(25) lgkmcnt(4)\n\t" \
  "s_barrier\n\t" \
  "v_mfma_f32_16x16x32_bf16 v[192:195], a[200:203], a[80:83], v[192:195]\n\t" \
  "ds_read_b64_tr_b16 a[192:193], v26 offset:0\n\t" \
  "ds_read_b64_tr_b16 a[194:195], v26 offset:3072\n\t" \
  "v_mfma_f32_16x16x32_bf16 v[196:199], a[200:203], a[84:87], v[196:199]\n\t" \
  "buffer_store_dwordx4 v[140:143], v32, %3, s67 offen offset:3072 sc1\n\t" \
  "v_mfma_f32_16x16x32_bf16 v[200:203], a[200:203], a[88:91], v[200:203]\n\t" \
  "ds_read_b128 v[44:47], v35 offset:512\n\t" \
  "ds_read_b128 v[48:51], v35 offset:576\n\t" \
  "s_add_u32 s67, s45, 12288\n\t" \
  "v_mfma_f32_16x16x32_bf16 v[204:207], a[200:203], a[92:95], v[204:207]\n\t" \
  "buffer_load_dwordx4 v19, %2, s43 offen offset:2048 lds\n\t" \
  "v_fmac_f32 v160, v40, v52\n\t" \
  "v_mfma_f32_16x16x32_bf16 v[208:211], a[204:207], a[80:83], v[208:211]\n\t" \
  "ds_read_b64_tr_b16 a[196:197], v27 offset:0\n\t" \
  "ds_read_b64_tr_b16 a[198:199], v27 offset:3072\n\t" \
  "v_fmac_f32 v161, v40, v53\n\t" \
  "v_mfma_f32_16x16x32_bf16 v[212:215], a[204:207], a[84:87], v[212:215]\n\t" \
  "v_fmac_f32 v162, v40, v54\n\t" \
  "v_fmac_f32 v163, v40, v55\n\t" \
  "v_fmac_f32 v176, v40, v56\n\t" \
  "v_mfma_f32_16x16x32_bf16 v[216:219], a[204:207], a[88:91], v[216:219]\n\t" \
  "v_fmac_f32 v177, v40, v57\n\t" \
  "v_fmac_f32 v178, v40, v58\n\t" \
  "v_fmac_f32 v179, v40, v59\n\t" \
  "v_mfma_f32_16x16x32_bf16 v[220:223], a[204:207], a[92:95], v[220:223]\n\t" \
  "v_cvt_pk_bf16_f32 v160, v160, v161\n\t" \
  "v_cvt_pk_bf16_f32 v161, v162, v163\n\t" \
  "s_waitcnt lgkmcnt(6)\n\t" \
  "v_mfma_f32_16x16x32_bf16 v[224:227], a[208:211], a[80:83], v[224:227]\n\t" \
  "ds_read_b64_tr_b16 a[200:201], v26 offset:6144\n\t" \
  "ds_read_b64_tr_b16 a[202:203], v26 offset:9216\n\t" \
  "v_cvt_pk_bf16_f32 v162, v176, v177\n\t" \
  "v_mfma_f32_16x16x32_bf16 v[228:231], a[208:211], a[84:87], v[228:231]\n\t" \
  "v_cvt_pk_bf16_f32 v163, v178, v179\n\t" \
  "v_pk_max_i16 v160, v160, 0\n\t" \
  "v_mfma_f32_16x16x32_bf16 v[232:235], a[208:211], a[88:91], v[232:235]\n\t" \
  "v_pk_max_i16 v161, v161, 0\n\t" \
  "v_pk_max_i16 v162, v162, 0\n\t" \
  "v_mfma_f32_16x16x32_bf16 v[236:239], a[208:211], a[92:95], v[236:239]\n\t" \
  "v_pk_max_i16 v163, v163, 0\n\t" \
  "v_mfma_f32_16x16x32_bf16 v[240:243], a[212:215], a[80:83], v[240:243]\n\t" \
  "ds_read_b64_tr_b16 a[204:205], v27 offset:6144\n\t" \
  "ds_read_b64_tr_b16 a[206:207], v27 offset:9216\n\t" \
  "v_mfma_f32_16x16x32_bf16 v[244:247], a[212:215], a[84:87], v[244:247]\n\t" \
  "buffer_store_dwordx4 v[160:163], v32, %3, s67 offen offset:0 sc1\n\t" \
  "v_fmac_f32 v164, v41, v52\n\t" \
  "v_mfma_f32_16x16x32_bf16 v[248:251], a[212:215], a[88:91], v[248:251]\n\t" \
  "v_fmac_f32 v165, v41, v53\n\t" \
  "v_fmac_f32 v166, v41, v54\n\t" \
  "v_mfma_f32_16x16x32_bf16 v[252:255], a[212:215], a[92:95], v[252:255]\n\t" \
  "v_fmac_f32 v167, v41, v55\n\t" \
  "s_waitcnt lgkmcnt(4)\n\t" \
  "v_mfma_f32_16x16x32_bf16 a[0:3], a[192:195], v[64:67], 0\n\t" \
  "ds_read_b64_tr_b16 a[208:209], v26 offset:1024\n\t" \
  "ds_read_b64_tr_b16 a[210:211], v26 offset:4096\n\t" \
  "v_mfma_f32_16x16x32_bf16 a[4:7], a[192:195], v[68:71], 0\n\t" \
  "v_fmac_f32 v180, v41, v56\n\t" \
  "v_fmac_f32 v181, v41, v57\n\t" \
  "v_mfma_f32_16x16x32_bf16 a[8:11], a[192:195], v[72:75], 0\n\t" \
  "v_fmac_f32 v182, v41, v58\n\t" \
  "v_fmac_f32 v183, v41, v59\n\t" \
  "v_mfma_f32_16x16x32_bf16 a[12:15], a[192:195], v[76:79], 0\n\t" \
  "v_cvt_pk_bf16_f32 v164, v164, v165\n\t" \
  "v_cvt_pk_bf16_f32 v165, v166, v167\n\t" \
  "v_mfma_f32_16x16x32_bf16 a[16:19], a[196:199], v[64:67], 0\n\t" \
  "ds_read_b64_tr_b16 a[212:213], v27 offset:1024\n\t" \
  "ds_read_b64_tr_b16 a[214:215], v27 offset:4096\n\t" \
  "v_mfma_f32_16x16x32_bf16 a[20:23], a[196:199], v[68:71], 0\n\t" \
  "v_cvt_pk_bf16_f32 v166, v180, v181\n\t" \
  "v_cvt_pk_bf16_f32 v167, v182, v183\n\t" \
  "v_mfma_f32_16x16x32_bf16 a[24:27], a[196:199], v[72:75], 0\n\t" \
  "v_pk_max_i16 v164, v164, 0\n\t" \
  "v_pk_max_i16 v165, v165, 0\n\t" \
  "v_mfma_f32_16x16x32_bf16 a[28:31], a[196:199], v[76:79], 0\n\t" \
  "v_pk_max_i16 v166, v166, 0\n\t" \
  "s_waitcnt lgkmcnt(4)\n\t" \
  "v_mfma_f32_16x16x32_bf16 a[32:35], a[200:203], v[64:67], 0\n\t" \
  "ds_read_b64_tr_b16 a[192:193], v26 offset:7168\n\t" \
  "ds_read_b64_tr_b16 a[194:195], v26 offset:10240\n\t" \
  "v_mfma_f32_16x16x32_bf16 a[36:39], a[200:203], v[68:71], 0\n\t" \
  "v_pk_max_i16 v167, v167, 0\n\t" \
  "v_mfma_f32_16x16x32_bf16 a[40:43], a[200:203], v[72:75], 0\n\t" \
  "buffer_store_dwordx4 v[164:167], v32, %3, s67 offen offset:1024 sc1\n\t" \
  "v_fmac_f32 v168, v42, v52\n\t" \
  "v_mfma_f32_16x16x32_bf16 a[44:47], a[200:203], v[76:79], 0\n\t" \
  "v_fmac_f32 v169, v42, v53\n\t" \
  "v_fmac_f32 v170, v42, v54\n\t" \
  "v_mfma_f32_16x16x32_bf16 a[48:51], a[204:207], v[64:67], 0\n\t" \
  "ds_read_b64_tr_b16 a[196:197], v27 offset:7168\n\t" \
  "ds_read_b64_tr_b16 a[198:199], v27 offset:10240\n\t" \
  "v_mfma_f32_16x16x32_bf16 a[52:55], a[204:207], v[68:71], 0\n\t" \
  "v_fmac_f32 v171, v42, v55\n\t" \
  "v_fmac_f32 v184, v42, v56\n\t" \
  "v_mfma_f32_16x16x32_bf16 a[56:59], a[204:207], v[72:75], 0\n\t" \
  "v_fmac_f32 v185, v42, v57\n\t" \
  "v_fmac_f32 v186, v42, v58\n\t" \
  "v_mfma_f32_16x16x32_bf16 a[60:63], a[204:207], v[76:79], 0\n\t" \
  "v_fmac_f32 v187, v42, v59\n\t" \
  "s_waitcnt lgkmcnt(4)\n\t" \
  "v_mfma_f32_16x16x32_bf16 a[64:67], a[208:211], v[64:67], 0\n\t" \
  "ds_read_b64_tr_b16 a[200:201], v26 offset:2048\n\t" \
  "ds_read_b64_tr_b16 a[202:203], v26 offset:5120\n\t" \
  "v_mfma_f32_16x16x32_bf16 a[68:71], a[208:211], v[68:71], 0\n\t" \
  "v_cvt_pk_bf16_f32 v168, v168, v169\n\t" \
  "v_cvt_pk_bf16_f32 v169, v170, v171\n\t" \
  "v_mfma_f32_16x16x32_bf16 a[72:75], a[208:211], v[72:75], 0\n\t" \
  "v_cvt_pk_bf16_f32 v170, v184, v185\n\t" \
  "v_cvt_pk_bf16_f32 v171, v186, v187\n\t" \
  "v_mfma_f32_16x16x32_bf16 a[76:79], a[208:211], v[76:79], 0\n\t" \
  "v_pk_max_i16 v168, v168, 0\n\t" \
  "v_pk_max_i16 v169, v169, 0\n\t" \
  "v_mfma_f32_16x16x32_bf16 a[80:83], a[212:215], v[64:67], 0\n\t" \
  "ds_read_b64_tr_b16 a[204:205], v27 offset:2048\n\t" \
  "ds_read_b64_tr_b16 a[206:207], v27 offset:5120\n\t" \
  "v_mfma_f32_16x16x32_bf16 a[84:87], a[212:215], v[68:71], 0\n\t" \
  "v_pk_max_i16 v170, v170, 0\n\t" \
  "v_pk_max_i16 v171, v171, 0\n\t" \
  "v_mfma_f32_16x16x32_bf16 a[88:91], a[212:215], v[72:75], 0\n\t" \
  "buffer_store_dwordx4 v[168:171], v32, %3, s67 offen offset:2048 sc1\n\t" \
  "v_mfma_f32_16x16x32_bf16 a[92:95], a[212:215], v[76:79], 0\n\t" \
  "v_fmac_f32 v172, v43, v52\n\t" \
  "s_waitcnt lgkmcnt(4)\n\t" \
  "v_mfma_f32_16x16x32_bf16 a[96:99], a[192:195], v[64:67], 0\n\t" \
  "ds_read_b64_tr_b16 a[208:209], v26 offset:8192\n\t" \
  "ds_read_b64_tr_b16 a[210:211], v26 offset:11264\n\t" \
  "v_mfma_f32_16x16x32_bf16 a[100:103], a[192:195], v[68:71], 0\n\t" \
  "s_add_u32 m0, s40, 135168\n\t" \
  "s_add_u32 s43, s52, 0\n\t" \
  "v_mfma_f32_16x16x32_bf16 a[104:107], a[192:195], v[72:75], 0\n\t" \
  "buffer_load_dwordx4 v17, %2, s43 offen offset:0 lds\n\t" \
  "v_mfma_f32_16x16x32_bf16 a[108:111], a[192:195], v[76:79], 0\n\t" \
  "v_fmac_f32 v173, v43, v53\n\t" \
  "v_fmac_f32 v174, v43, v54\n\t" \
  "v_mfma_f32_16x16x32_bf16 a[112:115], a[196:199], v[64:67], 0\n\t" \
  "ds_read_b64_tr_b16 a[212:213], v27 offset:8192\n\t" \
  "ds_read_b64_tr_b16 a[214:215], v27 offset:11264\n\t" \
  "v_mfma_f32_16x16x32_bf16 a[116:119], a[196:199], v[68:71], 0\n\t" \
  "buffer_load_dwordx4 v18, %2, s43 offen offset:1024 lds\n\t" \
  "v_mfma_f32_16x16x32_bf16 a[120:123], a[196:199], v[72:75], 0\n\t" \
  "v_fmac_f32 v175, v43, v55\n\t" \
  "v_fmac_f32 v188, v43, v56\n\t" \
  "v_mfma_f32_16x16x32_bf16 a[124:127], a[196:199], v[76:79], 0\n\t" \
  "s_waitcnt vmcnt(28) lgkmcnt(4)\n\t" \
  "s_barrier\n\t" \
  "v_mfma_f32_16x16x32_bf16 a[128:131], a[200:203], v[64:67], 0\n\t" \
  "ds_read_b64_tr_b16 a[192:193], v26 offset:12288\n\t" \
  "ds_read_b64_tr_b16 a[194:195], v26 offset:15360\n\t" \
  "v_mfma_f32_16x16x32_bf16 a[132:135], a[200:203], v[68:71], 0\n\t" \
  "v_fmac_f32 v189, v43, v57\n\t" \
  "v_fmac_f32 v190, v43, v58\n\t" \
  "v_mfma_f32_16x16x32_bf16 a[136:139], a[200:203], v[72:75], 0\n\t" \
  "buffer_load_dwordx4 v19, %2, s43 offen offset:2048 lds\n\t" \
  "v_mfma_f32_16x16x32_bf16 a[140:143], a[200:203], v[76:79], 0\n\t" \
  "v_fmac_f32 v191, v43, v59\n\t" \
  "v_cvt_pk_bf16_f32 v172, v172, v173\n\t" \
  "v_mfma_f32_16x16x32_bf16 a[144:147], a[204:207], v[64:67], 0\n\t" \
  "ds_read_b64_tr_b16 a[196:197], v27 offset:12288\n\t" \
  "ds_read_b64_tr_b16 a[198:199], v27 offset:15360\n\t" \
  "v_mfma_f32_16x16x32_bf16 a[148:151], a[204:207], v[68:71], 0\n\t" \
  "v_cvt_pk_bf16_f32 v173, v174, v175\n\t" \
  "v_cvt_pk_bf16_f32 v174, v188, v189\n\t" \
  "v_mfma_f32_16x16x32_bf16 a[152:155], a[204:207], v[72:75], 0\n\t" \
  "v_cvt_pk_bf16_f32 v175, v190, v191\n\t" \
  "v_pk_max_i16 v172, v172, 0\n\t" \
  "v_mfma_f32_16x16x32_bf16 a[156:159], a[204:207], v[76:79], 0\n\t" \
  "v_pk_max_i16 v173, v173, 0\n\t" \
  "s_waitcnt lgkmcnt(4)\n\t" \
  "v_mfma_f32_16x16x32_bf16 a[160:163], a[208:211], v[64:67], 0\n\t" \
  "ds_read_b64_tr_b16 a[200:201], v26 offset:18432\n\t" \
  "ds_read_b64_tr_b16 a[202:203], v26 offset:21504\n\t" \
  "v_mfma_f32_16x16x32_bf16 a[164:167], a[208:211], v[68:71], 0\n\t" \
  "v_pk_max_i16 v174, v174, 0\n\t" \
  "v_pk_max_i16 v175, v175, 0\n\t" \
  "v_mfma_f32_16x16x32_bf16 a[168:171], a[208:211], v[72:75], 0\n\t" \
  "buffer_store_dwordx4 v[172:175], v32, %3, s67 offen offset:3072 sc1\n\t" \
  "v_mfma_f32_16x16x32_bf16 a[172:175], a[208:211], v[76:79], 0\n\t" \
  "ds_read_b128 v[52:55], v35 offset:640\n\t" \
  "ds_read_b128 v[56:59], v35 offset:704\n\t" \
  "s_add_u32 s67, s45, 16384\n\t" \
  "v_mfma_f32_16x16x32_bf16 a[176:179], a[212:215], v[64:67], 0\n\t" \
  "ds_read_b64_tr_b16 a[204:205], v27 offset:18432\n\t" \
  "ds_read_b64_tr_b16 a[206:207], v27 offset:21504\n\t" \
  "v_fmac_f32 v192, v40, v44\n\t" \
  "v_mfma_f32_16x16x32_bf16 a[180:183], a[212:215], v[68:71], 0\n\t" \
  "v_fmac_f32 v193, v40, v45\n\t" \
  "v_fmac_f32 v194, v40, v46\n\t" \
  "v_fmac_f32 v195, v40, v47\n\t" \
  "v_mfma_f32_16x16x32_bf16 a[184:187], a[212:215], v[72:75], 0\n\t" \
  "v_fmac_f32 v208, v40, v48\n\t" \
  "v_fmac_f32 v209, v40, v49\n\t" \
  "v_fmac_f32 v210, v40, v50\n\t" \
  "v_mfma_f32_16x16x32_bf16 a[188:191], a[212:215], v[76:79], 0\n\t" \
  "v_fmac_f32 v211, v40, v51\n\t" \
  "v_cvt_pk_bf16_f32 v192, v192, v193\n\t" \
  "s_waitcnt lgkmcnt(6)\n\t" \
  "v_mfma_f32_16x16x32_bf16 a[0:3], a[192:195], v[96:99], a[0:3]\n\t" \
  "ds_read_b64_tr_b16 a[208:209], v26 offset:13312\n\t" \
  "ds_read_b64_tr_b16 a[210:211], v26 offset:16384\n\t" \
  "v_cvt_pk_bf16_f32 v193, v194, v195\n\t" \
  "v_mfma_f32_16x16x32_bf16 a[4:7], a[192:195], v[100:103], a[4:7]\n\t" \
  "v_subrev_u32 v24, 61440, v26\n\t" \
  "v_subrev_u32 v25, 61440, v27\n\t" \
  "v_cvt_pk_bf16_f32 v194, v208, v209\n\t" \
  "v_mfma_f32_16x16x32_bf16 a[8:11], a[192:195], v[104:107], a[8:11]\n\t" \
  "v_cvt_pk_bf16_f32 v195, v210, v211\n\t" \
  "v_pk_max_i16 v192, v192, 0\n\t" \
  "v_pk_max_i16 v193, v193, 0\n\t" \
  "v_mfma_f32_16x16x32_bf16 a[12:15], a[192:195], v[108:111], a[12:15]\n\t" \
  "v_pk_max_i16 v194, v194, 0\n\t" \
  "v_pk_max_i16 v195, v195, 0\n\t" \
  "v_mfma_f32_16x16x32_bf16 a[16:19], a[196:199], v[96:99], a[16:19]\n\t" \
  "ds_read_b64_tr_b16 a[212:213], v27 offset:13312\n\t" \
  "ds_read_b64_tr_b16 a[214:215], v27 offset:16384\n\t" \
  "v_mfma_f32_16x16x32_bf16 a[20:23], a[196:199], v[100:103], a[20:23]\n\t" \
  "buffer_store_dwordx4 v[192:195], v32, %3, s67 offen offset:0 sc1\n\t" \
  "v_fmac_f32 v196, v41, v44\n\t" \
  "v_mfma_f32_16x16x32_bf16 a[24:27], a[196:199], v[104:107], a[24:27]\n\t" \
  "v_fmac_f32 v197, v41, v45\n\t" \
  "v_fmac_f32 v198, v41, v46\n\t" \
  "v_fmac_f32 v199, v41, v47\n\t" \
  "v_mfma_f32_16x16x32_bf16 a[28:31], a[196:199], v[108:111], a[28:31]\n\t" \
  "v_fmac_f32 v212, v41, v48\n\t" \
  "v_fmac_f32 v213, v41, v49\n\t" \
  "s_waitcnt lgkmcnt(4)\n\t" \
  "v_mfma_f32_16x16x32_bf16 a[32:35], a[200:203], v[96:99], a[32:35]\n\t" \
  "ds_read_b64_tr_b16 a[192:193], v26 offset:19456\n\t" \
  "ds_read_b64_tr_b16 a[194:195], v26 offset:22528\n\t" \
  "v_fmac_f32 v214, v41, v50\n\t" \
  "v_mfma_f32_16x16x32_bf16 a[36:39], a[200:203], v[100:103], a[36:39]\n\t" \
  "v_fmac_f32 v215, v41, v51\n\t" \
  "v_cvt_pk_bf16_f32 v196, v196, v197\n\t" \
  "v_cvt_pk_bf16_f32 v197, v198, v199\n\t" \
  "v_mfma_f32_16x16x32_bf16 a[40:43], a[200:203], v[104:107], a[40:43]\n\t" \
  "v_cvt_pk_bf16_f32 v198, v212, v213\n\t" \
  "v_cvt_pk_bf16_f32 v199, v214, v215\n\t" \
  "v_pk_max_i16 v196, v196, 0\n\t" \
  "v_mfma_f32_16x16x32_bf16 a[44:47], a[200:203], v[108:111], a[44:47]\n\t" \
  "v_pk_max_i16 v197, v197, 0\n\t" \
  "v_pk_max_i16 v198, v198, 0\n\t" \
  "v_pk_max_i16 v199, v199, 0\n\t" \
  "v_mfma_f32_16x16x32_bf16 a[48:51], a[204:207], v[96:99], a[48:51]\n\t" \
  "ds_read_b64_tr_b16 a[196:197], v27 offset:19456\n\t" \
  "ds_read_b64_tr_b16 a[198:199], v27 offset:22528\n\t" \
  "v_mfma_f32_16x16x32_bf16 a[52:55], a[204:207], v[100:103], a[52:55]\n\t" \
  "buffer_store_dwordx4 v[196:199], v32, %3, s67 offen offset:1024 sc1\n\t" \
  "v_fmac_f32 v200, v42, v44\n\t" \
  "v_mfma_f32_16x16x32_bf16 a[56:59], a[204:207], v[104:107], a[56:59]\n\t" \
  "v_fmac_f32 v201, v42, v45\n\t" \
  "v_fmac_f32 v202, v42, v46\n\t" \
  "v_fmac_f32 v203, v42, v47\n\t" \
  "v_mfma_f32_16x16x32_bf16 a[60:63], a[204:207], v[108:111], a[60:63]\n\t" \
  "v_fmac_f32 v216, v42, v48\n\t" \
  "v_fmac_f32 v217, v42, v49\n\t" \
  "s_waitcnt lgkmcnt(4)\n\t" \
  "v_mfma_f32_16x16x32_bf16 a[64:67], a[208:211], v[96:99], a[64:67]\n\t" \
  "ds_read_b64_tr_b16 a[200:201], v26 offset:14336\n\t" \
  "ds_read_b64_tr_b16 a[202:203], v26 offset:17408\n\t" \
  "v_fmac_f32 v218, v42, v50\n\t" \
  "v_mfma_f32_16x16x32_bf16 a[68:71], a[208:211], v[100:103], a[68:71]\n\t" \
  "v_fmac_f32 v219, v42, v51\n\t" \
  "v_cvt_pk_bf16_f32 v200, v200, v201\n\t" \
  "v_cvt_pk_bf16_f32 v201, v202, v203\n\t" \
  "v_mfma_f32_16x16x32_bf16 a[72:75], a[208:211], v[104:107], a[72:75]\n\t" \
  "v_cvt_pk_bf16_f32 v202, v216, v217\n\t" \
  "v_cvt_pk_bf16_f32 v203, v218, v219\n\t" \
  "v_pk_max_i16 v200, v200, 0\n\t" \
  "v_mfma_f32_16x16x32_bf16 a[76:79], a[208:211], v[108:111], a[76:79]\n\t" \
  "v_pk_max_i16 v201, v201, 0\n\t" \
  "v_pk_max_i16 v202, v202, 0\n\t" \
  "v_pk_max_i16 v203, v203, 0\n\t" \
  "v_mfma_f32_16x16x32_bf16 a[80:83], a[212:215], v[96:99], a[80:83]\n\t" \
  "ds_read_b64_tr_b16 a[204:205], v27 offset:14336\n\t" \
  "ds_read_b64_tr_b16 a[206:207], v27 offset:17408\n\t" \
  "v_mfma_f32_16x16x32_bf16 a[84:87], a[212:215], v[100:103], a[84:87]\n\t" \
  "buffer_store_dwordx4 v[200:203], v32, %3, s67 offen offset:2048 sc1\n\t" \
  "v_fmac_f32 v204, v43, v44\n\t" \
  "v_mfma_f32_16x16x32_bf16 a[88:91], a[212:215], v[104:107], a[88:91]\n\t" \
  "v_fmac_f32 v205, v43, v45\n\t" \
  "v_fmac_f32 v206, v43, v46\n\t" \
  "v_fmac_f32 v207, v43, v47\n\t" \
  "v_mfma_f32_16x16x32_bf16 a[92:95], a[212:215], v[108:111], a[92:95]\n\t" \
  "v_fmac_f32 v220, v43, v48\n\t" \
  "v_fmac_f32 v221, v43, v49\n\t" \
  "s_waitcnt lgkmcnt(4)\n\t" \
  "v_mfma_f32_16x16x32_bf16 a[96:99], a[192:195], v[96:99], a[96:99]\n\t" \
  "ds_read_b64_tr_b16 a[208:209], v26 offset:20480\n\t" \
  "ds_read_b64_tr_b16 a[210:211], v26 offset:23552\n\t" \
  "v_fmac_f32 v222, v43, v50\n\t" \
  "v_mfma_f32_16x16x32_bf16 a[100:103], a[192:195], v[100:103], a[100:103]\n\t" \
  "s_add_u32 m0, s40, 147456\n\t" \
  "s_add_u32 s43, s52, 36864\n\t" \
  "v_fmac_f32 v223, v43, v51\n\t" \
  "v_mfma_f32_16x16x32_bf16 a[104:107], a[192:195], v[104:107], a[104:107]\n\t" \
  "buffer_load_dwordx4 v17, %2, s43 offen offset:0 lds\n\t" \
  "v_cvt_pk_bf16_f32 v204, v204, v205\n\t" \
  "v_mfma_f32_16x16x32_bf16 a[108:111], a[192:195], v[108:111], a[108:111]\n\t" \
  "v_cvt_pk_bf16_f32 v205, v206, v207\n\t" \
  "v_cvt_pk_bf16_f32 v206, v220, v221\n\t" \
  "v_cvt_pk_bf16_f32 v207, v222, v223\n\t" \
  "v_mfma_f32_16x16x32_bf16 a[112:115], a[196:199], v[96:99], a[112:115]\n\t" \
  "ds_read_b64_tr_b16 a[212:213], v27 offset:20480\n\t" \
  "ds_read_b64_tr_b16 a[214:215], v27 offset:23552\n\t" \
  "v_mfma_f32_16x16x32_bf16 a[116:119], a[196:199], v[100:103], a[116:119]\n\t" \
  "buffer_load_dwordx4 v18, %2, s43 offen offset:1024 lds\n\t" \
  "v_mfma_f32_16x16x32_bf16 a[120:123], a[196:199], v[104:107], a[120:123]\n\t" \
  "s_lshl_b32 s54, %4, 3\n\t" \
  "s_add_u32 s54, s54, %5\n\t" \
  "s_lshl_b32 s54, s54, 1\n\t" \
  "v_mfma_f32_16x16x32_bf16 a[124:127], a[196:199], v[108:111], a[124:127]\n\t" \
  "s_lshr_b32 s55, %7, 1\n\t" \
  "s_waitcnt vmcnt(31) lgkmcnt(4)\n\t" \
  "s_barrier\n\t" \
  "v_mfma_f32_16x16x32_bf16 a[128:131], a[200:203], v[96:99], a[128:131]\n\t" \
  "ds_read_b64_tr_b16 a[192:193], v26 offset:24576\n\t" \
  "ds_read_b64_tr_b16 a[194:195], v26 offset:27648\n\t" \
  "s_add_u32 s54, s54, s55\n\t" \
  "v_mfma_f32_16x16x32_bf16 a[132:135], a[200:203], v[100:103], a[132:135]\n\t" \
  "s_lshl_b32 s62, s54, 7\n\t" \
  "s_add_u32 s62, s62, 327680\n\t" \
  "s_mul_i32 s96, s54, 18432\n\t" \
  "v_mfma_f32_16x16x32_bf16 a[136:139], a[200:203], v[104:107], a[136:139]\n\t" \
  "buffer_load_dwordx4 v19, %2, s43 offen offset:2048 lds\n\t" \
  "s_lshl_b32 s97, s54, 5\n\t" \
  "v_mfma_f32_16x16x32_bf16 a[140:143], a[200:203], v[108:111], a[140:143]\n\t" \
  "s_and_b32 s55, %7, 1\n\t" \
  "s_mul_i32 s54, %6, 6\n\t" \
  "s_add_u32 s54, s54, s55\n\t" \
  "v_mfma_f32_16x16x32_bf16 a[144:147], a[204:207], v[96:99], a[144:147]\n\t" \
  "ds_read_b64_tr_b16 a[196:197], v27 offset:24576\n\t" \
  "ds_read_b64_tr_b16 a[198:199], v27 offset:27648\n\t" \
  "s_lshl_b32 s63, s54, 6\n\t" \
  "v_mfma_f32_16x16x32_bf16 a[148:151], a[204:207], v[100:103], a[148:151]\n\t" \
  "s_add_u32 s96, s96, s63\n\t" \
  "s_add_u32 s96, s96, 4096\n\t" \
  "s_lshl_b32 s54, s54, 17\n\t" \
  "v_mfma_f32_16x16x32_bf16 a[152:155], a[204:207], v[104:107], a[152:155]\n\t" \
  "s_add_u32 s97, s97, s54\n\t" \
  "s_add_u32 s97, s97, 7340032\n\t" \
  "v_lshrrev_b32 v21, 4, v16\n\t" \
  "v_mfma_f32_16x16x32_bf16 a[156:159], a[204:207], v[108:111], a[156:159]\n\t" \
  "v_lshlrev_b32 v21, 3, v21\n\t" \
  "v_mul_u32_u24 v20, 144, v60\n\t" \
  "s_waitcnt lgkmcnt(4)\n\t" \
  "v_mfma_f32_16x16x32_bf16 a[160:163], a[208:211], v[96:99], a[160:163]\n\t" \
  "ds_read_b64_tr_b16 a[200:201], v26 offset:30720\n\t" \
  "ds_read_b64_tr_b16 a[202:203], v26 offset:33792\n\t" \
  "v_add_u32 v20, v20, v21\n\t" \
  "buffer_load_dwordx2 a[240:241], v60, %3, s62 offen sc1\n\t" \
  "v_mfma_f32_16x16x32_bf16 a[164:167], a[208:211], v[100:103], a[164:167]\n\t" \
  "v_pk_max_i16 v204, v204, 0\n\t" \
  "v_pk_max_i16 v205, v205, 0\n\t" \
  "v_mfma_f32_16x16x32_bf16 a[168:171], a[208:211], v[104:107], a[168:171]\n\t" \
  "buffer_load_dwordx2 a[216:217], v20, %0, s96 offen offset:0\n\t" \
  "v_mfma_f32_16x16x32_bf16 a[172:175], a[208:211], v[108:111], a[172:175]\n\t" \
  "v_pk_max_i16 v206, v206, 0\n\t" \
  "v_pk_max_i16 v207, v207, 0\n\t" \
  "v_mfma_f32_16x16x32_bf16 a[176:179], a[212:215], v[96:99], a[176:179]\n\t" \
  "ds_read_b64_tr_b16 a[204:205], v27 offset:30720\n\t" \
  "ds_read_b64_tr_b16 a[206:207], v27 offset:33792\n\t" \
  "v_mfma_f32_16x16x32_bf16 a[180:183], a[212:215], v[100:103], a[180:183]\n\t" \
  "buffer_store_dwordx4 v[204:207], v32, %3, s67 offen offset:3072 sc1\n\t" \
  "s_add_u32 s67, s45, 20480\n\t" \
  "v_mfma_f32_16x16x32_bf16 a[184:187], a[212:215], v[104:107], a[184:187]\n\t" \
  "v_fmac_f32 v224, v40, v52\n\t" \
  "v_fmac_f32 v225, v40, v53\n\t" \
  "v_fmac_f32 v226, v40, v54\n\t" \
  "v_mfma_f32_16x16x32_bf16 a[188:191], a[212:215], v[108:111], a[188:191]\n\t" \
  "v_fmac_f32 v227, v40, v55\n\t" \
  "v_fmac_f32 v240, v40, v56\n\t" \
  "s_waitcnt lgkmcnt(4)\n\t" \
  "v_mfma_f32_16x16x32_bf16 a[0:3], a[192:195], v[128:131], a[0:3]\n\t" \
  "ds_read_b64_tr_b16 a[208:209], v26 offset:25600\n\t" \
  "ds_read_b64_tr_b16 a[210:211], v26 offset:28672\n\t" \
  "v_fmac_f32 v241, v40, v57\n\t" \
  "v_mfma_f32_16x16x32_bf16 a[4:7], a[192:195], v[132:135], a[4:7]\n\t" \
  "buffer_load_dwordx2 a[228:229], v21, %9, s63 offen offset:0\n\t" \
  "v_fmac_f32 v242, v40, v58\n\t" \
  "v_mfma_f32_16x16x32_bf16 a[8:11], a[192:195], v[136:139], a[8:11]\n\t" \
  "v_fmac_f32 v243, v40, v59\n\t" \
  "v_cvt_pk_bf16_f32 v224, v224, v225\n\t" \
  "v_cvt_pk_bf16_f32 v225, v226, v227\n\t" \
  "v_mfma_f32_16x16x32_bf16 a[12:15], a[192:195], v[140:143], a[12:15]\n\t" \
  "v_cvt_pk_bf16_f32 v226, v240, v241\n\t" \
  "v_cvt_pk_bf16_f32 v227, v242, v243\n\t" \
  "v_pk_max_i16 v224, v224, 0\n\t" \
  "v_mfma_f32_16x16x32_bf16 a[16:19], a[196:199], v[128:131], a[16:19]\n\t" \
  "ds_read_b64_tr_b16 a[212:213], v27 offset:25600\n\t" \
  "ds_read_b64_tr_b16 a[214:215], v27 offset:28672\n\t" \
  "v_pk_max_i16 v225, v225, 0\n\t" \
  "v_mfma_f32_16x16x32_bf16 a[20:23], a[196:199], v[132:135], a[20:23]\n\t" \
  "buffer_load_dwordx2 a[218:219], v20, %0, s96 offen offset:32\n\t" \
  "v_pk_max_i16 v226, v226, 0\n\t" \
  "v_mfma_f32_16x16x32_bf16 a[24:27], a[196:199], v[136:139], a[24:27]\n\t" \
  "v_pk_max_i16 v227, v227, 0\n\t" \
  "v_mfma_f32_16x16x32_bf16 a[28:31], a[196:199], v[140:143], a[28:31]\n\t" \
  "buffer_store_dwordx4 v[224:227], v32, %3, s67 offen offset:0 sc1\n\t" \
  "s_waitcnt lgkmcnt(4)\n\t" \
  "v_mfma_f32_16x16x32_bf16 a[32:35], a[200:203], v[128:131], a[32:35]\n\t" \
  "ds_read_b64_tr_b16 a[192:193], v26 offset:31744\n\t" \
  "ds_read_b64_tr_b16 a[194:195], v26 offset:34816\n\t" \
  "v_fmac_f32 v228, v41, v52\n\t" \
  "v_mfma_f32_16x16x32_bf16 a[36:39], a[200:203], v[132:135], a[36:39]\n\t" \
  "v_fmac_f32 v229, v41, v53\n\t" \
  "v_fmac_f32 v230, v41, v54\n\t" \
  "v_fmac_f32 v231, v41, v55\n\t" \
  "v_mfma_f32_16x16x32_bf16 a[40:43], a[200:203], v[136:139], a[40:43]\n\t" \
  "v_fmac_f32 v244, v41, v56\n\t" \
  "v_fmac_f32 v245, v41, v57\n\t" \
  "v_fmac_f32 v246, v41, v58\n\t" \
  "v_mfma_f32_16x16x32_bf16 a[44:47], a[200:203], v[140:143], a[44:47]\n\t" \
  "v_fmac_f32 v247, v41, v59\n\t" \
  "v_cvt_pk_bf16_f32 v228, v228, v229\n\t" \
  "v_cvt_pk_bf16_f32 v229, v230, v231\n\t" \
  "v_mfma_f32_16x16x32_bf16 a[48:51], a[204:207], v[128:131], a[48:51]\n\t" \
  "ds_read_b64_tr_b16 a[196:197], v27 offset:31744\n\t" \
  "ds_read_b64_tr_b16 a[198:199], v27 offset:34816\n\t" \
  "v_cvt_pk_bf16_f32 v230, v244, v245\n\t" \
  "v_mfma_f32_16x16x32_bf16 a[52:55], a[204:207], v[132:135], a[52:55]\n\t" \
  "buffer_load_dwordx2 a[230:231], v21, %9, s63 offen offset:32\n\t" \
  "v_mfma_f32_16x16x32_bf16 a[56:59], a[204:207], v[136:139], a[56:59]\n\t" \
  "v_cvt_pk_bf16_f32 v231, v246, v247\n\t" \
  "v_pk_max_i16 v228, v228, 0\n\t" \
  "v_mfma_f32_16x16x32_bf16 a[60:63], a[204:207], v[140:143], a[60:63]\n\t" \
  "v_pk_max_i16 v229, v229, 0\n\t" \
  "s_waitcnt lgkmcnt(4)\n\t" \
  "v_mfma_f32_16x16x32_bf16 a[64:67], a[208:211], v[128:131], a[64:67]\n\t" \
  "ds_read_b64_tr_b16 a[200:201], v26 offset:26624\n\t" \
  "ds_read_b64_tr_b16 a[202:203], v26 offset:29696\n\t" \
  "s_waitcnt vmcnt(21)\n\t" \
  "v_mfma_f32_16x16x32_bf16 a[68:71], a[208:211], v[132:135], a[68:71]\n\t" \
  "buffer_store_dwordx2 v[36:37], v34, %3, s64 offen offset:0 sc1\n\t" \
  "v_mfma_f32_16x16x32_bf16 a[72:75], a[208:211], v[136:139], a[72:75]\n\t" \
  "v_pk_max_i16 v230, v230, 0\n\t" \
  "v_pk_max_i16 v231, v231, 0\n\t" \
  "v_mfma_f32_16x16x32_bf16 a[76:79], a[208:211], v[140:143], a[76:79]\n\t" \
  "buffer_store_dwordx4 v[228:231], v32, %3, s67 offen offset:1024 sc1\n\t" \
  "v_mfma_f32_16x16x32_bf16 a[80:83], a[212:215], v[128:131], a[80:83]\n\t" \
  "ds_read_b64_tr_b16 a[204:205], v27 offset:26624\n\t" \
  "ds_read_b64_tr_b16 a[206:207], v27 offset:29696\n\t" \
  "v_mfma_f32_16x16x32_bf16 a[84:87], a[212:215], v[132:135], a[84:87]\n\t" \
  "buffer_load_dwordx2 a[220:221], v20, %0, s96 offen offset:128\n\t" \
  "v_mfma_f32_16x16x32_bf16 a[88:91], a[212:215], v[136:139], a[88:91]\n\t" \
  "v_fmac_f32 v232, v42, v52\n\t" \
  "v_fmac_f32 v233, v42, v53\n\t" \
  "v_mfma_f32_16x16x32_bf16 a[92:95], a[212:215], v[140:143], a[92:95]\n\t" \
  "v_fmac_f32 v234, v42, v54\n\t" \
  "s_waitcnt lgkmcnt(4)\n\t" \
  "v_mfma_f32_16x16x32_bf16 a[96:99], a[192:195], v[128:131], a[96:99]\n\t" \
  "ds_read_b64_tr_b16 a[208:209], v26 offset:32768\n\t" \
  "ds_read_b64_tr_b16 a[210:211], v26 offset:35840\n\t" \
  "v_mfma_f32_16x16x32_bf16 a[100:103], a[192:195], v[132:135], a[100:103]\n\t" \
  "s_add_u32 m0, s40, 36864\n\t" \
  "s_add_u32 s43, s52, 73728\n\t" \
  "v_mfma_f32_16x16x32_bf16 a[104:107], a[192:195], v[136:139], a[104:107]\n\t" \
  "buffer_load_dwordx4 v17, %2, s43 offen offset:0 lds\n\t" \
  "v_mfma_f32_16x16x32_bf16 a[108:111], a[192:195], v[140:143], a[108:111]\n\t" \
  "v_fmac_f32 v235, v42, v55\n\t" \
  "v_fmac_f32 v248, v42, v56\n\t" \
  "v_mfma_f32_16x16x32_bf16 a[112:115], a[196:199], v[128:131], a[112:115]\n\t" \
  "ds_read_b64_tr_b16 a[212:213], v27 offset:32768\n\t" \
  "ds_read_b64_tr_b16 a[214:215], v27 offset:35840\n\t" \
  "v_mfma_f32_16x16x32_bf16 a[116:119], a[196:199], v[132:135], a[116:119]\n\t" \
  "buffer_load_dwordx4 v18, %2, s43 offen offset:1024 lds\n\t" \
  "v_mfma_f32_16x16x32_bf16 a[120:123], a[196:199], v[136:139], a[120:123]\n\t" \
  "v_fmac_f32 v249, v42, v57\n\t" \
  "v_fmac_f32 v250, v42, v58\n\t" \
  "v_mfma_f32_16x16x32_bf16 a[124:127], a[196:199], v[140:143], a[124:127]\n\t" \
  "s_waitcnt lgkmcnt(4)\n\t" \
  "s_barrier\n\t" \
  "v_mfma_f32_16x16x32_bf16 a[128:131], a[200:203], v[128:131], a[128:131]\n\t" \
  "ds_read_b64_tr_b16 a[192:193], v26 offset:36864\n\t" \
  "ds_read_b64_tr_b16 a[194:195], v26 offset:39936\n\t" \
  "v_mfma_f32_16x16x32_bf16 a[132:135], a[200:203], v[132:135], a[132:135]\n\t" \
  "buffer_load_dwordx2 a[232:233], v21, %9, s63 offen offset:128\n\t" \
  "v_mfma_f32_16x16x32_bf16 a[136:139], a[200:203], v[136:139], a[136:139]\n\t" \
  "v_fmac_f32 v251, v42, v59\n\t" \
  "v_cvt_pk_bf16_f32 v232, v232, v233\n\t" \
  "v_mfma_f32_16x16x32_bf16 a[140:143], a[200:203], v[140:143], a[140:143]\n\t" \
  "buffer_load_dwordx4 v19, %2, s43 offen offset:2048 lds\n\t" \
  "v_mfma_f32_16x16x32_bf16 a[144:147], a[204:207], v[128:131], a[144:147]\n\t" \
  "ds_read_b64_tr_b16 a[196:197], v27 offset:36864\n\t" \
  "ds_read_b64_tr_b16 a[198:199], v27 offset:39936\n\t" \
  "v_mfma_f32_16x16x32_bf16 a[148:151], a[204:207], v[132:135], a[148:151]\n\t" \
  "buffer_load_dwordx2 a[222:223], v20, %0, s96 offen offset:160\n\t" \
  "v_mfma_f32_16x16x32_bf16 a[152:155], a[204:207], v[136:139], a[152:155]\n\t" \
  "v_cvt_pk_bf16_f32 v233, v234, v235\n\t" \
  "v_cvt_pk_bf16_f32 v234, v248, v249\n\t" \
  "v_mfma_f32_16x16x32_bf16 a[156:159], a[204:207], v[140:143], a[156:159]\n\t" \
  "v_cvt_pk_bf16_f32 v235, v250, v251\n\t" \
  "s_waitcnt lgkmcnt(4)\n\t" \
  "v_mfma_f32_16x16x32_bf16 a[160:163], a[208:211], v[128:131], a[160:163]\n\t" \
  "ds_read_b64_tr_b16 a[200:201], v26 offset:43008\n\t" \
  "ds_read_b64_tr_b16 a[202:203], v26 offset:46080\n\t" \
  "v_mfma_f32_16x16x32_bf16 a[164:167], a[208:211], v[132:135], a[164:167]\n\t" \
  "v_pk_max_i16 v232, v232, 0\n\t" \
  "v_pk_max_i16 v233, v233, 0\n\t" \
  "v_mfma_f32_16x16x32_bf16 a[168:171], a[208:211], v[136:139], a[168:171]\n\t" \
  "v_pk_max_i16 v234, v234, 0\n\t" \
  "v_pk_max_i16 v235, v235, 0\n\t" \
  "v_mfma_f32_16x16x32_bf16 a[172:175], a[208:211], v[140:143], a[172:175]\n\t" \
  "buffer_load_dwordx2 a[234:235], v21, %9, s63 offen offset:160\n\t" \
  "v_mfma_f32_16x16x32_bf16 a[176:179], a[212:215], v[128:131], a[176:179]\n\t" \
  "ds_read_b64_tr_b16 a[204:205], v27 offset:43008\n\t" \
  "ds_read_b64_tr_b16 a[206:207], v27 offset:46080\n\t" \
  "v_mfma_f32_16x16x32_bf16 a[180:183], a[212:215], v[132:135], a[180:183]\n\t" \
  "v_add_u32 v17, 4096, v63\n\t" \
  "v_add_u32 v18, 39936, v63\n\t" \
  "v_mfma_f32_16x16x32_bf16 a[184:187], a[212:215], v[136:139], a[184:187]\n\t" \
  "v_add_u32 v19, 75776, v63\n\t" \
  "v_mfma_f32_16x16x32_bf16 a[188:191], a[212:215], v[140:143], a[188:191]\n\t" \
  "buffer_store_dwordx4 v[232:235], v32, %3, s67 offen offset:2048 sc1\n\t" \
  "s_waitcnt lgkmcnt(4)\n\t" \
  "v_mfma_f32_16x16x32_bf16 a[0:3], a[192:195], v[160:163], a[0:3]\n\t" \
  "ds_read_b64_tr_b16 a[208:209], v26 offset:37888\n\t" \
  "ds_read_b64_tr_b16 a[210:211], v26 offset:40960\n\t" \
  "v_mfma_f32_16x16x32_bf16 a[4:7], a[192:195], v[164:167], a[4:7]\n\t" \
  "buffer_load_dwordx2 v[38:39], v30, %3, 0 offen offset:0 sc1\n\t" \
  "v_mfma_f32_16x16x32_bf16 a[8:11], a[192:195], v[168:171], a[8:11]\n\t" \
  "v_fmac_f32 v236, v43, v52\n\t" \
  "v_fmac_f32 v237, v43, v53\n\t" \
  "v_mfma_f32_16x16x32_bf16 a[12:15], a[192:195], v[172:175], a[12:15]\n\t" \
  "buffer_load_dwordx2 a[224:225], v20, %0, s96 offen offset:256\n\t" \
  "v_mfma_f32_16x16x32_bf16 a[16:19], a[196:199], v[160:163], a[16:19]\n\t" \
  "ds_read_b64_tr_b16 a[212:213], v27 offset:37888\n\t" \
  "ds_read_b64_tr_b16 a[214:215], v27 offset:40960\n\t" \
  "v_mfma_f32_16x16x32_bf16 a[20:23], a[196:199], v[164:167], a[20:23]\n\t" \
  "v_fmac_f32 v238, v43, v54\n\t" \
  "v_fmac_f32 v239, v43, v55\n\t" \
  "v_mfma_f32_16x16x32_bf16 a[24:27], a[196:199], v[168:171], a[24:27]\n\t" \
  "v_fmac_f32 v252, v43, v56\n\t" \
  "v_fmac_f32 v253, v43, v57\n\t" \
  "v_mfma_f32_16x16x32_bf16 a[28:31], a[196:199], v[172:175], a[28:31]\n\t" \
  "v_fmac_f32 v254, v43, v58\n\t" \
  "s_waitcnt lgkmcnt(4)\n\t" \
  "v_mfma_f32_16x16x32_bf16 a[32:35], a[200:203], v[160:163], a[32:35]\n\t" \
  "ds_read_b64_tr_b16 a[192:193], v26 offset:44032\n\t" \
  "ds_read_b64_tr_b16 a[194:195], v26 offset:47104\n\t" \
  "v_mfma_f32_16x16x32_bf16 a[36:39], a[200:203], v[164:167], a[36:39]\n\t" \
  "v_fmac_f32 v255, v43, v59\n\t" \
  "v_cvt_pk_bf16_f32 v236, v236, v237\n\t" \
  "v_mfma_f32_16x16x32_bf16 a[40:43], a[200:203], v[168:171], a[40:43]\n\t" \
  "v_cvt_pk_bf16_f32 v237, v238, v239\n\t" \
  "v_cvt_pk_bf16_f32 v238, v252, v253\n\t" \
  "v_mfma_f32_16x16x32_bf16 a[44:47], a[200:203], v[172:175], a[44:47]\n\t" \
  "v_cvt_pk_bf16_f32 v239, v254, v255\n\t" \
  "v_pk_max_i16 v236, v236, 0\n\t" \
  "v_mfma_f32_16x16x32_bf16 a[48:51], a[204:207], v[160:163], a[48:51]\n\t" \
  "ds_read_b64_tr_b16 a[196:197], v27 offset:44032\n\t" \
  "ds_read_b64_tr_b16 a[198:199], v27 offset:47104\n\t" \
  "v_mfma_f32_16x16x32_bf16 a[52:55], a[204:207], v[164:167], a[52:55]\n\t" \
  "v_pk_max_i16 v237, v237, 0\n\t" \
  "v_pk_max_i16 v238, v238, 0\n\t" \
  "v_mfma_f32_16x16x32_bf16 a[56:59], a[204:207], v[168:171], a[56:59]\n\t" \
  "v_pk_max_i16 v239, v239, 0\n\t" \
  "v_mfma_f32_16x16x32_bf16 a[60:63], a[204:207], v[172:175], a[60:63]\n\t" \
  "buffer_store_dwordx4 v[236:239], v32, %3, s67 offen offset:3072 sc1\n\t" \
  "s_waitcnt lgkmcnt(4)\n\t" \
  "v_mfma_f32_16x16x32_bf16 a[64:67], a[208:211], v[160:163], a[64:67]\n\t" \
  "ds_read_b64_tr_b16 a[200:201], v26 offset:38912\n\t" \
  "ds_read_b64_tr_b16 a[202:203], v26 offset:41984\n\t" \
  "v_mfma_f32_16x16x32_bf16 a[68:71], a[208:211], v[164:167], a[68:71]\n\t" \
  "buffer_load_dwordx2 a[236:237], v21, %9, s63 offen offset:256\n\t" \
  "v_mfma_f32_16x16x32_bf16 a[72:75], a[208:211], v[168:171], a[72:75]\n\t" \
  "v_mfma_f32_16x16x32_bf16 a[76:79], a[208:211], v[172:175], a[76:79]\n\t" \
  "buffer_load_dwordx2 a[226:227], v20, %0, s96 offen offset:288\n\t" \
  "v_mfma_f32_16x16x32_bf16 a[80:83], a[212:215], v[160:163], a[80:83]\n\t" \
  "ds_read_b64_tr_b16 a[204:205], v27 offset:38912\n\t" \
  "ds_read_b64_tr_b16 a[206:207], v27 offset:41984\n\t" \
  "v_mfma_f32_16x16x32_bf16 a[84:87], a[212:215], v[164:167], a[84:87]\n\t" \
  "v_mfma_f32_16x16x32_bf16 a[88:91], a[212:215], v[168:171], a[88:91]\n\t" \
  "v_mfma_f32_16x16x32_bf16 a[92:95], a[212:215], v[172:175], a[92:95]\n\t" \
  "s_waitcnt lgkmcnt(4)\n\t" \
  "v_mfma_f32_16x16x32_bf16 a[96:99], a[192:195], v[160:163], a[96:99]\n\t" \
  "ds_read_b64_tr_b16 a[208:209], v26 offset:45056\n\t" \
  "ds_read_b64_tr_b16 a[210:211], v26 offset:48128\n\t" \
  "v_mfma_f32_16x16x32_bf16 a[100:103], a[192:195], v[164:167], a[100:103]\n\t" \
  "s_add_u32 m0, s40, 49152\n\t" \
  "s_add_u32 s43, s53, 0\n\t" \
  "v_mfma_f32_16x16x32_bf16 a[104:107], a[192:195], v[168:171], a[104:107]\n\t" \
  "buffer_load_dwordx4 v17, %2, s43 offen offset:0 lds\n\t" \
  "v_mfma_f32_16x16x32_bf16 a[108:111], a[192:195], v[172:175], a[108:111]\n\t" \
  "v_mfma_f32_16x16x32_bf16 a[112:115], a[196:199], v[160:163], a[112:115]\n\t" \
  "ds_read_b64_tr_b16 a[212:213], v27 offset:45056\n\t" \
  "ds_read_b64_tr_b16 a[214:215], v27 offset:48128\n\t" \
  "v_mfma_f32_16x16x32_bf16 a[116:119], a[196:199], v[164:167], a[116:119]\n\t" \
  "buffer_load_dwordx4 v18, %2, s43 offen offset:1024 lds\n\t" \
  "v_mfma_f32_16x16x32_bf16 a[120:123], a[196:199], v[168:171], a[120:123]\n\t" \
  "v_mfma_f32_16x16x32_bf16 a[124:127], a[196:199], v[172:175], a[124:127]\n\t" \
  "s_waitcnt lgkmcnt(4)\n\t" \
  "s_barrier\n\t" \
  "v_mfma_f32_16x16x32_bf16 a[128:131], a[200:203], v[160:163], a[128:131]\n\t" \
  "ds_read_b64_tr_b16 a[192:193], v26 offset:49152\n\t" \
  "ds_read_b64_tr_b16 a[194:195], v26 offset:52224\n\t" \
  "v_mfma_f32_16x16x32_bf16 a[132:135], a[200:203], v[164:167], a[132:135]\n\t" \
  "buffer_load_dwordx2 a[238:239], v21, %9, s63 offen offset:288\n\t" \
  "v_mfma_f32_16x16x32_bf16 a[136:139], a[200:203], v[168:171], a[136:139]\n\t" \
  "v_mfma_f32_16x16x32_bf16 a[140:143], a[200:203], v[172:175], a[140:143]\n\t" \
  "buffer_load_dwordx4 v19, %2, s43 offen offset:2048 lds\n\t" \
  "v_mfma_f32_16x16x32_bf16 a[144:147], a[204:207], v[160:163], a[144:147]\n\t" \
  "ds_read_b64_tr_b16 a[196:197], v27 offset:49152\n\t" \
  "ds_read_b64_tr_b16 a[198:199], v27 offset:52224\n\t" \
  "v_mfma_f32_16x16x32_bf16 a[148:151], a[204:207], v[164:167], a[148:151]\n\t" \
  "v_mfma_f32_16x16x32_bf16 a[152:155], a[204:207], v[168:171], a[152:155]\n\t" \
  "v_mfma_f32_16x16x32_bf16 a[156:159], a[204:207], v[172:175], a[156:159]\n\t" \
  "s_waitcnt lgkmcnt(4)\n\t" \
  "v_mfma_f32_16x16x32_bf16 a[160:163], a[208:211], v[160:163], a[160:163]\n\t" \
  "ds_read_b64_tr_b16 a[200:201], v26 offset:55296\n\t" \
  "ds_read_b64_tr_b16 a[202:203], v26 offset:58368\n\t" \
  "v_mfma_f32_16x16x32_bf16 a[164:167], a[208:211], v[164:167], a[164:167]\n\t" \
  "v_mfma_f32_16x16x32_bf16 a[168:171], a[208:211], v[168:171], a[168:171]\n\t" \
  "v_mfma_f32_16x16x32_bf16 a[172:175], a[208:211], v[172:175], a[172:175]\n\t" \
  "v_mfma_f32_16x16x32_bf16 a[176:179], a[212:215], v[160:163], a[176:179]\n\t" \
  "ds_read_b64_tr_b16 a[204:205], v27 offset:55296\n\t" \
  "ds_read_b64_tr_b16 a[206:207], v27 offset:58368\n\t" \
  "v_mfma_f32_16x16x32_bf16 a[180:183], a[212:215], v[164:167], a[180:183]\n\t" \
  "v_mfma_f32_16x16x32_bf16 a[184:187], a[212:215], v[168:171], a[184:187]\n\t" \
  "v_mfma_f32_16x16x32_bf16 a[188:191], a[212:215], v[172:175], a[188:191]\n\t" \
  "s_waitcnt lgkmcnt(4)\n\t" \
  "v_mfma_f32_16x16x32_bf16 a[0:3], a[192:195], v[192:195], a[0:3]\n\t" \
  "ds_read_b64_tr_b16 a[208:209], v26 offset:50176\n\t" \
  "ds_read_b64_tr_b16 a[210:211], v26 offset:53248\n\t" \
  "v_mfma_f32_16x16x32_bf16 a[4:7], a[192:195], v[196:199], a[4:7]\n\t" \
  "v_mfma_f32_16x16x32_bf16 a[8:11], a[192:195], v[200:203], a[8:11]\n\t" \
  "v_mfma_f32_16x16x32_bf16 a[12:15], a[192:195], v[204:207], a[12:15]\n\t" \
  "v_mfma_f32_16x16x32_bf16 a[16:19], a[196:199], v[192:195], a[16:19]\n\t" \
  "ds_read_b64_tr_b16 a[212:213], v27 offset:50176\n\t" \
  "ds_read_b64_tr_b16 a[214:215], v27 offset:53248\n\t" \
  "v_mfma_f32_16x16x32_bf16 a[20:23], a[196:199], v[196:199], a[20:23]\n\t" \
  "v_mfma_f32_16x16x32_bf16 a[24:27], a[196:199], v[200:203], a[24:27]\n\t" \
  "v_mfma_f32_16x16x32_bf16 a[28:31], a[196:199], v[204:207], a[28:31]\n\t" \
  "s_waitcnt lgkmcnt(4)\n\t" \
  "v_mfma_f32_16x16x32_bf16 a[32:35], a[200:203], v[192:195], a[32:35]\n\t" \
  "ds_read_b64_tr_b16 a[192:193], v26 offset:56320\n\t" \
  "ds_read_b64_tr_b16 a[194:195], v26 offset:59392\n\t" \
  "v_mfma_f32_16x16x32_bf16 a[36:39], a[200:203], v[196:199], a[36:39]\n\t" \
  "v_mfma_f32_16x16x32_bf16 a[40:43], a[200:203], v[200:203], a[40:43]\n\t" \
  "v_mfma_f32_16x16x32_bf16 a[44:47], a[200:203], v[204:207], a[44:47]\n\t" \
  "v_mfma_f32_16x16x32_bf16 a[48:51], a[204:207], v[192:195], a[48:51]\n\t" \
  "ds_read_b64_tr_b16 a[196:197], v27 offset:56320\n\t" \
  "ds_read_b64_tr_b16 a[198:199], v27 offset:59392\n\t" \
  "v_mfma_f32_16x16x32_bf16 a[52:55], a[204:207], v[196:199], a[52:55]\n\t" \
  "v_mfma_f32_16x16x32_bf16 a[56:59], a[204:207], v[200:203], a[56:59]\n\t" \
  "v_mfma_f32_16x16x32_bf16 a[60:63], a[204:207], v[204:207], a[60:63]\n\t" \
  "s_waitcnt lgkmcnt(4)\n\t" \
  "v_mfma_f32_16x16x32_bf16 a[64:67], a[208:211], v[192:195], a[64:67]\n\t" \
  "ds_read_b64_tr_b16 a[200:201], v26 offset:51200\n\t" \
  "ds_read_b64_tr_b16 a[202:203], v26 offset:54272\n\t" \
  "v_mfma_f32_16x16x32_bf16 a[68:71], a[208:211], v[196:199], a[68:71]\n\t" \
  "s_mul_i32 s54, %6, 21\n\t" \
  "v_subrev_u32 v31, 8, v16\n\t" \
  "v_mfma_f32_16x16x32_bf16 a[72:75], a[208:211], v[200:203], a[72:75]\n\t" \
  "v_min_u32 v31, 21, v31\n\t" \
  "v_add_u32 v31, s54, v31\n\t" \
  "v_mfma_f32_16x16x32_bf16 a[76:79], a[208:211], v[204:207], a[76:79]\n\t" \
  "v_lshlrev_b32 v31, 7, v31\n\t" \
  "v_add_u32 v31, 49152, v31\n\t" \
  "v_mfma_f32_16x16x32_bf16 a[80:83], a[212:215], v[192:195], a[80:83]\n\t" \
  "ds_read_b64_tr_b16 a[204:205], v27 offset:51200\n\t" \
  "ds_read_b64_tr_b16 a[206:207], v27 offset:54272\n\t" \
  "v_mfma_f32_16x16x32_bf16 a[84:87], a[212:215], v[196:199], a[84:87]\n\t" \
  "s_mul_i32 s54, %4, 3\n\t" \
  "s_add_u32 s54, s54, %6\n\t" \
  "v_mfma_f32_16x16x32_bf16 a[88:91], a[212:215], v[200:203], a[88:91]\n\t" \
  "s_lshl_b32 s55, s54, 2\n\t" \
  "s_lshr_b32 s56, %5, 1\n\t" \
  "v_mfma_f32_16x16x32_bf16 a[92:95], a[212:215], v[204:207], a[92:95]\n\t" \
  "s_add_u32 s55, s55, s56\n\t" \
  "s_waitcnt lgkmcnt(4)\n\t" \
  "v_mfma_f32_16x16x32_bf16 a[96:99], a[192:195], v[192:195], a[96:99]\n\t" \
  "ds_read_b64_tr_b16 a[208:209], v26 offset:57344\n\t" \
  "ds_read_b64_tr_b16 a[210:211], v26 offset:60416\n\t" \
  "v_mfma_f32_16x16x32_bf16 a[100:103], a[192:195], v[196:199], a[100:103]\n\t" \
  "s_add_u32 m0, s40, 0\n\t" \
  "s_add_u32 s43, s53, 128\n\t" \
  "v_mfma_f32_16x16x32_bf16 a[104:107], a[192:195], v[200:203], a[104:107]\n\t" \
  "buffer_load_dwordx4 v17, %2, s43 offen offset:0 lds\n\t" \
  "v_mfma_f32_16x16x32_bf16 a[108:111], a[192:195], v[204:207], a[108:111]\n\t" \
  "s_mul_i32 s55, s55, 1536\n\t" \
  "s_add_u32 s55, s55, 163840\n\t" \
  "v_mfma_f32_16x16x32_bf16 a[112:115], a[196:199], v[192:195], a[112:115]\n\t" \
  "ds_read_b64_tr_b16 a[212:213], v27 offset:57344\n\t" \
  "ds_read_b64_tr_b16 a[214:215], v27 offset:60416\n\t" \
  "v_mfma_f32_16x16x32_bf16 a[116:119], a[196:199], v[196:199], a[116:119]\n\t" \
  "buffer_load_dwordx4 v18, %2, s43 offen offset:1024 lds\n\t" \
  "v_mfma_f32_16x16x32_bf16 a[120:123], a[196:199], v[200:203], a[120:123]\n\t" \
  "s_waitcnt vmcnt(10)\n\t" \
  "v_cmp_eq_u64 vcc, v[36:37], v[38:39]\n\t" \
  "v_mfma_f32_16x16x32_bf16 a[124:127], a[196:199], v[204:207], a[124:127]\n\t" \
  "s_waitcnt lgkmcnt(4)\n\t" \
  "s_barrier\n\t" \
  "v_mfma_f32_16x16x32_bf16 a[128:131], a[200:203], v[192:195], a[128:131]\n\t" \
  "ds_read_b64_tr_b16 a[192:193], v28 offset:0\n\t" \
  "ds_read_b64_tr_b16 a[194:195], v28 offset:3072\n\t" \
  "v_mfma_f32_16x16x32_bf16 a[132:135], a[200:203], v[196:199], a[132:135]\n\t" \
  "s_nop 1\n\t" \
  "s_cmp_eq_u64 vcc, -1\n\t" \
  "s_cbranch_scc0 L_slow2_%=\n\t" \
  "L_back2_%=:\n\t" \
  "v_mfma_f32_16x16x32_bf16 a[136:139], a[200:203], v[200:203], a[136:139]\n\t" \
  "buffer_load_dwordx4 v19, %2, s43 offen offset:2048 lds\n\t" \
  "v_mfma_f32_16x16x32_bf16 a[140:143], a[200:203], v[204:207], a[140:143]\n\t" \
  "v_and_b32 v35, 7, v16\n\t" \
  "v_lshlrev_b32 v35, 6, v35\n\t" \
  "v_mfma_f32_16x16x32_bf16 a[144:147], a[204:207], v[192:195], a[144:147]\n\t" \
  "ds_read_b64_tr_b16 a[196:197], v29 offset:0\n\t" \
  "ds_read_b64_tr_b16 a[198:199], v29 offset:3072\n\t" \
  "v_mfma_f32_16x16x32_bf16 a[148:151], a[204:207], v[196:199], a[148:151]\n\t" \
  "s_add_u32 s68, s46, 0\n\t" \
  "v_add_u32 v35, s55, v35\n\t" \
  "v_mfma_f32_16x16x32_bf16 a[152:155], a[204:207], v[200:203], a[152:155]\n\t" \
  "buffer_load_dwordx4 v[80:83], v32, %3, s68 offen offset:0 sc1\n\t" \
  "v_mfma_f32_16x16x32_bf16 a[156:159], a[204:207], v[204:207], a[156:159]\n\t" \
  "s_lshl_b32 s54, s54, 3\n\t" \
  "s_waitcnt lgkmcnt(4)\n\t" \
  "v_mfma_f32_16x16x32_bf16 a[160:163], a[208:211], v[192:195], a[160:163]\n\t" \
  "ds_read_b64_tr_b16 a[200:201], v28 offset:6144\n\t" \
  "ds_read_b64_tr_b16 a[202:203], v28 offset:9216\n\t" \
  "v_mfma_f32_16x16x32_bf16 a[164:167], a[208:211], v[196:199], a[164:167]\n\t" \
  "s_add_u32 s54, s54, %5\n\t" \
  "s_mul_i32 s54, s54, 12\n\t" \
  "v_mfma_f32_16x16x32_bf16 a[168:171], a[208:211], v[200:203], a[168:171]\n\t" \
  "buffer_load_dwordx4 v[84:87], v32, %3, s68 offen offset:1024 sc1\n\t" \
  "v_mfma_f32_16x16x32_bf16 a[172:175], a[208:211], v[204:207], a[172:175]\n\t" \
  "s_add_u32 s54, s54, %7\n\t" \
  "s_lshl_b32 s70, s54, 13\n\t" \
  "v_mfma_f32_16x16x32_bf16 a[176:179], a[212:215], v[192:195], a[176:179]\n\t" \
  "ds_read_b64_tr_b16 a[204:205], v29 offset:6144\n\t" \
  "ds_read_b64_tr_b16 a[206:207], v29 offset:9216\n\t" \
  "v_mfma_f32_16x16x32_bf16 a[180:183], a[212:215], v[196:199], a[180:183]\n\t" \
  "s_add_u32 s70, s70, 29360128\n\t" \
  "v_cmp_gt_u32 vcc, 8, v16\n\t" \
  "s_nop 1\n\t" \
  "v_cndmask_b32 v31, v31, v35, vcc\n\t" \
  "v_mfma_f32_16x16x32_bf16 a[184:187], a[212:215], v[200:203], a[184:187]\n\t" \
  "v_lshrrev_b32 v22, 1, v16\n\t" \
  "v_lshlrev_b32 v22, 12, v22\n\t" \
  "v_mfma_f32_16x16x32_bf16 a[188:191], a[212:215], v[204:207], a[188:191]\n\t" \
  "v_and_b32 v33, 1, v16\n\t" \
  "s_waitcnt lgkmcnt(4)\n\t" \
  "v_mfma_f32_16x16x32_bf16 a[0:3], a[192:195], v[224:227], a[0:3]\n\t" \
  "ds_read_b64_tr_b16 a[208:209], v28 offset:1024\n\t" \
  "ds_read_b64_tr_b16 a[210:211], v28 offset:4096\n\t" \
  "v_mfma_f32_16x16x32_bf16 a[4:7], a[192:195], v[228:231], a[4:7]\n\t" \
  "buffer_load_dwordx4 v[88:91], v32, %3, s68 offen offset:2048 sc1\n\t" \
  "v_mfma_f32_16x16x32_bf16 a[8:11], a[192:195], v[232:235], a[8:11]\n\t" \
  "v_lshl_add_u32 v33, v33, 4, v22\n\t" \
  "v_accvgpr_write_b32 a255, v20\n\t" \
  "s_waitcnt vmcnt(7)\n\t" \
  "v_mfma_f32_16x16x32_bf16 a[12:15], a[192:195], v[236:239], a[12:15]\n\t" \
  "v_accvgpr_read_b32 v58, a241\n\t" \
  "v_accvgpr_read_b32 v59, a240\n\t" \
  "v_mfma_f32_16x16x32_bf16 a[16:19], a[196:199], v[224:227], a[16:19]\n\t" \
  "ds_read_b64_tr_b16 a[212:213], v29 offset:1024\n\t" \
  "ds_read_b64_tr_b16 a[214:215], v29 offset:4096\n\t" \
  "v_mfma_f32_16x16x32_bf16 a[20:23], a[196:199], v[228:231], a[20:23]\n\t" \
  "v_rcp_f32 v58, v58\n\t" \
  "s_nop 0\n\t" \
  "v_mfma_f32_16x16x32_bf16 a[24:27], a[196:199], v[232:235], a[24:27]\n\t" \
  "v_mul_f32 v59, v59, v58\n\t" \
  "v_accvgpr_write_b32 a254, v58\n\t" \
  "v_mfma_f32_16x16x32_bf16 a[28:31], a[196:199], v[236:239], a[28:31]\n\t" \
  "v_accvgpr_read_b32 v52, a228\n\t" \
  "s_waitcnt lgkmcnt(4)\n\t" \
  "v_mfma_f32_16x16x32_bf16 a[32:35], a[200:203], v[224:227], a[32:35]\n\t" \
  "ds_read_b64_tr_b16 a[192:193], v28 offset:7168\n\t" \
  "ds_read_b64_tr_b16 a[194:195], v28 offset:10240\n\t" \
  "v_mfma_f32_16x16x32_bf16 a[36:39], a[200:203], v[228:231], a[36:39]\n\t" \
  "buffer_load_dwordx4 v[92:95], v32, %3, s68 offen offset:3072 sc1\n\t" \
  "v_mfma_f32_16x16x32_bf16 a[40:43], a[200:203], v[232:235], a[40:43]\n\t" \
  "v_accvgpr_read_b32 v53, a229\n\t" \
  "v_accvgpr_read_b32 v54, a216\n\t" \
  "v_mfma_f32_16x16x32_bf16 a[44:47], a[200:203], v[236:239], a[44:47]\n\t" \
  "v_accvgpr_read_b32 v55, a217\n\t" \
  "v_lshlrev_b32 v48, 16, v52\n\t" \
  "v_mfma_f32_16x16x32_bf16 a[48:51], a[204:207], v[224:227], a[48:51]\n\t" \
  "ds_read_b64_tr_b16 a[196:197], v29 offset:7168\n\t" \
  "ds_read_b64_tr_b16 a[198:199], v29 offset:10240\n\t" \
  "v_mfma_f32_16x16x32_bf16 a[52:55], a[204:207], v[228:231], a[52:55]\n\t" \
  "v_and_b32 v49, 0xffff0000, v52\n\t" \
  "v_lshlrev_b32 v50, 16, v53\n\t" \
  "v_mfma_f32_16x16x32_bf16 a[56:59], a[204:207], v[232:235], a[56:59]\n\t" \
  "s_add_u32 s68, s46, 4096\n\t" \
  "v_and_b32 v51, 0xffff0000, v53\n\t" \
  "v_mfma_f32_16x16x32_bf16 a[60:63], a[204:207], v[236:239], a[60:63]\n\t" \
  "v_lshlrev_b32 v44, 16, v54\n\t" \
  "s_waitcnt lgkmcnt(4)\n\t" \
  "v_mfma_f32_16x16x32_bf16 a[64:67], a[208:211], v[224:227], a[64:67]\n\t" \
  "ds_read_b64_tr_b16 a[200:201], v28 offset:2048\n\t" \
  "ds_read_b64_tr_b16 a[202:203], v28 offset:5120\n\t" \
  "v_mfma_f32_16x16x32_bf16 a[68:71], a[208:211], v[228:231], a[68:71]\n\t" \
  "buffer_load_dwordx4 v[112:115], v32, %3, s68 offen offset:0 sc1\n\t" \
  "v_mfma_f32_16x16x32_bf16 a[72:75], a[208:211], v[232:235], a[72:75]\n\t" \
  "v_and_b32 v45, 0xffff0000, v54\n\t" \
  "v_lshlrev_b32 v46, 16, v55\n\t" \
  "v_mfma_f32_16x16x32_bf16 a[76:79], a[208:211], v[236:239], a[76:79]\n\t" \
  "v_and_b32 v47, 0xffff0000, v55\n\t" \
  "v_fma_f32 v56, v44, v58, v59\n\t" \
  "v_mfma_f32_16x16x32_bf16 a[80:83], a[212:215], v[224:227], a[80:83]\n\t" \
  "ds_read_b64_tr_b16 a[204:205], v29 offset:2048\n\t" \
  "ds_read_b64_tr_b16 a[206:207], v29 offset:5120\n\t" \
  "v_mfma_f32_16x16x32_bf16 a[84:87], a[212:215], v[228:231], a[84:87]\n\t" \
  "buffer_store_dwordx2 v[36:37], v34, %3, s64 offen offset:64 sc1\n\t" \
  "v_mfma_f32_16x16x32_bf16 a[88:91], a[212:215], v[232:235], a[88:91]\n\t" \
  "v_fmac_f32 v44, v56, v48\n\t" \
  "v_fma_f32 v57, v45, v58, v59\n\t" \
  "v_mfma_f32_16x16x32_bf16 a[92:95], a[212:215], v[236:239], a[92:95]\n\t" \
  "v_fmac_f32 v45, v57, v49\n\t" \
  "s_waitcnt lgkmcnt(4)\n\t" \
  "v_mfma_f32_16x16x32_bf16 a[96:99], a[192:195], v[224:227], a[96:99]\n\t" \
  "ds_read_b64_tr_b16 a[208:209], v28 offset:8192\n\t" \
  "ds_read_b64_tr_b16 a[210:211], v28 offset:11264\n\t" \
  "v_mfma_f32_16x16x32_bf16 a[100:103], a[192:195], v[228:231], a[100:103]\n\t" \
  "s_add_u32 m0, s40, 12288\n\t" \
  "s_add_u32 s43, s52, 110592\n\t" \
  "v_mfma_f32_16x16x32_bf16 a[104:107], a[192:195], v[232:235], a[104:107]\n\t" \
  "buffer_load_dwordx4 v17, %2, s43 offen offset:0 lds\n\t" \
  "v_mfma_f32_16x16x32_bf16 a[108:111], a[192:195], v[236:239], a[108:111]\n\t" \
  "v_fma_f32 v56, v46, v58, v59\n\t" \
  "v_fmac_f32 v46, v56, v50\n\t" \
  "v_mfma_f32_16x16x32_bf16 a[112:115], a[196:199], v[224:227], a[112:115]\n\t" \
  "ds_read_b64_tr_b16 a[212:213], v29 offset:8192\n\t" \
  "ds_read_b64_tr_b16 a[214:215], v29 offset:11264\n\t" \
  "v_mfma_f32_16x16x32_bf16 a[116:119], a[196:199], v[228:231], a[116:119]\n\t" \
  "buffer_load_dwordx4 v[116:119], v32, %3, s68 offen offset:1024 sc1\n\t" \
  "v_mfma_f32_16x16x32_bf16 a[120:123], a[196:199], v[232:235], a[120:123]\n\t" \
  "buffer_load_dwordx4 v18, %2, s43 offen offset:1024 lds\n\t" \
  "v_mfma_f32_16x16x32_bf16 a[124:127], a[196:199], v[236:239], a[124:127]\n\t" \
  "s_waitcnt lgkmcnt(4)\n\t" \
  "s_barrier\n\t" \
  "v_mfma_f32_16x16x32_bf16 a[128:131], a[200:203], v[224:227], a[128:131]\n\t" \
  "ds_read_b64_tr_b16 a[192:193], v28 offset:12288\n\t" \
  "ds_read_b64_tr_b16 a[194:195], v28 offset:15360\n\t" \
  "v_mfma_f32_16x16x32_bf16 a[132:135], a[200:203], v[228:231], a[132:135]\n\t" \
  "buffer_load_dwordx4 v[120:123], v32, %3, s68 offen offset:2048 sc1\n\t" \
  "v_mfma_f32_16x16x32_bf16 a[136:139], a[200:203], v[232:235], a[136:139]\n\t" \
  "v_fma_f32 v57, v47, v58, v59\n\t" \
  "v_fmac_f32 v47, v57, v51\n\t" \
  "v_mfma_f32_16x16x32_bf16 a[140:143], a[200:203], v[236:239], a[140:143]\n\t" \
  "buffer_load_dwordx4 v19, %2, s43 offen offset:2048 lds\n\t" \
  "v_mfma_f32_16x16x32_bf16 a[144:147], a[204:207], v[224:227], a[144:147]\n\t" \
  "ds_read_b64_tr_b16 a[196:197], v29 offset:12288\n\t" \
  "ds_read_b64_tr_b16 a[198:199], v29 offset:15360\n\t" \
  "v_mfma_f32_16x16x32_bf16 a[148:151], a[204:207], v[228:231], a[148:151]\n\t" \
  "buffer_load_dwordx4 v[124:127], v32, %3, s68 offen offset:3072 sc1\n\t" \
  "v_mfma_f32_16x16x32_bf16 a[152:155], a[204:207], v[232:235], a[152:155]\n\t" \
  "v_accvgpr_write_b32 a242, v44\n\t" \
  "v_accvgpr_write_b32 a243, v45\n\t" \
  "v_mfma_f32_16x16x32_bf16 a[156:159], a[204:207], v[236:239], a[156:159]\n\t" \
  "v_accvgpr_write_b32 a244, v46\n\t" \
  "s_waitcnt lgkmcnt(4)\n\t" \
  "v_mfma_f32_16x16x32_bf16 a[160:163], a[208:211], v[224:227], a[160:163]\n\t" \
  "ds_read_b64_tr_b16 a[200:201], v28 offset:18432\n\t" \
  "ds_read_b64_tr_b16 a[202:203], v28 offset:21504\n\t" \
  "v_mfma_f32_16x16x32_bf16 a[164:167], a[208:211], v[228:231], a[164:167]\n\t" \
  "s_add_u32 s68, s46, 8192\n\t" \
  "v_accvgpr_write_b32 a245, v47\n\t" \
  "v_mfma_f32_16x16x32_bf16 a[168:171], a[208:211], v[232:235], a[168:171]\n\t" \
  "buffer_load_dwordx4 v[144:147], v32, %3, s68 offen offset:0 sc1\n\t" \
  "v_mfma_f32_16x16x32_bf16 a[172:175], a[208:211], v[236:239], a[172:175]\n\t" \
  "v_accvgpr_read_b32 v52, a230\n\t" \
  "v_accvgpr_read_b32 v53, a231\n\t" \
  "v_mfma_f32_16x16x32_bf16 a[176:179], a[212:215], v[224:227], a[176:179]\n\t" \
  "ds_read_b64_tr_b16 a[204:205], v29 offset:18432\n\t" \
  "ds_read_b64_tr_b16 a[206:207], v29 offset:21504\n\t" \
  "v_mfma_f32_16x16x32_bf16 a[180:183], a[212:215], v[228:231], a[180:183]\n\t" \
  "v_accvgpr_read_b32 v54, a218\n\t" \
  "v_accvgpr_read_b32 v55, a219\n\t" \
  "v_mfma_f32_16x16x32_bf16 a[184:187], a[212:215], v[232:235], a[184:187]\n\t" \
  "buffer_load_dwordx4 v[148:151], v32, %3, s68 offen offset:1024 sc1\n\t" \
  "v_mfma_f32_16x16x32_bf16 a[188:191], a[212:215], v[236:239], a[188:191]\n\t" \
  "v_lshlrev_b32 v48, 16, v52\n\t" \
  "s_waitcnt vmcnt(10) lgkmcnt(4)\n\t" \
  "v_mfma_f32_16x16x32_bf16 a[0:3], a[192:195], v[80:83], a[0:3]\n\t" \
  "ds_read_b64_tr_b16 a[208:209], v28 offset:13312\n\t" \
  "ds_read_b64_tr_b16 a[210:211], v28 offset:16384\n\t" \
  "v_mfma_f32_16x16x32_bf16 a[4:7], a[192:195], v[84:87], a[4:7]\n\t" \
  "v_and_b32 v49, 0xffff0000, v52\n\t" \
  "v_lshlrev_b32 v50, 16, v53\n\t" \
  "v_mfma_f32_16x16x32_bf16 a[8:11], a[192:195], v[88:91], a[8:11]\n\t" \
  "v_and_b32 v51, 0xffff0000, v53\n\t" \
  "v_lshlrev_b32 v44, 16, v54\n\t" \
  "v_mfma_f32_16x16x32_bf16 a[12:15], a[192:195], v[92:95], a[12:15]\n\t" \
  "v_and_b32 v45, 0xffff0000, v54\n\t" \
  "v_lshlrev_b32 v46, 16, v55\n\t" \
  "v_mfma_f32_16x16x32_bf16 a[16:19], a[196:199], v[80:83], a[16:19]\n\t" \
  "ds_read_b64_tr_b16 a[212:213], v29 offset:13312\n\t" \
  "ds_read_b64_tr_b16 a[214:215], v29 offset:16384\n\t" \
  "v_mfma_f32_16x16x32_bf16 a[20:23], a[196:199], v[84:87], a[20:23]\n\t" \
  "buffer_load_dwordx4 v[152:155], v32, %3, s68 offen offset:2048 sc1\n\t" \
  "v_mfma_f32_16x16x32_bf16 a[24:27], a[196:199], v[88:91], a[24:27]\n\t" \
  "v_and_b32 v47, 0xffff0000, v55\n\t" \
  "v_fma_f32 v56, v44, v58, v59\n\t" \
  "v_mfma_f32_16x16x32_bf16 a[28:31], a[196:199], v[92:95], a[28:31]\n\t" \
  "v_fmac_f32 v44, v56, v48\n\t" \
  "s_waitcnt lgkmcnt(4)\n\t" \
  "v_mfma_f32_16x16x32_bf16 a[32:35], a[200:203], v[80:83], a[32:35]\n\t" \
  "ds_read_b64_tr_b16 a[192:193], v28 offset:19456\n\t" \
  "ds_read_b64_tr_b16 a[194:195], v28 offset:22528\n\t" \
  "v_mfma_f32_16x16x32_bf16 a[36:39], a[200:203], v[84:87], a[36:39]\n\t" \
  "v_fma_f32 v57, v45, v58, v59\n\t" \
  "v_fmac_f32 v45, v57, v49\n\t" \
  "v_mfma_f32_16x16x32_bf16 a[40:43], a[200:203], v[88:91], a[40:43]\n\t" \
  "buffer_load_dwordx2 v[38:39], v30, %3, 0 offen offset:64 sc1\n\t" \
  "v_mfma_f32_16x16x32_bf16 a[44:47], a[200:203], v[92:95], a[44:47]\n\t" \
  "v_fma_f32 v56, v46, v58, v59\n\t" \
  "v_fmac_f32 v46, v56, v50\n\t" \
  "v_mfma_f32_16x16x32_bf16 a[48:51], a[204:207], v[80:83], a[48:51]\n\t" \
  "ds_read_b64_tr_b16 a[196:197], v29 offset:19456\n\t" \
  "ds_read_b64_tr_b16 a[198:199], v29 offset:22528\n\t" \
  "v_mfma_f32_16x16x32_bf16 a[52:55], a[204:207], v[84:87], a[52:55]\n\t" \
  "buffer_load_dwordx4 v[156:159], v32, %3, s68 offen offset:3072 sc1\n\t" \
  "v_mfma_f32_16x16x32_bf16 a[56:59], a[204:207], v[88:91], a[56:59]\n\t" \
  "v_fma_f32 v57, v47, v58, v59\n\t" \
  "v_fmac_f32 v47, v57, v51\n\t" \
  "v_mfma_f32_16x16x32_bf16 a[60:63], a[204:207], v[92:95], a[60:63]\n\t" \
  "v_accvgpr_write_b32 a246, v44\n\t" \
  "s_waitcnt lgkmcnt(4)\n\t" \
  "v_mfma_f32_16x16x32_bf16 a[64:67], a[208:211], v[80:83], a[64:67]\n\t" \
  "ds_read_b64_tr_b16 a[200:201], v28 offset:14336\n\t" \
  "ds_read_b64_tr_b16 a[202:203], v28 offset:17408\n\t" \
  "v_mfma_f32_16x16x32_bf16 a[68:71], a[208:211], v[84:87], a[68:71]\n\t" \
  "v_accvgpr_write_b32 a247, v45\n\t" \
  "v_accvgpr_write_b32 a248, v46\n\t" \
  "v_mfma_f32_16x16x32_bf16 a[72:75], a[208:211], v[88:91], a[72:75]\n\t" \
  "s_add_u32 s68, s47, 0\n\t" \
  "v_accvgpr_write_b32 a249, v47\n\t" \
  "v_mfma_f32_16x16x32_bf16 a[76:79], a[208:211], v[92:95], a[76:79]\n\t" \
  "buffer_load_dwordx4 v[64:67], v32, %3, s68 offen offset:0 sc1\n\t" \
  "v_mfma_f32_16x16x32_bf16 a[80:83], a[212:215], v[80:83], a[80:83]\n\t" \
  "ds_read_b64_tr_b16 a[204:205], v29 offset:14336\n\t" \
  "ds_read_b64_tr_b16 a[206:207], v29 offset:17408\n\t" \
  "v_mfma_f32_16x16x32_bf16 a[84:87], a[212:215], v[84:87], a[84:87]\n\t" \
  "v_accvgpr_read_b32 v52, a232\n\t" \
  "v_accvgpr_read_b32 v53, a233\n\t" \
  "v_mfma_f32_16x16x32_bf16 a[88:91], a[212:215], v[88:91], a[88:91]\n\t" \
  "v_accvgpr_read_b32 v54, a220\n\t" \
  "v_accvgpr_read_b32 v55, a221\n\t" \
  "v_mfma_f32_16x16x32_bf16 a[92:95], a[212:215], v[92:95], a[92:95]\n\t" \
  "v_lshlrev_b32 v48, 16, v52\n\t" \
  "s_waitcnt lgkmcnt(4)\n\t" \
  "v_mfma_f32_16x16x32_bf16 a[96:99], a[192:195], v[80:83], a[96:99]\n\t" \
  "ds_read_b64_tr_b16 a[208:209], v28 offset:20480\n\t" \
  "ds_read_b64_tr_b16 a[210:211], v28 offset:23552\n\t" \
  "v_mfma_f32_16x16x32_bf16 a[100:103], a[192:195], v[84:87], a[100:103]\n\t" \
  "s_add_u32 m0, s40, 24576\n\t" \
  "s_add_u32 s43, s53, 110592\n\t" \
  "v_mfma_f32_16x16x32_bf16 a[104:107], a[192:195], v[88:91], a[104:107]\n\t" \
  "buffer_load_dwordx4 v17, %2, s43 offen offset:0 lds\n\t" \
  "v_mfma_f32_16x16x32_bf16 a[108:111], a[192:195], v[92:95], a[108:111]\n\t" \
  "v_and_b32 v49, 0xffff0000, v52\n\t" \
  "v_lshlrev_b32 v50, 16, v53\n\t" \
  "v_mfma_f32_16x16x32_bf16 a[112:115], a[196:199], v[80:83], a[112:115]\n\t" \
  "ds_read_b64_tr_b16 a[212:213], v29 offset:20480\n\t" \
  "ds_read_b64_tr_b16 a[214:215], v29 offset:23552\n\t" \
  "v_mfma_f32_16x16x32_bf16 a[116:119], a[196:199], v[84:87], a[116:119]\n\t" \
  "buffer_load_dwordx4 v18, %2, s43 offen offset:1024 lds\n\t" \
  "v_mfma_f32_16x16x32_bf16 a[120:123], a[196:199], v[88:91], a[120:123]\n\t" \
  "v_and_b32 v51, 0xffff0000, v53\n\t" \
  "v_lshlrev_b32 v44, 16, v54\n\t" \
  "v_mfma_f32_16x16x32_bf16 a[124:127], a[196:199], v[92:95], a[124:127]\n\t" \
  "s_waitcnt lgkmcnt(4)\n\t" \
  "s_barrier\n\t" \
  "v_mfma_f32_16x16x32_bf16 a[128:131], a[200:203], v[80:83], a[128:131]\n\t" \
  "ds_read_b64_tr_b16 a[192:193], v28 offset:24576\n\t" \
  "ds_read_b64_tr_b16 a[194:195], v28 offset:27648\n\t" \
  "v_mfma_f32_16x16x32_bf16 a[132:135], a[200:203], v[84:87], a[132:135]\n\t" \
  "buffer_load_dwordx4 v[68:71], v32, %3, s68 offen offset:1024 sc1\n\t" \
  "v_mfma_f32_16x16x32_bf16 a[136:139], a[200:203], v[88:91], a[136:139]\n\t" \
  "v_and_b32 v45, 0xffff0000, v54\n\t" \
  "v_lshlrev_b32 v46, 16, v55\n\t" \
  "v_mfma_f32_16x16x32_bf16 a[140:143], a[200:203], v[92:95], a[140:143]\n\t" \
  "buffer_load_dwordx4 v19, %2, s43 offen offset:2048 lds\n\t" \
  "v_mfma_f32_16x16x32_bf16 a[144:147], a[204:207], v[80:83], a[144:147]\n\t" \
  "ds_read_b64_tr_b16 a[196:197], v29 offset:24576\n\t" \
  "ds_read_b64_tr_b16 a[198:199], v29 offset:27648\n\t" \
  "v_mfma_f32_16x16x32_bf16 a[148:151], a[204:207], v[84:87], a[148:151]\n\t" \
  "buffer_load_dwordx4 v[72:75], v32, %3, s68 offen offset:2048 sc1\n\t" \
  "v_mfma_f32_16x16x32_bf16 a[152:155], a[204:207], v[88:91], a[152:155]\n\t" \
  "v_and_b32 v47, 0xffff0000, v55\n\t" \
  "v_fma_f32 v56, v44, v58, v59\n\t" \
  "v_mfma_f32_16x16x32_bf16 a[156:159], a[204:207], v[92:95], a[156:159]\n\t" \
  "v_fmac_f32 v44, v56, v48\n\t" \
  "s_waitcnt lgkmcnt(4)\n\t" \
  "v_mfma_f32_16x16x32_bf16 a[160:163], a[208:211], v[80:83], a[160:163]\n\t" \
  "ds_read_b64_tr_b16 a[200:201], v28 offset:30720\n\t" \
  "ds_read_b64_tr_b16 a[202:203], v28 offset:33792\n\t" \
  "v_mfma_f32_16x16x32_bf16 a[164:167], a[208:211], v[84:87], a[164:167]\n\t" \
  "buffer_load_dwordx4 v[76:79], v32, %3, s68 offen offset:3072 sc1\n\t" \
  "v_mfma_f32_16x16x32_bf16 a[168:171], a[208:211], v[88:91], a[168:171]\n\t" \
  "v_fma_f32 v57, v45, v58, v59\n\t" \
  "v_fmac_f32 v45, v57, v49\n\t" \
  "v_mfma_f32_16x16x32_bf16 a[172:175], a[208:211], v[92:95], a[172:175]\n\t" \
  "v_fma_f32 v56, v46, v58, v59\n\t" \
  "v_fmac_f32 v46, v56, v50\n\t" \
  "v_mfma_f32_16x16x32_bf16 a[176:179], a[212:215], v[80:83], a[176:179]\n\t" \
  "ds_read_b64_tr_b16 a[204:205], v29 offset:30720\n\t" \
  "ds_read_b64_tr_b16 a[206:207], v29 offset:33792\n\t" \
  "v_mfma_f32_16x16x32_bf16 a[180:183], a[212:215], v[84:87], a[180:183]\n\t" \
  "s_add_u32 s68, s47, 4096\n\t" \
  "v_fma_f32 v57, v47, v58, v59\n\t" \
  "v_mfma_f32_16x16x32_bf16 a[184:187], a[212:215], v[88:91], a[184:187]\n\t" \
  "buffer_load_dwordx4 v[96:99], v32, %3, s68 offen offset:0 sc1\n\t" \
  "v_mfma_f32_16x16x32_bf16 a[188:191], a[212:215], v[92:95], a[188:191]\n\t" \
  "v_fmac_f32 v47, v57, v51\n\t" \
  "s_waitcnt vmcnt(13) lgkmcnt(4)\n\t" \
  "v_mfma_f32_16x16x32_bf16 a[0:3], a[192:195], v[112:115], a[0:3]\n\t" \
  "ds_read_b64_tr_b16 a[208:209], v28 offset:25600\n\t" \
  "ds_read_b64_tr_b16 a[210:211], v28 offset:28672\n\t" \
  "v_mfma_f32_16x16x32_bf16 a[4:7], a[192:195], v[116:119], a[4:7]\n\t" \
  "v_accvgpr_write_b32 a250, v44\n\t" \
  "v_accvgpr_write_b32 a251, v45\n\t" \
  "v_mfma_f32_16x16x32_bf16 a[8:11], a[192:195], v[120:123], a[8:11]\n\t" \
  "buffer_load_dwordx4 v[100:103], v32, %3, s68 offen offset:1024 sc1\n\t" \
  "v_mfma_f32_16x16x32_bf16 a[12:15], a[192:195], v[124:127], a[12:15]\n\t" \
  "v_accvgpr_write_b32 a252, v46\n\t" \
  "v_accvgpr_write_b32 a253, v47\n\t" \
  "v_mfma_f32_16x16x32_bf16 a[16:19], a[196:199], v[112:115], a[16:19]\n\t" \
  "ds_read_b64_tr_b16 a[212:213], v29 offset:25600\n\t" \
  "ds_read_b64_tr_b16 a[214:215], v29 offset:28672\n\t" \
  "v_mfma_f32_16x16x32_bf16 a[20:23], a[196:199], v[116:119], a[20:23]\n\t" \
  "v_accvgpr_read_b32 v52, a234\n\t" \
  "v_accvgpr_read_b32 v53, a235\n\t" \
  "v_mfma_f32_16x16x32_bf16 a[24:27], a[196:199], v[120:123], a[24:27]\n\t" \
  "v_accvgpr_read_b32 v54, a222\n\t" \
  "v_accvgpr_read_b32 v55, a223\n\t" \
  "v_mfma_f32_16x16x32_bf16 a[28:31], a[196:199], v[124:127], a[28:31]\n\t" \
  "v_lshlrev_b32 v48, 16, v52\n\t" \
  "s_waitcnt lgkmcnt(4)\n\t" \
  "v_mfma_f32_16x16x32_bf16 a[32:35], a[200:203], v[112:115], a[32:35]\n\t" \
  "ds_read_b64_tr_b16 a[192:193], v28 offset:31744\n\t" \
  "ds_read_b64_tr_b16 a[194:195], v28 offset:34816\n\t" \
  "v_mfma_f32_16x16x32_bf16 a[36:39], a[200:203], v[116:119], a[36:39]\n\t" \
  "buffer_load_dwordx4 v[104:107], v32, %3, s68 offen offset:2048 sc1\n\t" \
  "v_mfma_f32_16x16x32_bf16 a[40:43], a[200:203], v[120:123], a[40:43]\n\t" \
  "v_and_b32 v49, 0xffff0000, v52\n\t" \
  "v_lshlrev_b32 v50, 16, v53\n\t" \
  "v_mfma_f32_16x16x32_bf16 a[44:47], a[200:203], v[124:127], a[44:47]\n\t" \
  "v_and_b32 v51, 0xffff0000, v53\n\t" \
  "v_lshlrev_b32 v44, 16, v54\n\t" \
  "v_mfma_f32_16x16x32_bf16 a[48:51], a[204:207], v[112:115], a[48:51]\n\t" \
  "ds_read_b64_tr_b16 a[196:197], v29 offset:31744\n\t" \
  "ds_read_b64_tr_b16 a[198:199], v29 offset:34816\n\t" \
  "v_mfma_f32_16x16x32_bf16 a[52:55], a[204:207], v[116:119], a[52:55]\n\t" \
  "v_and_b32 v45, 0xffff0000, v54\n\t" \
  "v_lshlrev_b32 v46, 16, v55\n\t" \
  "v_mfma_f32_16x16x32_bf16 a[56:59], a[204:207], v[120:123], a[56:59]\n\t" \
  "v_and_b32 v47, 0xffff0000, v55\n\t" \
  "v_fma_f32 v56, v44, v58, v59\n\t" \
  "v_mfma_f32_16x16x32_bf16 a[60:63], a[204:207], v[124:127], a[60:63]\n\t" \
  "v_fmac_f32 v44, v56, v48\n\t" \
  "s_waitcnt lgkmcnt(4)\n\t" \
  "v_mfma_f32_16x16x32_bf16 a[64:67], a[208:211], v[112:115], a[64:67]\n\t" \
  "ds_read_b64_tr_b16 a[200:201], v28 offset:26624\n\t" \
  "ds_read_b64_tr_b16 a[202:203], v28 offset:29696\n\t" \
  "v_mfma_f32_16x16x32_bf16 a[68:71], a[208:211], v[116:119], a[68:71]\n\t" \
  "buffer_load_dwordx4 v[108:111], v32, %3, s68 offen offset:3072 sc1\n\t" \
  "v_mfma_f32_16x16x32_bf16 a[72:75], a[208:211], v[120:123], a[72:75]\n\t" \
  "v_fma_f32 v57, v45, v58, v59\n\t" \
  "v_fmac_f32 v45, v57, v49\n\t" \
  "v_mfma_f32_16x16x32_bf16 a[76:79], a[208:211], v[124:127], a[76:79]\n\t" \
  "v_fma_f32 v56, v46, v58, v59\n\t" \
  "v_fmac_f32 v46, v56, v50\n\t" \
  "v_mfma_f32_16x16x32_bf16 a[80:83], a[212:215], v[112:115], a[80:83]\n\t" \
  "ds_read_b64_tr_b16 a[204:205], v29 offset:26624\n\t" \
  "ds_read_b64_tr_b16 a[206:207], v29 offset:29696\n\t" \
  "v_mfma_f32_16x16x32_bf16 a[84:87], a[212:215], v[116:119], a[84:87]\n\t" \
  "v_fma_f32 v57, v47, v58, v59\n\t" \
  "v_fmac_f32 v47, v57, v51\n\t" \
  "v_mfma_f32_16x16x32_bf16 a[88:91], a[212:215], v[120:123], a[88:91]\n\t" \
  "s_add_u32 s68, s47, 8192\n\t" \
  "v_accvgpr_write_b32 a216, v44\n\t" \
  "v_mfma_f32_16x16x32_bf16 a[92:95], a[212:215], v[124:127], a[92:95]\n\t" \
  "v_accvgpr_write_b32 a217, v45\n\t" \
  "s_waitcnt lgkmcnt(4)\n\t" \
  "v_mfma_f32_16x16x32_bf16 a[96:99], a[192:195], v[112:115], a[96:99]\n\t" \
  "ds_read_b64_tr_b16 a[208:209], v28 offset:32768\n\t" \
  "ds_read_b64_tr_b16 a[210:211], v28 offset:35840\n\t" \
  "v_mfma_f32_16x16x32_bf16 a[100:103], a[192:195], v[116:119], a[100:103]\n\t" \
  "s_add_u32 m0, s40, 61440\n\t" \
  "s_add_u32 s43, s52, 110720\n\t" \
  "v_mfma_f32_16x16x32_bf16 a[104:107], a[192:195], v[120:123], a[104:107]\n\t" \
  "buffer_load_dwordx4 v17, %2, s43 offen offset:0 lds\n\t" \
  "v_mfma_f32_16x16x32_bf16 a[108:111], a[192:195], v[124:127], a[108:111]\n\t" \
  "v_accvgpr_write_b32 a228, v46\n\t" \
  "v_accvgpr_write_b32 a229, v47\n\t" \
  "v_mfma_f32_16x16x32_bf16 a[112:115], a[196:199], v[112:115], a[112:115]\n\t" \
  "ds_read_b64_tr_b16 a[212:213], v29 offset:32768\n\t" \
  "ds_read_b64_tr_b16 a[214:215], v29 offset:35840\n\t" \
  "v_mfma_f32_16x16x32_bf16 a[116:119], a[196:199], v[116:119], a[116:119]\n\t" \
  "buffer_load_dwordx4 v18, %2, s43 offen offset:1024 lds\n\t" \
  "v_mfma_f32_16x16x32_bf16 a[120:123], a[196:199], v[120:123], a[120:123]\n\t" \
  "buffer_load_dwordx4 v[128:131], v32, %3, s68 offen offset:0 sc1\n\t" \
  "v_mfma_f32_16x16x32_bf16 a[124:127], a[196:199], v[124:127], a[124:127]\n\t" \
  "s_waitcnt lgkmcnt(4)\n\t" \
  "s_barrier\n\t" \
  "v_mfma_f32_16x16x32_bf16 a[128:131], a[200:203], v[112:115], a[128:131]\n\t" \
  "ds_read_b64_tr_b16 a[192:193], v24 offset:36864\n\t" \
  "ds_read_b64_tr_b16 a[194:195], v24 offset:39936\n\t" \
  "v_mfma_f32_16x16x32_bf16 a[132:135], a[200:203], v[116:119], a[132:135]\n\t" \
  "buffer_load_dwordx4 v[132:135], v32, %3, s68 offen offset:1024 sc1\n\t" \
  "v_mfma_f32_16x16x32_bf16 a[136:139], a[200:203], v[120:123], a[136:139]\n\t" \
  "v_accvgpr_read_b32 v52, a236\n\t" \
  "v_accvgpr_read_b32 v53, a237\n\t" \
  "v_mfma_f32_16x16x32_bf16 a[140:143], a[200:203], v[124:127], a[140:143]\n\t" \
  "buffer_load_dwordx4 v19, %2, s43 offen offset:2048 lds\n\t" \
  "v_mfma_f32_16x16x32_bf16 a[144:147], a[204:207], v[112:115], a[144:147]\n\t" \
  "ds_read_b64_tr_b16 a[196:197], v25 offset:36864\n\t" \
  "ds_read_b64_tr_b16 a[198:199], v25 offset:39936\n\t" \
  "v_mfma_f32_16x16x32_bf16 a[148:151], a[204:207], v[116:119], a[148:151]\n\t" \
  "buffer_load_dwordx4 v[136:139], v32, %3, s68 offen offset:2048 sc1\n\t" \
  "v_mfma_f32_16x16x32_bf16 a[152:155], a[204:207], v[120:123], a[152:155]\n\t" \
  "v_accvgpr_read_b32 v54, a224\n\t" \
  "v_accvgpr_read_b32 v55, a225\n\t" \
  "v_mfma_f32_16x16x32_bf16 a[156:159], a[204:207], v[124:127], a[156:159]\n\t" \
  "v_lshlrev_b32 v48, 16, v52\n\t" \
  "s_waitcnt lgkmcnt(4)\n\t" \
  "v_mfma_f32_16x16x32_bf16 a[160:163], a[208:211], v[112:115], a[160:163]\n\t" \
  "ds_read_b64_tr_b16 a[200:201], v24 offset:43008\n\t" \
  "ds_read_b64_tr_b16 a[202:203], v24 offset:46080\n\t" \
  "v_mfma_f32_16x16x32_bf16 a[164:167], a[208:211], v[116:119], a[164:167]\n\t" \
  "buffer_load_dwordx4 v[140:143], v32, %3, s68 offen offset:3072 sc1\n\t" \
  "v_mfma_f32_16x16x32_bf16 a[168:171], a[208:211], v[120:123], a[168:171]\n\t" \
  "v_and_b32 v49, 0xffff0000, v52\n\t" \
  "v_lshlrev_b32 v50, 16, v53\n\t" \
  "v_mfma_f32_16x16x32_bf16 a[172:175], a[208:211], v[124:127], a[172:175]\n\t" \
  "v_and_b32 v51, 0xffff0000, v53\n\t" \
  "v_lshlrev_b32 v44, 16, v54\n\t" \
  "v_mfma_f32_16x16x32_bf16 a[176:179], a[212:215], v[112:115], a[176:179]\n\t" \
  "ds_read_b64_tr_b16 a[204:205], v25 offset:43008\n\t" \
  "ds_read_b64_tr_b16 a[206:207], v25 offset:46080\n\t" \
  "v_mfma_f32_16x16x32_bf16 a[180:183], a[212:215], v[116:119], a[180:183]\n\t" \
  "v_and_b32 v45, 0xffff0000, v54\n\t" \
  "v_lshlrev_b32 v46, 16, v55\n\t" \
  "v_mfma_f32_16x16x32_bf16 a[184:187], a[212:215], v[120:123], a[184:187]\n\t" \
  "v_and_b32 v47, 0xffff0000, v55\n\t" \
  "v_fma_f32 v56, v44, v58, v59\n\t" \
  "v_mfma_f32_16x16x32_bf16 a[188:191], a[212:215], v[124:127], a[188:191]\n\t" \
  "v_fmac_f32 v44, v56, v48\n\t" \
  "s_waitcnt vmcnt(18) lgkmcnt(4)\n\t" \
  "v_mfma_f32_16x16x32_bf16 a[0:3], a[192:195], v[144:147], a[0:3]\n\t" \
  "ds_read_b64_tr_b16 a[208:209], v24 offset:37888\n\t" \
  "ds_read_b64_tr_b16 a[210:211], v24 offset:40960\n\t" \
  "v_mfma_f32_16x16x32_bf16 a[4:7], a[192:195], v[148:151], a[4:7]\n\t" \
  "v_cmp_eq_u64 vcc, v[36:37], v[38:39]\n\t" \
  "s_nop 1\n\t" \
  "v_mfma_f32_16x16x32_bf16 a[8:11], a[192:195], v[152:155], a[8:11]\n\t" \
  "s_cmp_eq_u64 vcc, -1\n\t" \
  "s_cbranch_scc0 L_slow3_%=\n\t" \
  "L_back3_%=:\n\t" \
  "v_mfma_f32_16x16x32_bf16 a[12:15], a[192:195], v[156:159], a[12:15]\n\t" \
  "v_fma_f32 v57, v45, v58, v59\n\t" \
  "v_fmac_f32 v45, v57, v49\n\t" \
  "v_mfma_f32_16x16x32_bf16 a[16:19], a[196:199], v[144:147], a[16:19]\n\t" \
  "ds_read_b64_tr_b16 a[212:213], v25 offset:37888\n\t" \
  "ds_read_b64_tr_b16 a[214:215], v25 offset:40960\n\t" \
  "v_mfma_f32_16x16x32_bf16 a[20:23], a[196:199], v[148:151], a[20:23]\n\t" \
  "v_fma_f32 v56, v46, v58, v59\n\t" \
  "v_fmac_f32 v46, v56, v50\n\t" \
  "v_mfma_f32_16x16x32_bf16 a[24:27], a[196:199], v[152:155], a[24:27]\n\t" \
  "s_add_u32 s68, s46, 12288\n\t" \
  "v_fma_f32 v57, v47, v58, v59\n\t" \
  "v_mfma_f32_16x16x32_bf16 a[28:31], a[196:199], v[156:159], a[28:31]\n\t" \
  "v_fmac_f32 v47, v57, v51\n\t" \
  "s_waitcnt lgkmcnt(4)\n\t" \
  "v_mfma_f32_16x16x32_bf16 a[32:35], a[200:203], v[144:147], a[32:35]\n\t" \
  "ds_read_b64_tr_b16 a[192:193], v24 offset:44032\n\t" \
  "ds_read_b64_tr_b16 a[194:195], v24 offset:47104\n\t" \
  "v_mfma_f32_16x16x32_bf16 a[36:39], a[200:203], v[148:151], a[36:39]\n\t" \
  "buffer_load_dwordx4 v[176:179], v32, %3, s68 offen offset:0 sc1\n\t" \
  "v_mfma_f32_16x16x32_bf16 a[40:43], a[200:203], v[152:155], a[40:43]\n\t" \
  "v_accvgpr_write_b32 a218, v44\n\t" \
  "v_accvgpr_write_b32 a219, v45\n\t" \
  "v_mfma_f32_16x16x32_bf16 a[44:47], a[200:203], v[156:159], a[44:47]\n\t" \
  "v_accvgpr_write_b32 a230, v46\n\t" \
  "v_accvgpr_write_b32 a231, v47\n\t" \
  "v_mfma_f32_16x16x32_bf16 a[48:51], a[204:207], v[144:147], a[48:51]\n\t" \
  "ds_read_b64_tr_b16 a[196:197], v25 offset:44032\n\t" \
  "ds_read_b64_tr_b16 a[198:199], v25 offset:47104\n\t" \
  "v_mfma_f32_16x16x32_bf16 a[52:55], a[204:207], v[148:151], a[52:55]\n\t" \
  "buffer_load_dwordx4 v[180:183], v32, %3, s68 offen offset:1024 sc1\n\t" \
  "v_mfma_f32_16x16x32_bf16 a[56:59], a[204:207], v[152:155], a[56:59]\n\t" \
  "v_accvgpr_read_b32 v52, a238\n\t" \
  "v_accvgpr_read_b32 v53, a239\n\t" \
  "v_mfma_f32_16x16x32_bf16 a[60:63], a[204:207], v[156:159], a[60:63]\n\t" \
  "v_accvgpr_read_b32 v54, a226\n\t" \
  "s_waitcnt lgkmcnt(4)\n\t" \
  "v_mfma_f32_16x16x32_bf16 a[64:67], a[208:211], v[144:147], a[64:67]\n\t" \
  "ds_read_b64_tr_b16 a[200:201], v24 offset:38912\n\t" \
  "ds_read_b64_tr_b16 a[202:203], v24 offset:41984\n\t" \
  "v_mfma_f32_16x16x32_bf16 a[68:71], a[208:211], v[148:151], a[68:71]\n\t" \
  "v_accvgpr_read_b32 v55, a227\n\t" \
  "v_lshlrev_b32 v48, 16, v52\n\t" \
  "v_mfma_f32_16x16x32_bf16 a[72:75], a[208:211], v[152:155], a[72:75]\n\t" \
  "buffer_load_dwordx4 v[184:187], v32, %3, s68 offen offset:2048 sc1\n\t" \
  "v_mfma_f32_16x16x32_bf16 a[76:79], a[208:211], v[156:159], a[76:79]\n\t" \
  "v_and_b32 v49, 0xffff0000, v52\n\t" \
  "v_lshlrev_b32 v50, 16, v53\n\t" \
  "v_mfma_f32_16x16x32_bf16 a[80:83], a[212:215], v[144:147], a[80:83]\n\t" \
  "ds_read_b64_tr_b16 a[204:205], v25 offset:38912\n\t" \
  "ds_read_b64_tr_b16 a[206:207], v25 offset:41984\n\t" \
  "v_mfma_f32_16x16x32_bf16 a[84:87], a[212:215], v[148:151], a[84:87]\n\t" \
  "v_and_b32 v51, 0xffff0000, v53\n\t" \
  "v_lshlrev_b32 v44, 16, v54\n\t" \
  "v_mfma_f32_16x16x32_bf16 a[88:91], a[212:215], v[152:155], a[88:91]\n\t" \
  "v_and_b32 v45, 0xffff0000, v54\n\t" \
  "v_lshlrev_b32 v46, 16, v55\n\t" \
  "v_mfma_f32_16x16x32_bf16 a[92:95], a[212:215], v[156:159], a[92:95]\n\t" \
  "v_and_b32 v47, 0xffff0000, v55\n\t" \
  "s_waitcnt lgkmcnt(4)\n\t" \
  "v_mfma_f32_16x16x32_bf16 a[96:99], a[192:195], v[144:147], a[96:99]\n\t" \
  "ds_read_b64_tr_b16 a[208:209], v24 offset:45056\n\t" \
  "ds_read_b64_tr_b16 a[210:211], v24 offset:48128\n\t" \
  "v_mfma_f32_16x16x32_bf16 a[100:103], a[192:195], v[148:151], a[100:103]\n\t" \
  "s_add_u32 m0, s40, 73728\n\t" \
  "s_add_u32 s43, s53, 110720\n\t" \
  "v_mfma_f32_16x16x32_bf16 a[104:107], a[192:195], v[152:155], a[104:107]\n\t" \
  "buffer_load_dwordx4 v17, %2, s43 offen offset:0 lds\n\t" \
  "v_mfma_f32_16x16x32_bf16 a[108:111], a[192:195], v[156:159], a[108:111]\n\t" \
  "v_fma_f32 v56, v44, v58, v59\n\t" \
  "v_fmac_f32 v44, v56, v48\n\t" \
  "v_mfma_f32_16x16x32_bf16 a[112:115], a[196:199], v[144:147], a[112:115]\n\t" \
  "ds_read_b64_tr_b16 a[212:213], v25 offset:45056\n\t" \
  "ds_read_b64_tr_b16 a[214:215], v25 offset:48128\n\t" \
  "v_mfma_f32_16x16x32_bf16 a[116:119], a[196:199], v[148:151], a[116:119]\n\t" \
  "buffer_load_dwordx4 v18, %2, s43 offen offset:1024 lds\n\t" \
  "v_mfma_f32_16x16x32_bf16 a[120:123], a[196:199], v[152:155], a[120:123]\n\t" \
  "v_fma_f32 v57, v45, v58, v59\n\t" \
  "v_fmac_f32 v45, v57, v49\n\t" \
  "v_mfma_f32_16x16x32_bf16 a[124:127], a[196:199], v[156:159], a[124:127]\n\t" \
  "s_waitcnt lgkmcnt(4)\n\t" \
  "s_barrier\n\t" \
  "v_mfma_f32_16x16x32_bf16 a[128:131], a[200:203], v[144:147], a[128:131]\n\t" \
  "ds_read_b64_tr_b16 a[192:193], v24 offset:49152\n\t" \
  "ds_read_b64_tr_b16 a[194:195], v24 offset:52224\n\t" \
  "v_mfma_f32_16x16x32_bf16 a[132:135], a[200:203], v[148:151], a[132:135]\n\t" \
  "buffer_load_dwordx4 v[188:191], v32, %3, s68 offen offset:3072 sc1\n\t" \
  "v_mfma_f32_16x16x32_bf16 a[136:139], a[200:203], v[152:155], a[136:139]\n\t" \
  "s_add_u32 s68, s46, 16384\n\t" \
  "v_fma_f32 v56, v46, v58, v59\n\t" \
  "v_mfma_f32_16x16x32_bf16 a[140:143], a[200:203], v[156:159], a[140:143]\n\t" \
  "buffer_load_dwordx4 v19, %2, s43 offen offset:2048 lds\n\t" \
  "v_mfma_f32_16x16x32_bf16 a[144:147], a[204:207], v[144:147], a[144:147]\n\t" \
  "ds_read_b64_tr_b16 a[196:197], v25 offset:49152\n\t" \
  "ds_read_b64_tr_b16 a[198:199], v25 offset:52224\n\t" \
  "v_mfma_f32_16x16x32_bf16 a[148:151], a[204:207], v[148:151], a[148:151]\n\t" \
  "buffer_load_dwordx4 v[208:211], v32, %3, s68 offen offset:0 sc1\n\t" \
  "v_mfma_f32_16x16x32_bf16 a[152:155], a[204:207], v[152:155], a[152:155]\n\t" \
  "v_fmac_f32 v46, v56, v50\n\t" \
  "v_fma_f32 v57, v47, v58, v59\n\t" \
  "v_mfma_f32_16x16x32_bf16 a[156:159], a[204:207], v[156:159], a[156:159]\n\t" \
  "v_fmac_f32 v47, v57, v51\n\t" \
  "s_waitcnt lgkmcnt(4)\n\t" \
  "v_mfma_f32_16x16x32_bf16 a[160:163], a[208:211], v[144:147], a[160:163]\n\t" \
  "ds_read_b64_tr_b16 a[200:201], v24 offset:55296\n\t" \
  "ds_read_b64_tr_b16 a[202:203], v24 offset:58368\n\t" \
  "v_mfma_f32_16x16x32_bf16 a[164:167], a[208:211], v[148:151], a[164:167]\n\t" \
  "buffer_load_dwordx4 v[212:215], v32, %3, s68 offen offset:1024 sc1\n\t" \
  "v_mfma_f32_16x16x32_bf16 a[168:171], a[208:211], v[152:155], a[168:171]\n\t" \
  "v_accvgpr_write_b32 a220, v44\n\t" \
  "v_accvgpr_write_b32 a221, v45\n\t" \
  "v_mfma_f32_16x16x32_bf16 a[172:175], a[208:211], v[156:159], a[172:175]\n\t" \
  "v_accvgpr_write_b32 a232, v46\n\t" \
  "v_accvgpr_write_b32 a233, v47\n\t" \
  "v_mfma_f32_16x16x32_bf16 a[176:179], a[212:215], v[144:147], a[176:179]\n\t" \
  "ds_read_b64_tr_b16 a[204:205], v25 offset:55296\n\t" \
  "ds_read_b64_tr_b16 a[206:207], v25 offset:58368\n\t" \
  "v_mfma_f32_16x16x32_bf16 a[180:183], a[212:215], v[148:151], a[180:183]\n\t" \
  "buffer_load_dwordx4 v[216:219], v32, %3, s68 offen offset:2048 sc1\n\t" \
  "v_mfma_f32_16x16x32_bf16 a[184:187], a[212:215], v[152:155], a[184:187]\n\t" \
  "v_mfma_f32_16x16x32_bf16 a[188:191], a[212:215], v[156:159], a[188:191]\n\t" \
  "s_waitcnt vmcnt(10) lgkmcnt(4)\n\t" \
  "v_mfma_f32_16x16x32_bf16 a[0:3], a[192:195], v[64:67], a[0:3]\n\t" \
  "ds_read_b64_tr_b16 a[208:209], v24 offset:50176\n\t" \
  "ds_read_b64_tr_b16 a[210:211], v24 offset:53248\n\t" \
  "v_mfma_f32_16x16x32_bf16 a[4:7], a[192:195], v[68:71], a[4:7]\n\t" \
  "v_mfma_f32_16x16x32_bf16 a[8:11], a[192:195], v[72:75], a[8:11]\n\t" \
  "buffer_load_dwordx4 v[220:223], v32, %3, s68 offen offset:3072 sc1\n\t" \
  "v_mfma_f32_16x16x32_bf16 a[12:15], a[192:195], v[76:79], a[12:15]\n\t" \
  "v_mfma_f32_16x16x32_bf16 a[16:19], a[196:199], v[64:67], a[16:19]\n\t" \
  "ds_read_b64_tr_b16 a[212:213], v25 offset:50176\n\t" \
  "ds_read_b64_tr_b16 a[214:215], v25 offset:53248\n\t" \
  "v_mfma_f32_16x16x32_bf16 a[20:23], a[196:199], v[68:71], a[20:23]\n\t" \
  "v_mfma_f32_16x16x32_bf16 a[24:27], a[196:199], v[72:75], a[24:27]\n\t" \
  "v_mfma_f32_16x16x32_bf16 a[28:31], a[196:199], v[76:79], a[28:31]\n\t" \
  "s_waitcnt lgkmcnt(4)\n\t" \
  "v_mfma_f32_16x16x32_bf16 a[32:35], a[200:203], v[64:67], a[32:35]\n\t" \
  "ds_read_b64_tr_b16 a[192:193], v24 offset:56320\n\t" \
  "ds_read_b64_tr_b16 a[194:195], v24 offset:59392\n\t" \
  "v_mfma_f32_16x16x32_bf16 a[36:39], a[200:203], v[68:71], a[36:39]\n\t" \
  "s_add_u32 s68, s46, 20480\n\t" \
  "v_mfma_f32_16x16x32_bf16 a[40:43], a[200:203], v[72:75], a[40:43]\n\t" \
  "buffer_load_dwordx4 v[240:243], v32, %3, s68 offen offset:0 sc1\n\t" \
  "v_mfma_f32_16x16x32_bf16 a[44:47], a[200:203], v[76:79], a[44:47]\n\t" \
  "v_mfma_f32_16x16x32_bf16 a[48:51], a[204:207], v[64:67], a[48:51]\n\t" \
  "ds_read_b64_tr_b16 a[196:197], v25 offset:56320\n\t" \
  "ds_read_b64_tr_b16 a[198:199], v25 offset:59392\n\t" \
  "v_mfma_f32_16x16x32_bf16 a[52:55], a[204:207], v[68:71], a[52:55]\n\t" \
  "v_mfma_f32_16x16x32_bf16 a[56:59], a[204:207], v[72:75], a[56:59]\n\t" \
  "v_mfma_f32_16x16x32_bf16 a[60:63], a[204:207], v[76:79], a[60:63]\n\t" \
  "s_waitcnt lgkmcnt(4)\n\t" \
  "v_mfma_f32_16x16x32_bf16 a[0:3], a[208:211], v[96:99], a[0:3]\n\t" \
  "ds_read_b64_tr_b16 a[200:201], v24 offset:51200\n\t" \
  "ds_read_b64_tr_b16 a[202:203], v24 offset:54272\n\t" \
  "v_mfma_f32_16x16x32_bf16 a[4:7], a[208:211], v[100:103], a[4:7]\n\t" \
  "buffer_load_dwordx4 v[244:247], v32, %3, s68 offen offset:1024 sc1\n\t" \
  "v_mfma_f32_16x16x32_bf16 a[8:11], a[208:211], v[104:107], a[8:11]\n\t" \
  "v_mfma_f32_16x16x32_bf16 a[12:15], a[208:211], v[108:111], a[12:15]\n\t" \
  "v_mfma_f32_16x16x32_bf16 a[16:19], a[212:215], v[96:99], a[16:19]\n\t" \
  "ds_read_b64_tr_b16 a[204:205], v25 offset:51200\n\t" \
  "ds_read_b64_tr_b16 a[206:207], v25 offset:54272\n\t" \
  "v_mfma_f32_16x16x32_bf16 a[20:23], a[212:215], v[100:103], a[20:23]\n\t" \
  "v_mfma_f32_16x16x32_bf16 a[24:27], a[212:215], v[104:107], a[24:27]\n\t" \
  "buffer_load_dwordx4 v[248:251], v32, %3, s68 offen offset:2048 sc1\n\t" \
  "v_mfma_f32_16x16x32_bf16 a[28:31], a[212:215], v[108:111], a[28:31]\n\t" \
  "s_waitcnt lgkmcnt(4)\n\t" \
  "v_mfma_f32_16x16x32_bf16 a[32:35], a[192:195], v[96:99], a[32:35]\n\t" \
  "ds_read_b64_tr_b16 a[208:209], v24 offset:57344\n\t" \
  "ds_read_b64_tr_b16 a[210:211], v24 offset:60416\n\t" \
  "v_mfma_f32_16x16x32_bf16 a[36:39], a[192:195], v[100:103], a[36:39]\n\t" \
  "s_add_u32 m0, s40, 86016\n\t" \
  "s_add_u32 s43, s53, 256\n\t" \
  "v_mfma_f32_16x16x32_bf16 a[40:43], a[192:195], v[104:107], a[40:43]\n\t" \
  "buffer_load_dwordx4 v17, %2, s43 offen offset:0 lds\n\t" \
  "v_mfma_f32_16x16x32_bf16 a[44:47], a[192:195], v[108:111], a[44:47]\n\t" \
  "v_mfma_f32_16x16x32_bf16 a[48:51], a[196:199], v[96:99], a[48:51]\n\t" \
  "ds_read_b64_tr_b16 a[212:213], v25 offset:57344\n\t" \
  "ds_read_b64_tr_b16 a[214:215], v25 offset:60416\n\t" \
  "v_mfma_f32_16x16x32_bf16 a[52:55], a[196:199], v[100:103], a[52:55]\n\t" \
  "buffer_load_dwordx4 v18, %2, s43 offen offset:1024 lds\n\t" \
  "v_mfma_f32_16x16x32_bf16 a[56:59], a[196:199], v[104:107], a[56:59]\n\t" \
  "v_mfma_f32_16x16x32_bf16 a[60:63], a[196:199], v[108:111], a[60:63]\n\t" \
  "s_waitcnt lgkmcnt(4)\n\t" \
  "s_barrier\n\t" \
  "v_mfma_f32_16x16x32_bf16 a[0:3], a[200:203], v[128:131], a[0:3]\n\t" \
  "ds_read_b64_tr_b16 a[192:193], v24 offset:0\n\t" \
  "ds_read_b64_tr_b16 a[194:195], v24 offset:3072\n\t" \
  "v_mfma_f32_16x16x32_bf16 a[4:7], a[200:203], v[132:135], a[4:7]\n\t" \
  "buffer_load_dwordx4 v[252:255], v32, %3, s68 offen offset:3072 sc1\n\t" \
  "v_mfma_f32_16x16x32_bf16 a[8:11], a[200:203], v[136:139], a[8:11]\n\t" \
  "v_mfma_f32_16x16x32_bf16 a[12:15], a[200:203], v[140:143], a[12:15]\n\t" \
  "buffer_load_dwordx4 v19, %2, s43 offen offset:2048 lds\n\t" \
  "v_mfma_f32_16x16x32_bf16 a[16:19], a[204:207], v[128:131], a[16:19]\n\t" \
  "ds_read_b64_tr_b16 a[196:197], v25 offset:0\n\t" \
  "ds_read_b64_tr_b16 a[198:199], v25 offset:3072\n\t" \
  "v_mfma_f32_16x16x32_bf16 a[20:23], a[204:207], v[132:135], a[20:23]\n\t" \
  "s_add_u32 s68, s47, 12288\n\t" \
  "v_mfma_f32_16x16x32_bf16 a[24:27], a[204:207], v[136:139], a[24:27]\n\t" \
  "buffer_load_dwordx4 v[160:163], v32, %3, s68 offen offset:0 sc1\n\t" \
  "v_mfma_f32_16x16x32_bf16 a[28:31], a[204:207], v[140:143], a[28:31]\n\t" \
  "s_waitcnt lgkmcnt(4)\n\t" \
  "v_mfma_f32_16x16x32_bf16 a[32:35], a[208:211], v[128:131], a[32:35]\n\t" \
  "ds_read_b64_tr_b16 a[200:201], v24 offset:6144\n\t" \
  "ds_read_b64_tr_b16 a[202:203], v24 offset:9216\n\t" \
  "v_mfma_f32_16x16x32_bf16 a[36:39], a[208:211], v[132:135], a[36:39]\n\t" \
  "buffer_load_dwordx4 v[164:167], v32, %3, s68 offen offset:1024 sc1\n\t" \
  "v_mfma_f32_16x16x32_bf16 a[40:43], a[208:211], v[136:139], a[40:43]\n\t" \
  "v_mfma_f32_16x16x32_bf16 a[44:47], a[208:211], v[140:143], a[44:47]\n\t" \
  "v_mfma_f32_16x16x32_bf16 a[48:51], a[212:215], v[128:131], a[48:51]\n\t" \
  "ds_read_b64_tr_b16 a[204:205], v25 offset:6144\n\t" \
  "ds_read_b64_tr_b16 a[206:207], v25 offset:9216\n\t" \
  "v_mfma_f32_16x16x32_bf16 a[52:55], a[212:215], v[132:135], a[52:55]\n\t" \
  "v_mfma_f32_16x16x32_bf16 a[56:59], a[212:215], v[136:139], a[56:59]\n\t" \
  "v_mfma_f32_16x16x32_bf16 a[60:63], a[212:215], v[140:143], a[60:63]\n\t" \
  "s_waitcnt lgkmcnt(4)\n\t" \
  "v_mfma_f32_16x16x32_bf16 a[64:67], a[192:195], v[64:67], a[64:67]\n\t" \
  "ds_read_b64_tr_b16 a[208:209], v24 offset:1024\n\t" \
  "ds_read_b64_tr_b16 a[210:211], v24 offset:4096\n\t" \
  "v_mfma_f32_16x16x32_bf16 a[68:71], a[192:195], v[68:71], a[68:71]\n\t" \
  "buffer_load_dwordx4 v[168:171], v32, %3, s68 offen offset:2048 sc1\n\t" \
  "v_mfma_f32_16x16x32_bf16 a[72:75], a[192:195], v[72:75], a[72:75]\n\t" \
  "v_mfma_f32_16x16x32_bf16 a[76:79], a[192:195], v[76:79], a[76:79]\n\t" \
  "v_mfma_f32_16x16x32_bf16 a[80:83], a[196:199], v[64:67], a[80:83]\n\t" \
  "ds_read_b64_tr_b16 a[212:213], v25 offset:1024\n\t" \
  "ds_read_b64_tr_b16 a[214:215], v25 offset:4096\n\t" \
  "v_mfma_f32_16x16x32_bf16 a[84:87], a[196:199], v[68:71], a[84:87]\n\t" \
  "v_mfma_f32_16x16x32_bf16 a[88:91], a[196:199], v[72:75], a[88:91]\n\t" \
  "buffer_load_dwordx4 v[172:175], v32, %3, s68 offen offset:3072 sc1\n\t" \
  "v_mfma_f32_16x16x32_bf16 a[92:95], a[196:199], v[76:79], a[92:95]\n\t" \
  "s_waitcnt lgkmcnt(4)\n\t" \
  "v_mfma_f32_16x16x32_bf16 a[96:99], a[200:203], v[64:67], a[96:99]\n\t" \
  "ds_read_b64_tr_b16 a[192:193], v24 offset:7168\n\t" \
  "ds_read_b64_tr_b16 a[194:195], v24 offset:10240\n\t" \
  "v_mfma_f32_16x16x32_bf16 a[100:103], a[200:203], v[68:71], a[100:103]\n\t" \
  "v_mfma_f32_16x16x32_bf16 a[104:107], a[200:203], v[72:75], a[104:107]\n\t" \
  "v_mfma_f32_16x16x32_bf16 a[108:111], a[200:203], v[76:79], a[108:111]\n\t" \
  "v_mfma_f32_16x16x32_bf16 a[112:115], a[204:207], v[64:67], a[112:115]\n\t" \
  "ds_read_b64_tr_b16 a[196:197], v25 offset:7168\n\t" \
  "ds_read_b64_tr_b16 a[198:199], v25 offset:10240\n\t" \
  "v_mfma_f32_16x16x32_bf16 a[116:119], a[204:207], v[68:71], a[116:119]\n\t" \
  "s_add_u32 s68, s47, 16384\n\t" \
  "v_mfma_f32_16x16x32_bf16 a[120:123], a[204:207], v[72:75], a[120:123]\n\t" \
  "buffer_load_dwordx4 v[192:195], v32, %3, s68 offen offset:0 sc1\n\t" \
  "v_mfma_f32_16x16x32_bf16 a[124:127], a[204:207], v[76:79], a[124:127]\n\t" \
  "s_waitcnt lgkmcnt(4)\n\t" \
  "v_mfma_f32_16x16x32_bf16 a[64:67], a[208:211], v[96:99], a[64:67]\n\t" \
  "ds_read_b64_tr_b16 a[200:201], v24 offset:2048\n\t" \
  "ds_read_b64_tr_b16 a[202:203], v24 offset:5120\n\t" \
  "v_mfma_f32_16x16x32_bf16 a[68:71], a[208:211], v[100:103], a[68:71]\n\t" \
  "v_mfma_f32_16x16x32_bf16 a[72:75], a[208:211], v[104:107], a[72:75]\n\t" \
  "v_mfma_f32_16x16x32_bf16 a[76:79], a[208:211], v[108:111], a[76:79]\n\t" \
  "buffer_load_dwordx4 v[196:199], v32, %3, s68 offen offset:1024 sc1\n\t" \
  "v_mfma_f32_16x16x32_bf16 a[80:83], a[212:215], v[96:99], a[80:83]\n\t" \
  "ds_read_b64_tr_b16 a[204:205], v25 offset:2048\n\t" \
  "ds_read_b64_tr_b16 a[206:207], v25 offset:5120\n\t" \
  "v_mfma_f32_16x16x32_bf16 a[84:87], a[212:215], v[100:103], a[84:87]\n\t" \
  "v_mfma_f32_16x16x32_bf16 a[88:91], a[212:215], v[104:107], a[88:91]\n\t" \
  "v_mfma_f32_16x16x32_bf16 a[92:95], a[212:215], v[108:111], a[92:95]\n\t" \
  "s_waitcnt lgkmcnt(4)\n\t" \
  "v_mfma_f32_16x16x32_bf16 a[96:99], a[192:195], v[96:99], a[96:99]\n\t" \
  "ds_read_b64_tr_b16 a[208:209], v24 offset:8192\n\t" \
  "ds_read_b64_tr_b16 a[210:211], v24 offset:11264\n\t" \
  "v_mfma_f32_16x16x32_bf16 a[100:103], a[192:195], v[100:103], a[100:103]\n\t" \
  "s_add_u32 m0, s40, 98304\n\t" \
  "s_add_u32 s43, s52, 110848\n\t" \
  "v_mfma_f32_16x16x32_bf16 a[104:107], a[192:195], v[104:107], a[104:107]\n\t" \
  "buffer_load_dwordx4 v17, %2, s43 offen offset:0 lds\n\t" \
  "v_mfma_f32_16x16x32_bf16 a[108:111], a[192:195], v[108:111], a[108:111]\n\t" \
  "v_mfma_f32_16x16x32_bf16 a[112:115], a[196:199], v[96:99], a[112:115]\n\t" \
  "ds_read_b64_tr_b16 a[212:213], v25 offset:8192\n\t" \
  "ds_read_b64_tr_b16 a[214:215], v25 offset:11264\n\t" \
  "v_mfma_f32_16x16x32_bf16 a[116:119], a[196:199], v[100:103], a[116:119]\n\t" \
  "buffer_load_dwordx4 v18, %2, s43 offen offset:1024 lds\n\t" \
  "v_mfma_f32_16x16x32_bf16 a[120:123], a[196:199], v[104:107], a[120:123]\n\t" \
  "v_mfma_f32_16x16x32_bf16 a[124:127], a[196:199], v[108:111], a[124:127]\n\t" \
  "s_waitcnt lgkmcnt(4)\n\t" \
  "s_barrier\n\t" \
  "v_mfma_f32_16x16x32_bf16 a[64:67], a[200:203], v[128:131], a[64:67]\n\t" \
  "ds_read_b64_tr_b16 a[192:193], v24 offset:12288\n\t" \
  "ds_read_b64_tr_b16 a[194:195], v24 offset:15360\n\t" \
  "v_mfma_f32_16x16x32_bf16 a[68:71], a[200:203], v[132:135], a[68:71]\n\t" \
  "buffer_load_dwordx4 v[200:203], v32, %3, s68 offen offset:2048 sc1\n\t" \
  "v_mfma_f32_16x16x32_bf16 a[72:75], a[200:203], v[136:139], a[72:75]\n\t" \
  "v_mfma_f32_16x16x32_bf16 a[76:79], a[200:203], v[140:143], a[76:79]\n\t" \
  "buffer_load_dwordx4 v19, %2, s43 offen offset:2048 lds\n\t" \
  "v_mfma_f32_16x16x32_bf16 a[80:83], a[204:207], v[128:131], a[80:83]\n\t" \
  "ds_read_b64_tr_b16 a[196:197], v25 offset:12288\n\t" \
  "ds_read_b64_tr_b16 a[198:199], v25 offset:15360\n\t" \
  "v_mfma_f32_16x16x32_bf16 a[84:87], a[204:207], v[132:135], a[84:87]\n\t" \
  "buffer_load_dwordx4 v[204:207], v32, %3, s68 offen offset:3072 sc1\n\t" \
  "v_mfma_f32_16x16x32_bf16 a[88:91], a[204:207], v[136:139], a[88:91]\n\t" \
  "s_add_u32 s68, s47, 20480\n\t" \
  "v_mfma_f32_16x16x32_bf16 a[92:95], a[204:207], v[140:143], a[92:95]\n\t" \
  "s_waitcnt lgkmcnt(4)\n\t" \
  "v_mfma_f32_16x16x32_bf16 a[96:99], a[208:211], v[128:131], a[96:99]\n\t" \
  "ds_read_b64_tr_b16 a[200:201], v24 offset:18432\n\t" \
  "ds_read_b64_tr_b16 a[202:203], v24 offset:21504\n\t" \
  "v_mfma_f32_16x16x32_bf16 a[100:103], a[208:211], v[132:135], a[100:103]\n\t" \
  "buffer_load_dwordx4 v[224:227], v32, %3, s68 offen offset:0 sc1\n\t" \
  "v_mfma_f32_16x16x32_bf16 a[104:107], a[208:211], v[136:139], a[104:107]\n\t" \
  "v_mfma_f32_16x16x32_bf16 a[108:111], a[208:211], v[140:143], a[108:111]\n\t" \
  "v_mfma_f32_16x16x32_bf16 a[112:115], a[212:215], v[128:131], a[112:115]\n\t" \
  "ds_read_b64_tr_b16 a[204:205], v25 offset:18432\n\t" \
  "ds_read_b64_tr_b16 a[206:207], v25 offset:21504\n\t" \
  "v_mfma_f32_16x16x32_bf16 a[116:119], a[212:215], v[132:135], a[116:119]\n\t" \
  "buffer_load_dwordx4 v[228:231], v32, %3, s68 offen offset:1024 sc1\n\t" \
  "v_mfma_f32_16x16x32_bf16 a[120:123], a[212:215], v[136:139], a[120:123]\n\t" \
  "v_mfma_f32_16x16x32_bf16 a[124:127], a[212:215], v[140:143], a[124:127]\n\t" \
  "s_waitcnt vmcnt(14) lgkmcnt(4)\n\t" \
  "v_mfma_f32_16x16x32_bf16 a[0:3], a[192:195], v[176:179], a[0:3]\n\t" \
  "ds_read_b64_tr_b16 a[208:209], v24 offset:13312\n\t" \
  "ds_read_b64_tr_b16 a[210:211], v24 offset:16384\n\t" \
  "v_mfma_f32_16x16x32_bf16 a[4:7], a[192:195], v[180:183], a[4:7]\n\t" \
  "v_mfma_f32_16x16x32_bf16 a[8:11], a[192:195], v[184:187], a[8:11]\n\t" \
  "v_mfma_f32_16x16x32_bf16 a[12:15], a[192:195], v[188:191], a[12:15]\n\t" \
  "buffer_load_dwordx4 v[232:235], v32, %3, s68 offen offset:2048 sc1\n\t" \
  "v_mfma_f32_16x16x32_bf16 a[16:19], a[196:199], v[176:179], a[16:19]\n\t" \
  "ds_read_b64_tr_b16 a[212:213], v25 offset:13312\n\t" \
  "ds_read_b64_tr_b16 a[214:215], v25 offset:16384\n\t" \
  "v_mfma_f32_16x16x32_bf16 a[20:23], a[196:199], v[180:183], a[20:23]\n\t" \
  "v_mfma_f32_16x16x32_bf16 a[24:27], a[196:199], v[184:187], a[24:27]\n\t" \
  "v_mfma_f32_16x16x32_bf16 a[28:31], a[196:199], v[188:191], a[28:31]\n\t" \
  "s_waitcnt lgkmcnt(4)\n\t" \
  "v_mfma_f32_16x16x32_bf16 a[32:35], a[200:203], v[176:179], a[32:35]\n\t" \
  "ds_read_b64_tr_b16 a[192:193], v24 offset:19456\n\t" \
  "ds_read_b64_tr_b16 a[194:195], v24 offset:22528\n\t" \
  "v_mfma_f32_16x16x32_bf16 a[36:39], a[200:203], v[180:183], a[36:39]\n\t" \
  "buffer_load_dwordx4 v[236:239], v32, %3, s68 offen offset:3072 sc1\n\t" \
  "v_mfma_f32_16x16x32_bf16 a[40:43], a[200:203], v[184:187], a[40:43]\n\t" \
  "v_mfma_f32_16x16x32_bf16 a[44:47], a[200:203], v[188:191], a[44:47]\n\t" \
  "v_mfma_f32_16x16x32_bf16 a[48:51], a[204:207], v[176:179], a[48:51]\n\t" \
  "ds_read_b64_tr_b16 a[196:197], v25 offset:19456\n\t" \
  "ds_read_b64_tr_b16 a[198:199], v25 offset:22528\n\t" \
  "v_mfma_f32_16x16x32_bf16 a[52:55], a[204:207], v[180:183], a[52:55]\n\t" \
  "v_mfma_f32_16x16x32_bf16 a[56:59], a[204:207], v[184:187], a[56:59]\n\t" \
  "v_mfma_f32_16x16x32_bf16 a[60:63], a[204:207], v[188:191], a[60:63]\n\t" \
  "s_waitcnt lgkmcnt(4)\n\t" \
  "v_mfma_f32_16x16x32_bf16 a[0:3], a[208:211], v[208:211], a[0:3]\n\t" \
  "ds_read_b64_tr_b16 a[200:201], v24 offset:14336\n\t" \
  "ds_read_b64_tr_b16 a[202:203], v24 offset:17408\n\t" \
  "v_mfma_f32_16x16x32_bf16 a[4:7], a[208:211], v[212:215], a[4:7]\n\t" \
  "v_mfma_f32_16x16x32_bf16 a[8:11], a[208:211], v[216:219], a[8:11]\n\t" \
  "v_mfma_f32_16x16x32_bf16 a[12:15], a[208:211], v[220:223], a[12:15]\n\t" \
  "v_mfma_f32_16x16x32_bf16 a[16:19], a[212:215], v[208:211], a[16:19]\n\t" \
  "ds_read_b64_tr_b16 a[204:205], v25 offset:14336\n\t" \
  "ds_read_b64_tr_b16 a[206:207], v25 offset:17408\n\t" \
  "v_mfma_f32_16x16x32_bf16 a[20:23], a[212:215], v[212:215], a[20:23]\n\t" \
  "v_mfma_f32_16x16x32_bf16 a[24:27], a[212:215], v[216:219], a[24:27]\n\t" \
  "v_mfma_f32_16x16x32_bf16 a[28:31], a[212:215], v[220:223], a[28:31]\n\t" \
  "s_waitcnt lgkmcnt(4)\n\t" \
  "v_mfma_f32_16x16x32_bf16 a[32:35], a[192:195], v[208:211], a[32:35]\n\t" \
  "ds_read_b64_tr_b16 a[208:209], v24 offset:20480\n\t" \
  "ds_read_b64_tr_b16 a[210:211], v24 offset:23552\n\t" \
  "v_mfma_f32_16x16x32_bf16 a[36:39], a[192:195], v[212:215], a[36:39]\n\t" \
  "s_add_u32 m0, s40, 110592\n\t" \
  "s_add_u32 s43, s53, 110848\n\t" \
  "v_mfma_f32_16x16x32_bf16 a[40:43], a[192:195], v[216:219], a[40:43]\n\t" \
  "buffer_load_dwordx4 v17, %2, s43 offen offset:0 lds\n\t" \
  "v_mfma_f32_16x16x32_bf16 a[44:47], a[192:195], v[220:223], a[44:47]\n\t" \
  "v_mfma_f32_16x16x32_bf16 a[48:51], a[196:199], v[208:211], a[48:51]\n\t" \
  "ds_read_b64_tr_b16 a[212:213], v25 offset:20480\n\t" \
  "ds_read_b64_tr_b16 a[214:215], v25 offset:23552\n\t" \
  "v_mfma_f32_16x16x32_bf16 a[52:55], a[196:199], v[212:215], a[52:55]\n\t" \
  "buffer_load_dwordx4 v18, %2, s43 offen offset:1024 lds\n\t" \
  "v_mfma_f32_16x16x32_bf16 a[56:59], a[196:199], v[216:219], a[56:59]\n\t" \
  "v_mfma_f32_16x16x32_bf16 a[60:63], a[196:199], v[220:223], a[60:63]\n\t" \
  "s_waitcnt lgkmcnt(4)\n\t" \
  "s_barrier\n\t" \
  "v_mfma_f32_16x16x32_bf16 a[0:3], a[200:203], v[240:243], a[0:3]\n\t" \
  "ds_read_b64_tr_b16 a[192:193], v24 offset:24576\n\t" \
  "ds_read_b64_tr_b16 a[194:195], v24 offset:27648\n\t" \
  "v_mfma_f32_16x16x32_bf16 a[4:7], a[200:203], v[244:247], a[4:7]\n\t" \
  "v_mfma_f32_16x16x32_bf16 a[8:11], a[200:203], v[248:251], a[8:11]\n\t" \
  "buffer_load_dwordx4 v19, %2, s43 offen offset:2048 lds\n\t" \
  "v_mfma_f32_16x16x32_bf16 a[12:15], a[200:203], v[252:255], a[12:15]\n\t" \
  "v_mfma_f32_16x16x32_bf16 a[16:19], a[204:207], v[240:243], a[16:19]\n\t" \
  "ds_read_b64_tr_b16 a[196:197], v25 offset:24576\n\t" \
  "ds_read_b64_tr_b16 a[198:199], v25 offset:27648\n\t" \
  "v_mfma_f32_16x16x32_bf16 a[20:23], a[204:207], v[244:247], a[20:23]\n\t" \
  "v_mfma_f32_16x16x32_bf16 a[24:27], a[204:207], v[248:251], a[24:27]\n\t" \
  "v_mfma_f32_16x16x32_bf16 a[28:31], a[204:207], v[252:255], a[28:31]\n\t" \
  "s_waitcnt lgkmcnt(4)\n\t" \
  "v_mfma_f32_16x16x32_bf16 a[32:35], a[208:211], v[240:243], a[32:35]\n\t" \
  "ds_read_b64_tr_b16 a[200:201], v24 offset:30720\n\t" \
  "ds_read_b64_tr_b16 a[202:203], v24 offset:33792\n\t" \
  "v_mfma_f32_16x16x32_bf16 a[36:39], a[208:211], v[244:247], a[36:39]\n\t" \
  "v_mfma_f32_16x16x32_bf16 a[40:43], a[208:211], v[248:251], a[40:43]\n\t" \
  "v_mfma_f32_16x16x32_bf16 a[44:47], a[208:211], v[252:255], a[44:47]\n\t" \
  "v_mfma_f32_16x16x32_bf16 a[48:51], a[212:215], v[240:243], a[48:51]\n\t" \
  "ds_read_b64_tr_b16 a[204:205], v25 offset:30720\n\t" \
  "ds_read_b64_tr_b16 a[206:207], v25 offset:33792\n\t" \
  "v_mfma_f32_16x16x32_bf16 a[52:55], a[212:215], v[244:247], a[52:55]\n\t" \
  "v_mfma_f32_16x16x32_bf16 a[56:59], a[212:215], v[248:251], a[56:59]\n\t" \
  "v_mfma_f32_16x16x32_bf16 a[60:63], a[212:215], v[252:255], a[60:63]\n\t" \
  "s_waitcnt vmcnt(3) lgkmcnt(4)\n\t" \
  "v_mfma_f32_16x16x32_bf16 a[0:3], a[192:195], v[160:163], a[0:3]\n\t" \
  "ds_read_b64_tr_b16 a[208:209], v24 offset:25600\n\t" \
  "ds_read_b64_tr_b16 a[210:211], v24 offset:28672\n\t" \
  "v_mfma_f32_16x16x32_bf16 a[4:7], a[192:195], v[164:167], a[4:7]\n\t" \
  "v_mfma_f32_16x16x32_bf16 a[8:11], a[192:195], v[168:171], a[8:11]\n\t" \
  "v_mfma_f32_16x16x32_bf16 a[12:15], a[192:195], v[172:175], a[12:15]\n\t" \
  "v_mfma_f32_16x16x32_bf16 a[16:19], a[196:199], v[160:163], a[16:19]\n\t" \
  "ds_read_b64_tr_b16 a[212:213], v25 offset:25600\n\t" \
  "ds_read_b64_tr_b16 a[214:215], v25 offset:28672\n\t" \
  "v_mfma_f32_16x16x32_bf16 a[20:23], a[196:199], v[164:167], a[20:23]\n\t" \
  "v_mfma_f32_16x16x32_bf16 a[24:27], a[196:199], v[168:171], a[24:27]\n\t" \
  "v_mfma_f32_16x16x32_bf16 a[28:31], a[196:199], v[172:175], a[28:31]\n\t" \
  "s_waitcnt lgkmcnt(4)\n\t" \
  "v_mfma_f32_16x16x32_bf16 a[32:35], a[200:203], v[160:163], a[32:35]\n\t" \
  "ds_read_b64_tr_b16 a[192:193], v24 offset:31744\n\t" \
  "ds_read_b64_tr_b16 a[194:195], v24 offset:34816\n\t" \
  "v_mfma_f32_16x16x32_bf16 a[36:39], a[200:203], v[164:167], a[36:39]\n\t" \
  "v_mfma_f32_16x16x32_bf16 a[40:43], a[200:203], v[168:171], a[40:43]\n\t" \
  "v_mfma_f32_16x16x32_bf16 a[44:47], a[200:203], v[172:175], a[44:47]\n\t" \
  "v_mfma_f32_16x16x32_bf16 a[48:51], a[204:207], v[160:163], a[48:51]\n\t" \
  "ds_read_b64_tr_b16 a[196:197], v25 offset:31744\n\t" \
  "ds_read_b64_tr_b16 a[198:199], v25 offset:34816\n\t" \
  "v_mfma_f32_16x16x32_bf16 a[52:55], a[204:207], v[164:167], a[52:55]\n\t" \
  "v_mfma_f32_16x16x32_bf16 a[56:59], a[204:207], v[168:171], a[56:59]\n\t" \
  "v_mfma_f32_16x16x32_bf16 a[60:63], a[204:207], v[172:175], a[60:63]\n\t" \
  "s_waitcnt lgkmcnt(4)\n\t" \
  "v_mfma_f32_16x16x32_bf16 a[0:3], a[208:211], v[192:195], a[0:3]\n\t" \
  "ds_read_b64_tr_b16 a[200:201], v24 offset:26624\n\t" \
  "ds_read_b64_tr_b16 a[202:203], v24 offset:29696\n\t" \
  "v_mfma_f32_16x16x32_bf16 a[4:7], a[208:211], v[196:199], a[4:7]\n\t" \
  "v_mfma_f32_16x16x32_bf16 a[8:11], a[208:211], v[200:203], a[8:11]\n\t" \
  "v_mfma_f32_16x16x32_bf16 a[12:15], a[208:211], v[204:207], a[12:15]\n\t" \
  "v_mfma_f32_16x16x32_bf16 a[16:19], a[212:215], v[192:195], a[16:19]\n\t" \
  "ds_read_b64_tr_b16 a[204:205], v25 offset:26624\n\t" \
  "ds_read_b64_tr_b16 a[206:207], v25 offset:29696\n\t" \
  "v_mfma_f32_16x16x32_bf16 a[20:23], a[212:215], v[196:199], a[20:23]\n\t" \
  "v_mfma_f32_16x16x32_bf16 a[24:27], a[212:215], v[200:203], a[24:27]\n\t" \
  "v_mfma_f32_16x16x32_bf16 a[28:31], a[212:215], v[204:207], a[28:31]\n\t" \
  "s_waitcnt lgkmcnt(4)\n\t" \
  "v_mfma_f32_16x16x32_bf16 a[32:35], a[192:195], v[192:195], a[32:35]\n\t" \
  "ds_read_b64_tr_b16 a[208:209], v24 offset:32768\n\t" \
  "ds_read_b64_tr_b16 a[210:211], v24 offset:35840\n\t" \
  "v_mfma_f32_16x16x32_bf16 a[36:39], a[192:195], v[196:199], a[36:39]\n\t" \
  "v_mfma_f32_16x16x32_bf16 a[40:43], a[192:195], v[200:203], a[40:43]\n\t" \
  "v_mfma_f32_16x16x32_bf16 a[44:47], a[192:195], v[204:207], a[44:47]\n\t" \
  "v_mfma_f32_16x16x32_bf16 a[48:51], a[196:199], v[192:195], a[48:51]\n\t" \
  "ds_read_b64_tr_b16 a[212:213], v25 offset:32768\n\t" \
  "ds_read_b64_tr_b16 a[214:215], v25 offset:35840\n\t" \
  "v_mfma_f32_16x16x32_bf16 a[52:55], a[196:199], v[196:199], a[52:55]\n\t" \
  "v_mfma_f32_16x16x32_bf16 a[56:59], a[196:199], v[200:203], a[56:59]\n\t" \
  "v_mfma_f32_16x16x32_bf16 a[60:63], a[196:199], v[204:207], a[60:63]\n\t" \
  "s_waitcnt lgkmcnt(4)\n\t" \
  "s_barrier\n\t" \
  "v_mfma_f32_16x16x32_bf16 a[0:3], a[200:203], v[224:227], a[0:3]\n\t" \
  "ds_read_b64_tr_b16 a[192:193], v26 offset:0\n\t" \
  "ds_read_b64_tr_b16 a[194:195], v26 offset:3072\n\t" \
  "v_mfma_f32_16x16x32_bf16 a[4:7], a[200:203], v[228:231], a[4:7]\n\t" \
  "v_mfma_f32_16x16x32_bf16 a[8:11], a[200:203], v[232:235], a[8:11]\n\t" \
  "v_mfma_f32_16x16x32_bf16 a[12:15], a[200:203], v[236:239], a[12:15]\n\t" \
  "v_mfma_f32_16x16x32_bf16 a[16:19], a[204:207], v[224:227], a[16:19]\n\t" \
  "ds_read_b64_tr_b16 a[196:197], v27 offset:0\n\t" \
  "ds_read_b64_tr_b16 a[198:199], v27 offset:3072\n\t" \
  "v_mfma_f32_16x16x32_bf16 a[20:23], a[204:207], v[228:231], a[20:23]\n\t" \
  "v_mfma_f32_16x16x32_bf16 a[24:27], a[204:207], v[232:235], a[24:27]\n\t" \
  "v_mfma_f32_16x16x32_bf16 a[28:31], a[204:207], v[236:239], a[28:31]\n\t" \
  "s_waitcnt lgkmcnt(4)\n\t" \
  "v_mfma_f32_16x16x32_bf16 a[32:35], a[208:211], v[224:227], a[32:35]\n\t" \
  "ds_read_b64_tr_b16 a[200:201], v26 offset:6144\n\t" \
  "ds_read_b64_tr_b16 a[202:203], v26 offset:9216\n\t" \
  "v_mfma_f32_16x16x32_bf16 a[36:39], a[208:211], v[228:231], a[36:39]\n\t" \
  "v_mfma_f32_16x16x32_bf16 a[40:43], a[208:211], v[232:235], a[40:43]\n\t" \
  "v_mfma_f32_16x16x32_bf16 a[44:47], a[208:211], v[236:239], a[44:47]\n\t" \
  "v_mfma_f32_16x16x32_bf16 a[48:51], a[212:215], v[224:227], a[48:51]\n\t" \
  "ds_read_b64_tr_b16 a[204:205], v27 offset:6144\n\t" \
  "ds_read_b64_tr_b16 a[206:207], v27 offset:9216\n\t" \
  "v_mfma_f32_16x16x32_bf16 a[52:55], a[212:215], v[228:231], a[52:55]\n\t" \
  "v_mfma_f32_16x16x32_bf16 a[56:59], a[212:215], v[232:235], a[56:59]\n\t" \
  "v_mfma_f32_16x16x32_bf16 a[60:63], a[212:215], v[236:239], a[60:63]\n\t" \
  "s_waitcnt lgkmcnt(4)\n\t" \
  "v_mfma_f32_16x16x32_bf16 a[64:67], a[192:195], v[176:179], a[64:67]\n\t" \
  "ds_read_b64_tr_b16 a[208:209], v26 offset:1024\n\t" \
  "ds_read_b64_tr_b16 a[210:211], v26 offset:4096\n\t" \
  "v_mfma_f32_16x16x32_bf16 a[68:71], a[192:195], v[180:183], a[68:71]\n\t" \
  "s_cmp_eq_u32 s75, 7\n\t" \
  "s_cselect_b32 s75, 0x70000000, 0\n\t" \
  "v_or_b32 v52, s75, v32\n\t" \
  "v_mfma_f32_16x16x32_bf16 a[72:75], a[192:195], v[184:187], a[72:75]\n\t" \
  "s_cmp_eq_u32 s76, 7\n\t" \
  "s_cselect_b32 s76, 0x70000000, 0\n\t" \
  "v_or_b32 v53, s76, v32\n\t" \
  "v_mfma_f32_16x16x32_bf16 a[76:79], a[192:195], v[188:191], a[76:79]\n\t" \
  "v_mfma_f32_16x16x32_bf16 a[80:83], a[196:199], v[176:179], a[80:83]\n\t" \
  "ds_read_b64_tr_b16 a[212:213], v27 offset:1024\n\t" \
  "ds_read_b64_tr_b16 a[214:215], v27 offset:4096\n\t" \
  "v_accvgpr_read_b32 v44, a0\n\t" \
  "v_mfma_f32_16x16x32_bf16 a[84:87], a[196:199], v[180:183], a[84:87]\n\t" \
  "v_accvgpr_read_b32 v45, a1\n\t" \
  "v_accvgpr_read_b32 v46, a2\n\t" \
  "v_accvgpr_read_b32 v47, a3\n\t" \
  "v_mfma_f32_16x16x32_bf16 a[88:91], a[196:199], v[184:187], a[88:91]\n\t" \
  "v_accvgpr_read_b32 v48, a16\n\t" \
  "v_accvgpr_read_b32 v49, a17\n\t" \
  "v_accvgpr_read_b32 v50, a18\n\t" \
  "v_mfma_f32_16x16x32_bf16 a[92:95], a[196:199], v[188:191], a[92:95]\n\t" \
  "v_accvgpr_read_b32 v51, a19\n\t" \
  "v_cvt_pk_bf16_f32 v20, v44, v45\n\t" \
  "s_waitcnt lgkmcnt(4)\n\t" \
  "v_mfma_f32_16x16x32_bf16 a[96:99], a[200:203], v[176:179], a[96:99]\n\t" \
  "ds_read_b64_tr_b16 a[192:193], v26 offset:7168\n\t" \
  "ds_read_b64_tr_b16 a[194:195], v26 offset:10240\n\t" \
  "v_cvt_pk_bf16_f32 v21, v46, v47\n\t" \
  "v_mfma_f32_16x16x32_bf16 a[100:103], a[200:203], v[180:183], a[100:103]\n\t" \
  "v_cvt_pk_bf16_f32 v22, v48, v49\n\t" \
  "v_cvt_pk_bf16_f32 v23, v50, v51\n\t" \
  "s_add_u32 s69, s48, 0\n\t" \
  "v_mfma_f32_16x16x32_bf16 a[104:107], a[200:203], v[184:187], a[104:107]\n\t" \
  "buffer_store_dwordx4 v[20:23], v52, %3, s69 offen sc1\n\t" \
  "v_accvgpr_read_b32 v44, a32\n\t" \
  "v_mfma_f32_16x16x32_bf16 a[108:111], a[200:203], v[188:191], a[108:111]\n\t" \
  "v_accvgpr_read_b32 v45, a33\n\t" \
  "v_accvgpr_read_b32 v46, a34\n\t" \
  "v_accvgpr_read_b32 v47, a35\n\t" \
  "v_mfma_f32_16x16x32_bf16 a[112:115], a[204:207], v[176:179], a[112:115]\n\t" \
  "ds_read_b64_tr_b16 a[196:197], v27 offset:7168\n\t" \
  "ds_read_b64_tr_b16 a[198:199], v27 offset:10240\n\t" \
  "v_accvgpr_read_b32 v48, a48\n\t" \
  "v_mfma_f32_16x16x32_bf16 a[116:119], a[204:207], v[180:183], a[116:119]\n\t" \
  "v_accvgpr_read_b32 v49, a49\n\t" \
  "v_accvgpr_read_b32 v50, a50\n\t" \
  "v_accvgpr_read_b32 v51, a51\n\t" \
  "v_mfma_f32_16x16x32_bf16 a[120:123], a[204:207], v[184:187], a[120:123]\n\t" \
  "v_cvt_pk_bf16_f32 v60, v44, v45\n\t" \
  "v_cvt_pk_bf16_f32 v61, v46, v47\n\t" \
  "v_cvt_pk_bf16_f32 v62, v48, v49\n\t" \
  "v_mfma_f32_16x16x32_bf16 a[124:127], a[204:207], v[188:191], a[124:127]\n\t" \
  "v_cvt_pk_bf16_f32 v63, v50, v51\n\t" \
  "s_add_u32 s69, s48, 8192\n\t" \
  "s_waitcnt lgkmcnt(4)\n\t" \
  "v_mfma_f32_16x16x32_bf16 a[64:67], a[208:211], v[208:211], a[64:67]\n\t" \
  "ds_read_b64_tr_b16 a[200:201], v26 offset:2048\n\t" \
  "ds_read_b64_tr_b16 a[202:203], v26 offset:5120\n\t" \
  "v_mfma_f32_16x16x32_bf16 a[68:71], a[208:211], v[212:215], a[68:71]\n\t" \
  "buffer_store_dwordx4 v[60:63], v52, %3, s69 offen sc1\n\t" \
  "v_accvgpr_read_b32 v44, a4\n\t" \
  "v_mfma_f32_16x16x32_bf16 a[72:75], a[208:211], v[216:219], a[72:75]\n\t" \
  "v_accvgpr_read_b32 v45, a5\n\t" \
  "v_accvgpr_read_b32 v46, a6\n\t" \
  "v_accvgpr_read_b32 v47, a7\n\t" \
  "v_mfma_f32_16x16x32_bf16 a[76:79], a[208:211], v[220:223], a[76:79]\n\t" \
  "v_accvgpr_read_b32 v48, a20\n\t" \
  "v_accvgpr_read_b32 v49, a21\n\t" \
  "v_accvgpr_read_b32 v50, a22\n\t" \
  "v_mfma_f32_16x16x32_bf16 a[80:83], a[212:215], v[208:211], a[80:83]\n\t" \
  "ds_read_b64_tr_b16 a[204:205], v27 offset:2048\n\t" \
  "ds_read_b64_tr_b16 a[206:207], v27 offset:5120\n\t" \
  "v_accvgpr_read_b32 v51, a23\n\t" \
  "v_mfma_f32_16x16x32_bf16 a[84:87], a[212:215], v[212:215], a[84:87]\n\t" \
  "v_cvt_pk_bf16_f32 v20, v44, v45\n\t" \
  "v_cvt_pk_bf16_f32 v21, v46, v47\n\t" \
  "v_cvt_pk_bf16_f32 v22, v48, v49\n\t" \
  "v_mfma_f32_16x16x32_bf16 a[88:91], a[212:215], v[216:219], a[88:91]\n\t" \
  "v_cvt_pk_bf16_f32 v23, v50, v51\n\t" \
  "s_add_u32 s69, s48, 16384\n\t" \
  "v_mfma_f32_16x16x32_bf16 a[92:95], a[212:215], v[220:223], a[92:95]\n\t" \
  "buffer_store_dwordx4 v[20:23], v52, %3, s69 offen sc1\n\t" \
  "s_waitcnt lgkmcnt(4)\n\t" \
  "v_mfma_f32_16x16x32_bf16 a[96:99], a[192:195], v[208:211], a[96:99]\n\t" \
  "ds_read_b64_tr_b16 a[208:209], v26 offset:8192\n\t" \
  "ds_read_b64_tr_b16 a[210:211], v26 offset:11264\n\t" \
  "v_mfma_f32_16x16x32_bf16 a[100:103], a[192:195], v[212:215], a[100:103]\n\t" \
  "v_accvgpr_read_b32 v44, a36\n\t" \
  "v_accvgpr_read_b32 v45, a37\n\t" \
  "v_mfma_f32_16x16x32_bf16 a[104:107], a[192:195], v[216:219], a[104:107]\n\t" \
  "v_accvgpr_read_b32 v46, a38\n\t" \
  "v_accvgpr_read_b32 v47, a39\n\t" \
  "v_mfma_f32_16x16x32_bf16 a[108:111], a[192:195], v[220:223], a[108:111]\n\t" \
  "v_accvgpr_read_b32 v48, a52\n\t" \
  "v_accvgpr_read_b32 v49, a53\n\t" \
  "v_mfma_f32_16x16x32_bf16 a[112:115], a[196:199], v[208:211], a[112:115]\n\t" \
  "ds_read_b64_tr_b16 a[212:213], v27 offset:8192\n\t" \
  "ds_read_b64_tr_b16 a[214:215], v27 offset:11264\n\t" \
  "v_mfma_f32_16x16x32_bf16 a[116:119], a[196:199], v[212:215], a[116:119]\n\t" \
  "v_accvgpr_read_b32 v50, a54\n\t" \
  "v_accvgpr_read_b32 v51, a55\n\t" \
  "v_mfma_f32_16x16x32_bf16 a[120:123], a[196:199], v[216:219], a[120:123]\n\t" \
  "v_cvt_pk_bf16_f32 v60, v44, v45\n\t" \
  "v_cvt_pk_bf16_f32 v61, v46, v47\n\t" \
  "v_mfma_f32_16x16x32_bf16 a[124:127], a[196:199], v[220:223], a[124:127]\n\t" \
  "s_waitcnt lgkmcnt(4)\n\t" \
  "s_barrier\n\t" \
  "v_mfma_f32_16x16x32_bf16 a[64:67], a[200:203], v[240:243], a[64:67]\n\t" \
  "ds_read_b64_tr_b16 a[192:193], v26 offset:12288\n\t" \
  "ds_read_b64_tr_b16 a[194:195], v26 offset:15360\n\t" \
  "v_mfma_f32_16x16x32_bf16 a[68:71], a[200:203], v[244:247], a[68:71]\n\t" \
  "v_cvt_pk_bf16_f32 v62, v48, v49\n\t" \
  "v_cvt_pk_bf16_f32 v63, v50, v51\n\t" \
  "v_mfma_f32_16x16x32_bf16 a[72:75], a[200:203], v[248:251], a[72:75]\n\t" \
  "s_add_u32 s69, s48, 24576\n\t" \
  "v_mfma_f32_16x16x32_bf16 a[76:79], a[200:203], v[252:255], a[76:79]\n\t" \
  "buffer_store_dwordx4 v[60:63], v52, %3, s69 offen sc1\n\t" \
  "v_accvgpr_read_b32 v44, a8\n\t" \
  "v_mfma_f32_16x16x32_bf16 a[80:83], a[204:207], v[240:243], a[80:83]\n\t" \
  "ds_read_b64_tr_b16 a[196:197], v27 offset:12288\n\t" \
  "ds_read_b64_tr_b16 a[198:199], v27 offset:15360\n\t" \
  "v_mfma_f32_16x16x32_bf16 a[84:87], a[204:207], v[244:247], a[84:87]\n\t" \
  "v_accvgpr_read_b32 v45, a9\n\t" \
  "v_accvgpr_read_b32 v46, a10\n\t" \
  "v_mfma_f32_16x16x32_bf16 a[88:91], a[204:207], v[248:251], a[88:91]\n\t" \
  "v_accvgpr_read_b32 v47, a11\n\t" \
  "v_accvgpr_read_b32 v48, a24\n\t" \
  "v_mfma_f32_16x16x32_bf16 a[92:95], a[204:207], v[252:255], a[92:95]\n\t" \
  "v_accvgpr_read_b32 v49, a25\n\t" \
  "s_waitcnt lgkmcnt(4)\n\t" \
  "v_mfma_f32_16x16x32_bf16 a[96:99], a[208:211], v[240:243], a[96:99]\n\t" \
  "ds_read_b64_tr_b16 a[200:201], v26 offset:18432\n\t" \
  "ds_read_b64_tr_b16 a[202:203], v26 offset:21504\n\t" \
  "v_mfma_f32_16x16x32_bf16 a[100:103], a[208:211], v[244:247], a[100:103]\n\t" \
  "v_accvgpr_read_b32 v50, a26\n\t" \
  "v_accvgpr_read_b32 v51, a27\n\t" \
  "v_mfma_f32_16x16x32_bf16 a[104:107], a[208:211], v[248:251], a[104:107]\n\t" \
  "v_cvt_pk_bf16_f32 v20, v44, v45\n\t" \
  "v_cvt_pk_bf16_f32 v21, v46, v47\n\t" \
  "v_mfma_f32_16x16x32_bf16 a[108:111], a[208:211], v[252:255], a[108:111]\n\t" \
  "v_cvt_pk_bf16_f32 v22, v48, v49\n\t" \
  "v_cvt_pk_bf16_f32 v23, v50, v51\n\t" \
  "v_mfma_f32_16x16x32_bf16 a[112:115], a[212:215], v[240:243], a[112:115]\n\t" \
  "ds_read_b64_tr_b16 a[204:205], v27 offset:18432\n\t" \
  "ds_read_b64_tr_b16 a[206:207], v27 offset:21504\n\t" \
  "v_mfma_f32_16x16x32_bf16 a[116:119], a[212:215], v[244:247], a[116:119]\n\t" \
  "s_add_u32 s69, s74, 0\n\t" \
  "v_mfma_f32_16x16x32_bf16 a[120:123], a[212:215], v[248:251], a[120:123]\n\t" \
  "buffer_store_dwordx4 v[20:23], v53, %3, s69 offen sc1\n\t" \
  "v_accvgpr_read_b32 v44, a40\n\t" \
  "v_mfma_f32_16x16x32_bf16 a[124:127], a[212:215], v[252:255], a[124:127]\n\t" \
  "v_accvgpr_read_b32 v45, a41\n\t" \
  "s_waitcnt lgkmcnt(4)\n\t" \
  "v_mfma_f32_16x16x32_bf16 a[64:67], a[192:195], v[160:163], a[64:67]\n\t" \
  "ds_read_b64_tr_b16 a[208:209], v26 offset:13312\n\t" \
  "ds_read_b64_tr_b16 a[210:211], v26 offset:16384\n\t" \
  "v_mfma_f32_16x16x32_bf16 a[68:71], a[192:195], v[164:167], a[68:71]\n\t" \
  "v_accvgpr_read_b32 v46, a42\n\t" \
  "v_accvgpr_read_b32 v47, a43\n\t" \
  "v_mfma_f32_16x16x32_bf16 a[72:75], a[192:195], v[168:171], a[72:75]\n\t" \
  "v_accvgpr_read_b32 v48, a56\n\t" \
  "v_accvgpr_read_b32 v49, a57\n\t" \
  "v_mfma_f32_16x16x32_bf16 a[76:79], a[192:195], v[172:175], a[76:79]\n\t" \
  "v_accvgpr_read_b32 v50, a58\n\t" \
  "v_accvgpr_read_b32 v51, a59\n\t" \
  "v_mfma_f32_16x16x32_bf16 a[80:83], a[196:199], v[160:163], a[80:83]\n\t" \
  "ds_read_b64_tr_b16 a[212:213], v27 offset:13312\n\t" \
  "ds_read_b64_tr_b16 a[214:215], v27 offset:16384\n\t" \
  "v_mfma_f32_16x16x32_bf16 a[84:87], a[196:199], v[164:167], a[84:87]\n\t" \
  "v_cvt_pk_bf16_f32 v60, v44, v45\n\t" \
  "v_cvt_pk_bf16_f32 v61, v46, v47\n\t" \
  "v_mfma_f32_16x16x32_bf16 a[88:91], a[196:199], v[168:171], a[88:91]\n\t" \
  "v_cvt_pk_bf16_f32 v62, v48, v49\n\t" \
  "v_cvt_pk_bf16_f32 v63, v50, v51\n\t" \
  "v_mfma_f32_16x16x32_bf16 a[92:95], a[196:199], v[172:175], a[92:95]\n\t" \
  "s_add_u32 s69, s74, 8192\n\t" \
  "s_waitcnt lgkmcnt(4)\n\t" \
  "v_mfma_f32_16x16x32_bf16 a[96:99], a[200:203], v[160:163], a[96:99]\n\t" \
  "ds_read_b64_tr_b16 a[192:193], v26 offset:19456\n\t" \
  "ds_read_b64_tr_b16 a[194:195], v26 offset:22528\n\t" \
  "v_mfma_f32_16x16x32_bf16 a[100:103], a[200:203], v[164:167], a[100:103]\n\t" \
  "buffer_store_dwordx4 v[60:63], v53, %3, s69 offen sc1\n\t" \
  "v_mfma_f32_16x16x32_bf16 a[104:107], a[200:203], v[168:171], a[104:107]\n\t" \
  "v_accvgpr_read_b32 v44, a12\n\t" \
  "v_accvgpr_read_b32 v45, a13\n\t" \
  "v_mfma_f32_16x16x32_bf16 a[108:111], a[200:203], v[172:175], a[108:111]\n\t" \
  "v_accvgpr_read_b32 v46, a14\n\t" \
  "v_accvgpr_read_b32 v47, a15\n\t" \
  "v_mfma_f32_16x16x32_bf16 a[112:115], a[204:207], v[160:163], a[112:115]\n\t" \
  "ds_read_b64_tr_b16 a[196:197], v27 offset:19456\n\t" \
  "ds_read_b64_tr_b16 a[198:199], v27 offset:22528\n\t" \
  "v_mfma_f32_16x16x32_bf16 a[116:119], a[204:207], v[164:167], a[116:119]\n\t" \
  "v_accvgpr_read_b32 v48, a28\n\t" \
  "v_accvgpr_read_b32 v49, a29\n\t" \
  "v_mfma_f32_16x16x32_bf16 a[120:123], a[204:207], v[168:171], a[120:123]\n\t" \
  "v_accvgpr_read_b32 v50, a30\n\t" \
  "v_accvgpr_read_b32 v51, a31\n\t" \
  "v_mfma_f32_16x16x32_bf16 a[124:127], a[204:207], v[172:175], a[124:127]\n\t" \
  "v_cvt_pk_bf16_f32 v20, v44, v45\n\t" \
  "s_waitcnt lgkmcnt(4)\n\t" \
  "v_mfma_f32_16x16x32_bf16 a[64:67], a[208:211], v[192:195], a[64:67]\n\t" \
  "ds_read_b64_tr_b16 a[200:201], v26 offset:14336\n\t" \
  "ds_read_b64_tr_b16 a[202:203], v26 offset:17408\n\t" \
  "v_mfma_f32_16x16x32_bf16 a[68:71], a[208:211], v[196:199], a[68:71]\n\t" \
  "v_cvt_pk_bf16_f32 v21, v46, v47\n\t" \
  "v_cvt_pk_bf16_f32 v22, v48, v49\n\t" \
  "v_mfma_f32_16x16x32_bf16 a[72:75], a[208:211], v[200:203], a[72:75]\n\t" \
  "v_cvt_pk_bf16_f32 v23, v50, v51\n\t" \
  "s_add_u32 s69, s74, 16384\n\t" \
  "v_mfma_f32_16x16x32_bf16 a[76:79], a[208:211], v[204:207], a[76:79]\n\t" \
  "buffer_store_dwordx4 v[20:23], v53, %3, s69 offen sc1\n\t" \
  "v_mfma_f32_16x16x32_bf16 a[80:83], a[212:215], v[192:195], a[80:83]\n\t" \
  "ds_read_b64_tr_b16 a[204:205], v27 offset:14336\n\t" \
  "ds_read_b64_tr_b16 a[206:207], v27 offset:17408\n\t" \
  "v_mfma_f32_16x16x32_bf16 a[84:87], a[212:215], v[196:199], a[84:87]\n\t" \
  "v_accvgpr_read_b32 v44, a44\n\t" \
  "v_accvgpr_read_b32 v45, a45\n\t" \
  "v_mfma_f32_16x16x32_bf16 a[88:91], a[212:215], v[200:203], a[88:91]\n\t" \
  "v_accvgpr_read_b32 v46, a46\n\t" \
  "v_accvgpr_read_b32 v47, a47\n\t" \
  "v_mfma_f32_16x16x32_bf16 a[92:95], a[212:215], v[204:207], a[92:95]\n\t" \
  "v_accvgpr_read_b32 v48, a60\n\t" \
  "s_waitcnt lgkmcnt(4)\n\t" \
  "v_mfma_f32_16x16x32_bf16 a[96:99], a[192:195], v[192:195], a[96:99]\n\t" \
  "ds_read_b64_tr_b16 a[208:209], v26 offset:20480\n\t" \
  "ds_read_b64_tr_b16 a[210:211], v26 offset:23552\n\t" \
  "v_mfma_f32_16x16x32_bf16 a[100:103], a[192:195], v[196:199], a[100:103]\n\t" \
  "v_accvgpr_read_b32 v49, a61\n\t" \
  "v_accvgpr_read_b32 v50, a62\n\t" \
  "v_mfma_f32_16x16x32_bf16 a[104:107], a[192:195], v[200:203], a[104:107]\n\t" \
  "v_accvgpr_read_b32 v51, a63\n\t" \
  "v_cvt_pk_bf16_f32 v60, v44, v45\n\t" \
  "v_mfma_f32_16x16x32_bf16 a[108:111], a[192:195], v[204:207], a[108:111]\n\t" \
  "v_cvt_pk_bf16_f32 v61, v46, v47\n\t" \
  "v_cvt_pk_bf16_f32 v62, v48, v49\n\t" \
  "v_mfma_f32_16x16x32_bf16 a[112:115], a[196:199], v[192:195], a[112:115]\n\t" \
  "ds_read_b64_tr_b16 a[212:213], v27 offset:20480\n\t" \
  "ds_read_b64_tr_b16 a[214:215], v27 offset:23552\n\t" \
  "v_mfma_f32_16x16x32_bf16 a[116:119], a[196:199], v[196:199], a[116:119]\n\t" \
  "v_cvt_pk_bf16_f32 v63, v50, v51\n\t" \
  "s_add_u32 s69, s74, 24576\n\t" \
  "v_mfma_f32_16x16x32_bf16 a[120:123], a[196:199], v[200:203], a[120:123]\n\t" \
  "buffer_store_dwordx4 v[60:63], v53, %3, s69 offen sc1\n\t" \
  "v_mfma_f32_16x16x32_bf16 a[124:127], a[196:199], v[204:207], a[124:127]\n\t" \
  "s_waitcnt lgkmcnt(4)\n\t" \
  "s_barrier\n\t" \
  "v_mfma_f32_16x16x32_bf16 a[64:67], a[200:203], v[224:227], a[64:67]\n\t" \
  "ds_read_b64_tr_b16 a[192:193], v26 offset:24576\n\t" \
  "ds_read_b64_tr_b16 a[194:195], v26 offset:27648\n\t" \
  "v_mfma_f32_16x16x32_bf16 a[68:71], a[200:203], v[228:231], a[68:71]\n\t" \
  "v_mfma_f32_16x16x32_bf16 a[72:75], a[200:203], v[232:235], a[72:75]\n\t" \
  "v_mfma_f32_16x16x32_bf16 a[76:79], a[200:203], v[236:239], a[76:79]\n\t" \
  "v_mfma_f32_16x16x32_bf16 a[80:83], a[204:207], v[224:227], a[80:83]\n\t" \
  "ds_read_b64_tr_b16 a[196:197], v27 offset:24576\n\t" \
  "ds_read_b64_tr_b16 a[198:199], v27 offset:27648\n\t" \
  "v_mfma_f32_16x16x32_bf16 a[84:87], a[204:207], v[228:231], a[84:87]\n\t" \
  "v_mfma_f32_16x16x32_bf16 a[88:91], a[204:207], v[232:235], a[88:91]\n\t" \
  "v_mfma_f32_16x16x32_bf16 a[92:95], a[204:207], v[236:239], a[92:95]\n\t" \
  "s_waitcnt lgkmcnt(4)\n\t" \
  "v_mfma_f32_16x16x32_bf16 a[96:99], a[208:211], v[224:227], a[96:99]\n\t" \
  "ds_read_b64_tr_b16 a[200:201], v26 offset:30720\n\t" \
  "ds_read_b64_tr_b16 a[202:203], v26 offset:33792\n\t" \
  "v_mfma_f32_16x16x32_bf16 a[100:103], a[208:211], v[228:231], a[100:103]\n\t" \
  "v_mfma_f32_16x16x32_bf16 a[104:107], a[208:211], v[232:235], a[104:107]\n\t" \
  "v_mfma_f32_16x16x32_bf16 a[108:111], a[208:211], v[236:239], a[108:111]\n\t" \
  "v_mfma_f32_16x16x32_bf16 a[112:115], a[212:215], v[224:227], a[112:115]\n\t" \
  "ds_read_b64_tr_b16 a[204:205], v27 offset:30720\n\t" \
  "ds_read_b64_tr_b16 a[206:207], v27 offset:33792\n\t" \
  "v_mfma_f32_16x16x32_bf16 a[116:119], a[212:215], v[228:231], a[116:119]\n\t" \
  "v_mfma_f32_16x16x32_bf16 a[120:123], a[212:215], v[232:235], a[120:123]\n\t" \
  "v_mfma_f32_16x16x32_bf16 a[124:127], a[212:215], v[236:239], a[124:127]\n\t" \
  "s_waitcnt lgkmcnt(4)\n\t" \
  "v_mfma_f32_16x16x32_bf16 a[128:131], a[192:195], v[64:67], a[128:131]\n\t" \
  "ds_read_b64_tr_b16 a[208:209], v26 offset:25600\n\t" \
  "ds_read_b64_tr_b16 a[210:211], v26 offset:28672\n\t" \
  "v_mfma_f32_16x16x32_bf16 a[132:135], a[192:195], v[68:71], a[132:135]\n\t" \
  "v_mfma_f32_16x16x32_bf16 a[136:139], a[192:195], v[72:75], a[136:139]\n\t" \
  "v_mfma_f32_16x16x32_bf16 a[140:143], a[192:195], v[76:79], a[140:143]\n\t" \
  "v_mfma_f32_16x16x32_bf16 a[144:147], a[196:199], v[64:67], a[144:147]\n\t" \
  "ds_read_b64_tr_b16 a[212:213], v27 offset:25600\n\t" \
  "ds_read_b64_tr_b16 a[214:215], v27 offset:28672\n\t" \
  "v_accvgpr_read_b32 v44, a64\n\t" \
  "v_mfma_f32_16x16x32_bf16 a[148:151], a[196:199], v[68:71], a[148:151]\n\t" \
  "v_accvgpr_read_b32 v45, a65\n\t" \
  "v_accvgpr_read_b32 v46, a66\n\t" \
  "v_accvgpr_read_b32 v47, a67\n\t" \
  "v_mfma_f32_16x16x32_bf16 a[152:155], a[196:199], v[72:75], a[152:155]\n\t" \
  "v_accvgpr_read_b32 v48, a80\n\t" \
  "v_accvgpr_read_b32 v49, a81\n\t" \
  "v_accvgpr_read_b32 v50, a82\n\t" \
  "v_mfma_f32_16x16x32_bf16 a[156:159], a[196:199], v[76:79], a[156:159]\n\t" \
  "v_accvgpr_read_b32 v51, a83\n\t" \
  "v_cvt_pk_bf16_f32 v20, v44, v45\n\t" \
  "s_waitcnt lgkmcnt(4)\n\t" \
  "v_mfma_f32_16x16x32_bf16 a[160:163], a[200:203], v[64:67], a[160:163]\n\t" \
  "ds_read_b64_tr_b16 a[192:193], v26 offset:31744\n\t" \
  "ds_read_b64_tr_b16 a[194:195], v26 offset:34816\n\t" \
  "v_cvt_pk_bf16_f32 v21, v46, v47\n\t" \
  "v_mfma_f32_16x16x32_bf16 a[164:167], a[200:203], v[68:71], a[164:167]\n\t" \
  "v_cvt_pk_bf16_f32 v22, v48, v49\n\t" \
  "v_cvt_pk_bf16_f32 v23, v50, v51\n\t" \
  "s_add_u32 s69, s48, 32768\n\t" \
  "v_mfma_f32_16x16x32_bf16 a[168:171], a[200:203], v[72:75], a[168:171]\n\t" \
  "buffer_store_dwordx4 v[20:23], v52, %3, s69 offen sc1\n\t" \
  "v_accvgpr_read_b32 v44, a96\n\t" \
  "v_mfma_f32_16x16x32_bf16 a[172:175], a[200:203], v[76:79], a[172:175]\n\t" \
  "v_accvgpr_read_b32 v45, a97\n\t" \
  "v_accvgpr_read_b32 v46, a98\n\t" \
  "v_accvgpr_read_b32 v47, a99\n\t" \
  "v_mfma_f32_16x16x32_bf16 a[176:179], a[204:207], v[64:67], a[176:179]\n\t" \
  "ds_read_b64_tr_b16 a[196:197], v27 offset:31744\n\t" \
  "ds_read_b64_tr_b16 a[198:199], v27 offset:34816\n\t" \
  "v_accvgpr_read_b32 v48, a112\n\t" \
  "v_mfma_f32_16x16x32_bf16 a[180:183], a[204:207], v[68:71], a[180:183]\n\t" \
  "v_accvgpr_read_b32 v49, a113\n\t" \
  "v_accvgpr_read_b32 v50, a114\n\t" \
  "v_accvgpr_read_b32 v51, a115\n\t" \
  "v_mfma_f32_16x16x32_bf16 a[184:187], a[204:207], v[72:75], a[184:187]\n\t" \
  "v_cvt_pk_bf16_f32 v60, v44, v45\n\t" \
  "v_cvt_pk_bf16_f32 v61, v46, v47\n\t" \
  "v_cvt_pk_bf16_f32 v62, v48, v49\n\t" \
  "v_mfma_f32_16x16x32_bf16 a[188:191], a[204:207], v[76:79], a[188:191]\n\t" \
  "v_cvt_pk_bf16_f32 v63, v50, v51\n\t" \
  "s_add_u32 s69, s48, 40960\n\t" \
  "s_waitcnt lgkmcnt(4)\n\t" \
  "v_mfma_f32_16x16x32_bf16 a[128:131], a[208:211], v[96:99], a[128:131]\n\t" \
  "ds_read_b64_tr_b16 a[200:201], v26 offset:26624\n\t" \
  "ds_read_b64_tr_b16 a[202:203], v26 offset:29696\n\t" \
  "v_mfma_f32_16x16x32_bf16 a[132:135], a[208:211], v[100:103], a[132:135]\n\t" \
  "buffer_store_dwordx4 v[60:63], v52, %3, s69 offen sc1\n\t" \
  "v_accvgpr_read_b32 v44, a68\n\t" \
  "v_mfma_f32_16x16x32_bf16 a[136:139], a[208:211], v[104:107], a[136:139]\n\t" \
  "v_accvgpr_read_b32 v45, a69\n\t" \
  "v_accvgpr_read_b32 v46, a70\n\t" \
  "v_accvgpr_read_b32 v47, a71\n\t" \
  "v_mfma_f32_16x16x32_bf16 a[140:143], a[208:211], v[108:111], a[140:143]\n\t" \
  "v_accvgpr_read_b32 v48, a84\n\t" \
  "v_accvgpr_read_b32 v49, a85\n\t" \
  "v_accvgpr_read_b32 v50, a86\n\t" \
  "v_mfma_f32_16x16x32_bf16 a[144:147], a[212:215], v[96:99], a[144:147]\n\t" \
  "ds_read_b64_tr_b16 a[204:205], v27 offset:26624\n\t" \
  "ds_read_b64_tr_b16 a[206:207], v27 offset:29696\n\t" \
  "v_accvgpr_read_b32 v51, a87\n\t" \
  "v_mfma_f32_16x16x32_bf16 a[148:151], a[212:215], v[100:103], a[148:151]\n\t" \
  "v_cvt_pk_bf16_f32 v20, v44, v45\n\t" \
  "v_cvt_pk_bf16_f32 v21, v46, v47\n\t" \
  "v_cvt_pk_bf16_f32 v22, v48, v49\n\t" \
  "v_mfma_f32_16x16x32_bf16 a[152:155], a[212:215], v[104:107], a[152:155]\n\t" \
  "v_cvt_pk_bf16_f32 v23, v50, v51\n\t" \
  "s_add_u32 s69, s48, 49152\n\t" \
  "v_mfma_f32_16x16x32_bf16 a[156:159], a[212:215], v[108:111], a[156:159]\n\t" \
  "buffer_store_dwordx4 v[20:23], v52, %3, s69 offen sc1\n\t" \
  "s_waitcnt lgkmcnt(4)\n\t" \
  "v_mfma_f32_16x16x32_bf16 a[160:163], a[192:195], v[96:99], a[160:163]\n\t" \
  "ds_read_b64_tr_b16 a[208:209], v26 offset:32768\n\t" \
  "ds_read_b64_tr_b16 a[210:211], v26 offset:35840\n\t" \
  "v_accvgpr_read_b32 v44, a100\n\t" \
  "v_mfma_f32_16x16x32_bf16 a[164:167], a[192:195], v[100:103], a[164:167]\n\t" \
  "v_accvgpr_read_b32 v45, a101\n\t" \
  "v_accvgpr_read_b32 v46, a102\n\t" \
  "v_accvgpr_read_b32 v47, a103\n\t" \
  "v_mfma_f32_16x16x32_bf16 a[168:171], a[192:195], v[104:107], a[168:171]\n\t" \
  "v_accvgpr_read_b32 v48, a116\n\t" \
  "v_accvgpr_read_b32 v49, a117\n\t" \
  "v_accvgpr_read_b32 v50, a118\n\t" \
  "v_mfma_f32_16x16x32_bf16 a[172:175], a[192:195], v[108:111], a[172:175]\n\t" \
  "v_accvgpr_read_b32 v51, a119\n\t" \
  "v_cvt_pk_bf16_f32 v60, v44, v45\n\t" \
  "v_cvt_pk_bf16_f32 v61, v46, v47\n\t" \
  "v_mfma_f32_16x16x32_bf16 a[176:179], a[196:199], v[96:99], a[176:179]\n\t" \
  "ds_read_b64_tr_b16 a[212:213], v27 offset:32768\n\t" \
  "ds_read_b64_tr_b16 a[214:215], v27 offset:35840\n\t" \
  "v_cvt_pk_bf16_f32 v62, v48, v49\n\t" \
  "v_mfma_f32_16x16x32_bf16 a[180:183], a[196:199], v[100:103], a[180:183]\n\t" \
  "v_cvt_pk_bf16_f32 v63, v50, v51\n\t" \
  "s_add_u32 s69, s48, 57344\n\t" \
  "v_mfma_f32_16x16x32_bf16 a[184:187], a[196:199], v[104:107], a[184:187]\n\t" \
  "buffer_store_dwordx4 v[60:63], v52, %3, s69 offen sc1\n\t" \
  "v_accvgpr_read_b32 v44, a72\n\t" \
  "v_mfma_f32_16x16x32_bf16 a[188:191], a[196:199], v[108:111], a[188:191]\n\t" \
  "v_accvgpr_read_b32 v45, a73\n\t" \
  "s_waitcnt lgkmcnt(4)\n\t" \
  "s_barrier\n\t" \
  "v_mfma_f32_16x16x32_bf16 a[128:131], a[200:203], v[128:131], a[128:131]\n\t" \
  "ds_read_b64_tr_b16 a[192:193], v26 offset:36864\n\t" \
  "ds_read_b64_tr_b16 a[194:195], v26 offset:39936\n\t" \
  "v_accvgpr_read_b32 v46, a74\n\t" \
  "v_mfma_f32_16x16x32_bf16 a[132:135], a[200:203], v[132:135], a[132:135]\n\t" \
  "v_accvgpr_read_b32 v47, a75\n\t" \
  "v_accvgpr_read_b32 v48, a88\n\t" \
  "v_accvgpr_read_b32 v49, a89\n\t" \
  "v_mfma_f32_16x16x32_bf16 a[136:139], a[200:203], v[136:139], a[136:139]\n\t" \
  "v_accvgpr_read_b32 v50, a90\n\t" \
  "v_accvgpr_read_b32 v51, a91\n\t" \
  "v_cvt_pk_bf16_f32 v20, v44, v45\n\t" \
  "v_mfma_f32_16x16x32_bf16 a[140:143], a[200:203], v[140:143], a[140:143]\n\t" \
  "v_cvt_pk_bf16_f32 v21, v46, v47\n\t" \
  "v_cvt_pk_bf16_f32 v22, v48, v49\n\t" \
  "v_cvt_pk_bf16_f32 v23, v50, v51\n\t" \
  "v_mfma_f32_16x16x32_bf16 a[144:147], a[204:207], v[128:131], a[144:147]\n\t" \
  "ds_read_b64_tr_b16 a[196:197], v27 offset:36864\n\t" \
  "ds_read_b64_tr_b16 a[198:199], v27 offset:39936\n\t" \
  "s_add_u32 s69, s74, 32768\n\t" \
  "v_mfma_f32_16x16x32_bf16 a[148:151], a[204:207], v[132:135], a[148:151]\n\t" \
  "buffer_store_dwordx4 v[20:23], v53, %3, s69 offen sc1\n\t" \
  "v_accvgpr_read_b32 v44, a104\n\t" \
  "v_mfma_f32_16x16x32_bf16 a[152:155], a[204:207], v[136:139], a[152:155]\n\t" \
  "v_accvgpr_read_b32 v45, a105\n\t" \
  "v_accvgpr_read_b32 v46, a106\n\t" \
  "v_accvgpr_read_b32 v47, a107\n\t" \
  "v_mfma_f32_16x16x32_bf16 a[156:159], a[204:207], v[140:143], a[156:159]\n\t" \
  "v_accvgpr_read_b32 v48, a120\n\t" \
  "v_accvgpr_read_b32 v49, a121\n\t" \
  "s_waitcnt lgkmcnt(4)\n\t" \
  "v_mfma_f32_16x16x32_bf16 a[160:163], a[208:211], v[128:131], a[160:163]\n\t" \
  "ds_read_b64_tr_b16 a[200:201], v26 offset:43008\n\t" \
  "ds_read_b64_tr_b16 a[202:203], v26 offset:46080\n\t" \
  "v_accvgpr_read_b32 v50, a122\n\t" \
  "v_mfma_f32_16x16x32_bf16 a[164:167], a[208:211], v[132:135], a[164:167]\n\t" \
  "v_accvgpr_read_b32 v51, a123\n\t" \
  "v_cvt_pk_bf16_f32 v60, v44, v45\n\t" \
  "v_cvt_pk_bf16_f32 v61, v46, v47\n\t" \
  "v_mfma_f32_16x16x32_bf16 a[168:171], a[208:211], v[136:139], a[168:171]\n\t" \
  "v_cvt_pk_bf16_f32 v62, v48, v49\n\t" \
  "v_cvt_pk_bf16_f32 v63, v50, v51\n\t" \
  "s_add_u32 s69, s74, 40960\n\t" \
  "v_mfma_f32_16x16x32_bf16 a[172:175], a[208:211], v[140:143], a[172:175]\n\t" \
  "buffer_store_dwordx4 v[60:63], v53, %3, s69 offen sc1\n\t" \
  "v_accvgpr_read_b32 v44, a76\n\t" \
  "v_mfma_f32_16x16x32_bf16 a[176:179], a[212:215], v[128:131], a[176:179]\n\t" \
  "ds_read_b64_tr_b16 a[204:205], v27 offset:43008\n\t" \
  "ds_read_b64_tr_b16 a[206:207], v27 offset:46080\n\t" \
  "v_accvgpr_read_b32 v45, a77\n\t" \
  "v_mfma_f32_16x16x32_bf16 a[180:183], a[212:215], v[132:135], a[180:183]\n\t" \
  "v_accvgpr_read_b32 v46, a78\n\t" \
  "v_accvgpr_read_b32 v47, a79\n\t" \
  "v_accvgpr_read_b32 v48, a92\n\t" \
  "v_mfma_f32_16x16x32_bf16 a[184:187], a[212:215], v[136:139], a[184:187]\n\t" \
  "v_accvgpr_read_b32 v49, a93\n\t" \
  "v_accvgpr_read_b32 v50, a94\n\t" \
  "v_mfma_f32_16x16x32_bf16 a[188:191], a[212:215], v[140:143], a[188:191]\n\t" \
  "v_accvgpr_read_b32 v51, a95\n\t" \
  "s_waitcnt lgkmcnt(4)\n\t" \
  "v_mfma_f32_16x16x32_bf16 a[128:131], a[192:195], v[176:179], a[128:131]\n\t" \
  "ds_read_b64_tr_b16 a[208:209], v26 offset:37888\n\t" \
  "ds_read_b64_tr_b16 a[210:211], v26 offset:40960\n\t" \
  "v_mfma_f32_16x16x32_bf16 a[132:135], a[192:195], v[180:183], a[132:135]\n\t" \
  "v_cvt_pk_bf16_f32 v20, v44, v45\n\t" \
  "v_cvt_pk_bf16_f32 v21, v46, v47\n\t" \
  "v_mfma_f32_16x16x32_bf16 a[136:139], a[192:195], v[184:187], a[136:139]\n\t" \
  "v_cvt_pk_bf16_f32 v22, v48, v49\n\t" \
  "v_cvt_pk_bf16_f32 v23, v50, v51\n\t" \
  "v_mfma_f32_16x16x32_bf16 a[140:143], a[192:195], v[188:191], a[140:143]\n\t" \
  "s_add_u32 s69, s74, 49152\n\t" \
  "v_mfma_f32_16x16x32_bf16 a[144:147], a[196:199], v[176:179], a[144:147]\n\t" \
  "ds_read_b64_tr_b16 a[212:213], v27 offset:37888\n\t" \
  "ds_read_b64_tr_b16 a[214:215], v27 offset:40960\n\t" \
  "v_mfma_f32_16x16x32_bf16 a[148:151], a[196:199], v[180:183], a[148:151]\n\t" \
  "buffer_store_dwordx4 v[20:23], v53, %3, s69 offen sc1\n\t" \
  "v_accvgpr_read_b32 v44, a108\n\t" \
  "v_mfma_f32_16x16x32_bf16 a[152:155], a[196:199], v[184:187], a[152:155]\n\t" \
  "v_accvgpr_read_b32 v45, a109\n\t" \
  "v_accvgpr_read_b32 v46, a110\n\t" \
  "v_mfma_f32_16x16x32_bf16 a[156:159], a[196:199], v[188:191], a[156:159]\n\t" \
  "v_accvgpr_read_b32 v47, a111\n\t" \
  "s_waitcnt lgkmcnt(4)\n\t" \
  "v_mfma_f32_16x16x32_bf16 a[160:163], a[200:203], v[176:179], a[160:163]\n\t" \
  "ds_read_b64_tr_b16 a[192:193], v26 offset:44032\n\t" \
  "ds_read_b64_tr_b16 a[194:195], v26 offset:47104\n\t" \
  "v_mfma_f32_16x16x32_bf16 a[164:167], a[200:203], v[180:183], a[164:167]\n\t" \
  "v_accvgpr_read_b32 v48, a124\n\t" \
  "v_accvgpr_read_b32 v49, a125\n\t" \
  "v_mfma_f32_16x16x32_bf16 a[168:171], a[200:203], v[184:187], a[168:171]\n\t" \
  "v_accvgpr_read_b32 v50, a126\n\t" \
  "v_accvgpr_read_b32 v51, a127\n\t" \
  "v_mfma_f32_16x16x32_bf16 a[172:175], a[200:203], v[188:191], a[172:175]\n\t" \
  "v_cvt_pk_bf16_f32 v60, v44, v45\n\t" \
  "v_cvt_pk_bf16_f32 v61, v46, v47\n\t" \
  "v_mfma_f32_16x16x32_bf16 a[176:179], a[204:207], v[176:179], a[176:179]\n\t" \
  "ds_read_b64_tr_b16 a[196:197], v27 offset:44032\n\t" \
  "ds_read_b64_tr_b16 a[198:199], v27 offset:47104\n\t" \
  "v_mfma_f32_16x16x32_bf16 a[180:183], a[204:207], v[180:183], a[180:183]\n\t" \
  "v_cvt_pk_bf16_f32 v62, v48, v49\n\t" \
  "v_cvt_pk_bf16_f32 v63, v50, v51\n\t" \
  "v_mfma_f32_16x16x32_bf16 a[184:187], a[204:207], v[184:187], a[184:187]\n\t" \
  "s_add_u32 s69, s74, 57344\n\t" \
  "v_mfma_f32_16x16x32_bf16 a[188:191], a[204:207], v[188:191], a[188:191]\n\t" \
  "buffer_store_dwordx4 v[60:63], v53, %3, s69 offen sc1\n\t" \
  "s_waitcnt lgkmcnt(4)\n\t" \
  "v_mfma_f32_16x16x32_bf16 a[128:131], a[208:211], v[208:211], a[128:131]\n\t" \
  "ds_read_b64_tr_b16 a[200:201], v26 offset:38912\n\t" \
  "ds_read_b64_tr_b16 a[202:203], v26 offset:41984\n\t" \
  "v_mfma_f32_16x16x32_bf16 a[132:135], a[208:211], v[212:215], a[132:135]\n\t" \
  "v_mfma_f32_16x16x32_bf16 a[136:139], a[208:211], v[216:219], a[136:139]\n\t" \
  "v_mfma_f32_16x16x32_bf16 a[140:143], a[208:211], v[220:223], a[140:143]\n\t" \
  "v_mfma_f32_16x16x32_bf16 a[144:147], a[212:215], v[208:211], a[144:147]\n\t" \
  "ds_read_b64_tr_b16 a[204:205], v27 offset:38912\n\t" \
  "ds_read_b64_tr_b16 a[206:207], v27 offset:41984\n\t" \
  "s_waitcnt vmcnt(8)\n\t" \
  "v_mfma_f32_16x16x32_bf16 a[148:151], a[212:215], v[212:215], a[148:151]\n\t" \
  "buffer_store_dwordx2 v[36:37], v34, %3, s65 offen offset:0 sc1\n\t" \
  "v_mfma_f32_16x16x32_bf16 a[152:155], a[212:215], v[216:219], a[152:155]\n\t" \
  "v_mfma_f32_16x16x32_bf16 a[156:159], a[212:215], v[220:223], a[156:159]\n\t" \
  "s_waitcnt lgkmcnt(4)\n\t" \
  "v_mfma_f32_16x16x32_bf16 a[160:163], a[192:195], v[208:211], a[160:163]\n\t" \
  "ds_read_b64_tr_b16 a[208:209], v26 offset:45056\n\t" \
  "ds_read_b64_tr_b16 a[210:211], v26 offset:48128\n\t" \
  "v_mfma_f32_16x16x32_bf16 a[164:167], a[192:195], v[212:215], a[164:167]\n\t" \
  "v_mfma_f32_16x16x32_bf16 a[168:171], a[192:195], v[216:219], a[168:171]\n\t" \
  "v_mfma_f32_16x16x32_bf16 a[172:175], a[192:195], v[220:223], a[172:175]\n\t" \
  "v_mfma_f32_16x16x32_bf16 a[176:179], a[196:199], v[208:211], a[176:179]\n\t" \
  "ds_read_b64_tr_b16 a[212:213], v27 offset:45056\n\t" \
  "ds_read_b64_tr_b16 a[214:215], v27 offset:48128\n\t" \
  "v_mfma_f32_16x16x32_bf16 a[180:183], a[196:199], v[212:215], a[180:183]\n\t" \
  "v_mfma_f32_16x16x32_bf16 a[184:187], a[196:199], v[216:219], a[184:187]\n\t" \
  "v_mfma_f32_16x16x32_bf16 a[188:191], a[196:199], v[220:223], a[188:191]\n\t" \
  "s_waitcnt lgkmcnt(4)\n\t" \
  "s_barrier\n\t" \
  "v_mfma_f32_16x16x32_bf16 a[128:131], a[200:203], v[240:243], a[128:131]\n\t" \
  "ds_read_b64_tr_b16 a[192:193], v26 offset:49152\n\t" \
  "ds_read_b64_tr_b16 a[194:195], v26 offset:52224\n\t" \
  "v_mfma_f32_16x16x32_bf16 a[132:135], a[200:203], v[244:247], a[132:135]\n\t" \
  "v_mfma_f32_16x16x32_bf16 a[136:139], a[200:203], v[248:251], a[136:139]\n\t" \
  "v_mfma_f32_16x16x32_bf16 a[140:143], a[200:203], v[252:255], a[140:143]\n\t" \
  "v_mfma_f32_16x16x32_bf16 a[144:147], a[204:207], v[240:243], a[144:147]\n\t" \
  "ds_read_b64_tr_b16 a[196:197], v27 offset:49152\n\t" \
  "ds_read_b64_tr_b16 a[198:199], v27 offset:52224\n\t" \
  "v_mfma_f32_16x16x32_bf16 a[148:151], a[204:207], v[244:247], a[148:151]\n\t" \
  "v_mfma_f32_16x16x32_bf16 a[152:155], a[204:207], v[248:251], a[152:155]\n\t" \
  "v_mfma_f32_16x16x32_bf16 a[156:159], a[204:207], v[252:255], a[156:159]\n\t" \
  "s_waitcnt lgkmcnt(4)\n\t" \
  "v_mfma_f32_16x16x32_bf16 a[160:163], a[208:211], v[240:243], a[160:163]\n\t" \
  "ds_read_b64_tr_b16 a[200:201], v26 offset:55296\n\t" \
  "ds_read_b64_tr_b16 a[202:203], v26 offset:58368\n\t" \
  "v_mfma_f32_16x16x32_bf16 a[164:167], a[208:211], v[244:247], a[164:167]\n\t" \
  "v_mfma_f32_16x16x32_bf16 a[168:171], a[208:211], v[248:251], a[168:171]\n\t" \
  "v_mfma_f32_16x16x32_bf16 a[172:175], a[208:211], v[252:255], a[172:175]\n\t" \
  "v_mfma_f32_16x16x32_bf16 a[176:179], a[212:215], v[240:243], a[176:179]\n\t" \
  "ds_read_b64_tr_b16 a[204:205], v27 offset:55296\n\t" \
  "ds_read_b64_tr_b16 a[206:207], v27 offset:58368\n\t" \
  "v_mfma_f32_16x16x32_bf16 a[180:183], a[212:215], v[244:247], a[180:183]\n\t" \
  "v_mfma_f32_16x16x32_bf16 a[184:187], a[212:215], v[248:251], a[184:187]\n\t" \
  "v_mfma_f32_16x16x32_bf16 a[188:191], a[212:215], v[252:255], a[188:191]\n\t" \
  "s_waitcnt lgkmcnt(4)\n\t" \
  "v_mfma_f32_16x16x32_bf16 a[128:131], a[192:195], v[160:163], a[128:131]\n\t" \
  "ds_read_b64_tr_b16 a[208:209], v26 offset:50176\n\t" \
  "ds_read_b64_tr_b16 a[210:211], v26 offset:53248\n\t" \
  "v_mfma_f32_16x16x32_bf16 a[132:135], a[192:195], v[164:167], a[132:135]\n\t" \
  "v_mfma_f32_16x16x32_bf16 a[136:139], a[192:195], v[168:171], a[136:139]\n\t" \
  "v_mfma_f32_16x16x32_bf16 a[140:143], a[192:195], v[172:175], a[140:143]\n\t" \
  "v_mfma_f32_16x16x32_bf16 a[144:147], a[196:199], v[160:163], a[144:147]\n\t" \
  "ds_read_b64_tr_b16 a[212:213], v27 offset:50176\n\t" \
  "ds_read_b64_tr_b16 a[214:215], v27 offset:53248\n\t" \
  "v_mfma_f32_16x16x32_bf16 a[148:151], a[196:199], v[164:167], a[148:151]\n\t" \
  "v_mfma_f32_16x16x32_bf16 a[152:155], a[196:199], v[168:171], a[152:155]\n\t" \
  "v_mfma_f32_16x16x32_bf16 a[156:159], a[196:199], v[172:175], a[156:159]\n\t" \
  "s_waitcnt lgkmcnt(4)\n\t" \
  "v_mfma_f32_16x16x32_bf16 a[160:163], a[200:203], v[160:163], a[160:163]\n\t" \
  "ds_read_b64_tr_b16 a[192:193], v26 offset:56320\n\t" \
  "ds_read_b64_tr_b16 a[194:195], v26 offset:59392\n\t" \
  "v_mfma_f32_16x16x32_bf16 a[164:167], a[200:203], v[164:167], a[164:167]\n\t" \
  "v_mfma_f32_16x16x32_bf16 a[168:171], a[200:203], v[168:171], a[168:171]\n\t" \
  "v_mfma_f32_16x16x32_bf16 a[172:175], a[200:203], v[172:175], a[172:175]\n\t" \
  "v_mfma_f32_16x16x32_bf16 a[176:179], a[204:207], v[160:163], a[176:179]\n\t" \
  "ds_read_b64_tr_b16 a[196:197], v27 offset:56320\n\t" \
  "ds_read_b64_tr_b16 a[198:199], v27 offset:59392\n\t" \
  "v_mfma_f32_16x16x32_bf16 a[180:183], a[204:207], v[164:167], a[180:183]\n\t" \
  "v_mfma_f32_16x16x32_bf16 a[184:187], a[204:207], v[168:171], a[184:187]\n\t" \
  "v_mfma_f32_16x16x32_bf16 a[188:191], a[204:207], v[172:175], a[188:191]\n\t" \
  "s_waitcnt lgkmcnt(4)\n\t" \
  "v_mfma_f32_16x16x32_bf16 a[128:131], a[208:211], v[192:195], a[128:131]\n\t" \
  "ds_read_b64_tr_b16 a[200:201], v26 offset:51200\n\t" \
  "ds_read_b64_tr_b16 a[202:203], v26 offset:54272\n\t" \
  "v_mfma_f32_16x16x32_bf16 a[132:135], a[208:211], v[196:199], a[132:135]\n\t" \
  "v_mfma_f32_16x16x32_bf16 a[136:139], a[208:211], v[200:203], a[136:139]\n\t" \
  "v_mfma_f32_16x16x32_bf16 a[140:143], a[208:211], v[204:207], a[140:143]\n\t" \
  "v_mfma_f32_16x16x32_bf16 a[144:147], a[212:215], v[192:195], a[144:147]\n\t" \
  "ds_read_b64_tr_b16 a[204:205], v27 offset:51200\n\t" \
  "ds_read_b64_tr_b16 a[206:207], v27 offset:54272\n\t" \
  "v_mfma_f32_16x16x32_bf16 a[148:151], a[212:215], v[196:199], a[148:151]\n\t" \
  "v_mfma_f32_16x16x32_bf16 a[152:155], a[212:215], v[200:203], a[152:155]\n\t" \
  "v_mfma_f32_16x16x32_bf16 a[156:159], a[212:215], v[204:207], a[156:159]\n\t" \
  "s_waitcnt lgkmcnt(4)\n\t" \
  "v_mfma_f32_16x16x32_bf16 a[160:163], a[192:195], v[192:195], a[160:163]\n\t" \
  "ds_read_b64_tr_b16 a[208:209], v26 offset:57344\n\t" \
  "ds_read_b64_tr_b16 a[210:211], v26 offset:60416\n\t" \
  "v_mfma_f32_16x16x32_bf16 a[164:167], a[192:195], v[196:199], a[164:167]\n\t" \
  "v_mfma_f32_16x16x32_bf16 a[168:171], a[192:195], v[200:203], a[168:171]\n\t" \
  "v_mfma_f32_16x16x32_bf16 a[172:175], a[192:195], v[204:207], a[172:175]\n\t" \
  "v_mfma_f32_16x16x32_bf16 a[176:179], a[196:199], v[192:195], a[176:179]\n\t" \
  "ds_read_b64_tr_b16 a[212:213], v27 offset:57344\n\t" \
  "ds_read_b64_tr_b16 a[214:215], v27 offset:60416\n\t" \
  "v_mfma_f32_16x16x32_bf16 a[180:183], a[196:199], v[196:199], a[180:183]\n\t" \
  "v_mfma_f32_16x16x32_bf16 a[184:187], a[196:199], v[200:203], a[184:187]\n\t" \
  "s_waitcnt vmcnt(1)\n\t" \
  "v_mfma_f32_16x16x32_bf16 a[188:191], a[196:199], v[204:207], a[188:191]\n\t" \
  "s_waitcnt lgkmcnt(4)\n\t" \
  "v_mfma_f32_16x16x32_bf16 a[128:131], a[200:203], v[224:227], a[128:131]\n\t" \
  "buffer_store_dwordx2 v[36:37], v34, %3, s65 offen offset:512 sc1\n\t" \
  "v_mfma_f32_16x16x32_bf16 a[132:135], a[200:203], v[228:231], a[132:135]\n\t" \
  "v_mfma_f32_16x16x32_bf16 a[136:139], a[200:203], v[232:235], a[136:139]\n\t" \
  "v_mfma_f32_16x16x32_bf16 a[140:143], a[200:203], v[236:239], a[140:143]\n\t" \
  "v_mfma_f32_16x16x32_bf16 a[144:147], a[204:207], v[224:227], a[144:147]\n\t" \
  "buffer_load_dwordx2 v[40:41], v31, %3, 0 offen offset:0 sc1\n\t" \
  "v_mfma_f32_16x16x32_bf16 a[148:151], a[204:207], v[228:231], a[148:151]\n\t" \
  "v_mfma_f32_16x16x32_bf16 a[152:155], a[204:207], v[232:235], a[152:155]\n\t" \
  "v_mfma_f32_16x16x32_bf16 a[156:159], a[204:207], v[236:239], a[156:159]\n\t" \
  "s_waitcnt lgkmcnt(0)\n\t" \
  "v_mfma_f32_16x16x32_bf16 a[160:163], a[208:211], v[224:227], a[160:163]\n\t" \
  "v_mfma_f32_16x16x32_bf16 a[164:167], a[208:211], v[228:231], a[164:167]\n\t" \
  "v_mfma_f32_16x16x32_bf16 a[168:171], a[208:211], v[232:235], a[168:171]\n\t" \
  "v_mfma_f32_16x16x32_bf16 a[172:175], a[208:211], v[236:239], a[172:175]\n\t" \
  "v_mfma_f32_16x16x32_bf16 a[176:179], a[212:215], v[224:227], a[176:179]\n\t" \
  "v_mfma_f32_16x16x32_bf16 a[180:183], a[212:215], v[228:231], a[180:183]\n\t" \
  "v_mfma_f32_16x16x32_bf16 a[184:187], a[212:215], v[232:235], a[184:187]\n\t" \
  "v_mfma_f32_16x16x32_bf16 a[188:191], a[212:215], v[236:239], a[188:191]\n\t" \
  "s_nop 7\n\t" \
  "v_accvgpr_read_b32 v44, a128\n\t" \
  "v_accvgpr_read_b32 v45, a129\n\t" \
  "v_accvgpr_read_b32 v46, a130\n\t" \
  "v_accvgpr_read_b32 v47, a131\n\t" \
  "v_accvgpr_read_b32 v48, a144\n\t" \
  "v_accvgpr_read_b32 v49, a145\n\t" \
  "v_accvgpr_read_b32 v50, a146\n\t" \
  "v_accvgpr_read_b32 v51, a147\n\t" \
  "v_cvt_pk_bf16_f32 v20, v44, v45\n\t" \
  "v_cvt_pk_bf16_f32 v21, v46, v47\n\t" \
  "v_cvt_pk_bf16_f32 v22, v48, v49\n\t" \
  "v_cvt_pk_bf16_f32 v23, v50, v51\n\t" \
  "s_add_u32 s69, s48, 65536\n\t" \
  "buffer_store_dwordx4 v[20:23], v52, %3, s69 offen sc1\n\t" \
  "v_accvgpr_read_b32 v44, a160\n\t" \
  "v_accvgpr_read_b32 v45, a161\n\t" \
  "v_accvgpr_read_b32 v46, a162\n\t" \
  "v_accvgpr_read_b32 v47, a163\n\t" \
  "v_accvgpr_read_b32 v48, a176\n\t" \
  "v_accvgpr_read_b32 v49, a177\n\t" \
  "v_accvgpr_read_b32 v50, a178\n\t" \
  "v_accvgpr_read_b32 v51, a179\n\t" \
  "v_cvt_pk_bf16_f32 v60, v44, v45\n\t" \
  "v_cvt_pk_bf16_f32 v61, v46, v47\n\t" \
  "v_cvt_pk_bf16_f32 v62, v48, v49\n\t" \
  "v_cvt_pk_bf16_f32 v63, v50, v51\n\t" \
  "s_add_u32 s69, s48, 73728\n\t" \
  "buffer_store_dwordx4 v[60:63], v52, %3, s69 offen sc1\n\t" \
  "v_accvgpr_read_b32 v44, a132\n\t" \
  "v_accvgpr_read_b32 v45, a133\n\t" \
  "v_accvgpr_read_b32 v46, a134\n\t" \
  "v_accvgpr_read_b32 v47, a135\n\t" \
  "v_accvgpr_read_b32 v48, a148\n\t" \
  "v_accvgpr_read_b32 v49, a149\n\t" \
  "v_accvgpr_read_b32 v50, a150\n\t" \
  "v_accvgpr_read_b32 v51, a151\n\t" \
  "v_cvt_pk_bf16_f32 v20, v44, v45\n\t" \
  "v_cvt_pk_bf16_f32 v21, v46, v47\n\t" \
  "v_cvt_pk_bf16_f32 v22, v48, v49\n\t" \
  "v_cvt_pk_bf16_f32 v23, v50, v51\n\t" \
  "s_add_u32 s69, s48, 81920\n\t" \
  "buffer_store_dwordx4 v[20:23], v52, %3, s69 offen sc1\n\t" \
  "v_accvgpr_read_b32 v44, a164\n\t" \
  "v_accvgpr_read_b32 v45, a165\n\t" \
  "v_accvgpr_read_b32 v46, a166\n\t" \
  "v_accvgpr_read_b32 v47, a167\n\t" \
  "v_accvgpr_read_b32 v48, a180\n\t" \
  "v_accvgpr_read_b32 v49, a181\n\t" \
  "v_accvgpr_read_b32 v50, a182\n\t" \
  "v_accvgpr_read_b32 v51, a183\n\t" \
  "v_cvt_pk_bf16_f32 v60, v44, v45\n\t" \
  "v_cvt_pk_bf16_f32 v61, v46, v47\n\t" \
  "v_cvt_pk_bf16_f32 v62, v48, v49\n\t" \
  "v_cvt_pk_bf16_f32 v63, v50, v51\n\t" \
  "s_add_u32 s69, s48, 90112\n\t" \
  "buffer_store_dwordx4 v[60:63], v52, %3, s69 offen sc1\n\t" \
  "v_accvgpr_read_b32 v44, a136\n\t" \
  "v_accvgpr_read_b32 v45, a137\n\t" \
  "v_accvgpr_read_b32 v46, a138\n\t" \
  "v_accvgpr_read_b32 v47, a139\n\t" \
  "v_accvgpr_read_b32 v48, a152\n\t" \
  "v_accvgpr_read_b32 v49, a153\n\t" \
  "v_accvgpr_read_b32 v50, a154\n\t" \
  "v_accvgpr_read_b32 v51, a155\n\t" \
  "v_cvt_pk_bf16_f32 v20, v44, v45\n\t" \
  "v_cvt_pk_bf16_f32 v21, v46, v47\n\t" \
  "v_cvt_pk_bf16_f32 v22, v48, v49\n\t" \
  "v_cvt_pk_bf16_f32 v23, v50, v51\n\t" \
  "s_add_u32 s69, s74, 65536\n\t" \
  "buffer_store_dwordx4 v[20:23], v53, %3, s69 offen sc1\n\t" \
  "v_accvgpr_read_b32 v44, a168\n\t" \
  "v_accvgpr_read_b32 v45, a169\n\t" \
  "v_accvgpr_read_b32 v46, a170\n\t" \
  "v_accvgpr_read_b32 v47, a171\n\t" \
  "v_accvgpr_read_b32 v48, a184\n\t" \
  "v_accvgpr_read_b32 v49, a185\n\t" \
  "v_accvgpr_read_b32 v50, a186\n\t" \
  "v_accvgpr_read_b32 v51, a187\n\t" \
  "v_cvt_pk_bf16_f32 v60, v44, v45\n\t" \
  "v_cvt_pk_bf16_f32 v61, v46, v47\n\t" \
  "v_cvt_pk_bf16_f32 v62, v48, v49\n\t" \
  "v_cvt_pk_bf16_f32 v63, v50, v51\n\t" \
  "s_add_u32 s69, s74, 73728\n\t" \
  "buffer_store_dwordx4 v[60:63], v53, %3, s69 offen sc1\n\t" \
  "v_accvgpr_read_b32 v44, a140\n\t" \
  "v_accvgpr_read_b32 v45, a141\n\t" \
  "v_accvgpr_read_b32 v46, a142\n\t" \
  "v_accvgpr_read_b32 v47, a143\n\t" \
  "v_accvgpr_read_b32 v48, a156\n\t" \
  "v_accvgpr_read_b32 v49, a157\n\t" \
  "v_accvgpr_read_b32 v50, a158\n\t" \
  "v_accvgpr_read_b32 v51, a159\n\t" \
  "v_cvt_pk_bf16_f32 v20, v44, v45\n\t" \
  "v_cvt_pk_bf16_f32 v21, v46, v47\n\t" \
  "v_cvt_pk_bf16_f32 v22, v48, v49\n\t" \
  "v_cvt_pk_bf16_f32 v23, v50, v51\n\t" \
  "s_add_u32 s69, s74, 81920\n\t" \
  "buffer_store_dwordx4 v[20:23], v53, %3, s69 offen sc1\n\t" \
  "v_accvgpr_read_b32 v44, a172\n\t" \
  "v_accvgpr_read_b32 v45, a173\n\t" \
  "v_accvgpr_read_b32 v46, a174\n\t" \
  "v_accvgpr_read_b32 v47, a175\n\t" \
  "v_accvgpr_read_b32 v48, a188\n\t" \
  "v_accvgpr_read_b32 v49, a189\n\t" \
  "v_accvgpr_read_b32 v50, a190\n\t" \
  "v_accvgpr_read_b32 v51, a191\n\t" \
  "v_cvt_pk_bf16_f32 v60, v44, v45\n\t" \
  "v_cvt_pk_bf16_f32 v61, v46, v47\n\t" \
  "v_cvt_pk_bf16_f32 v62, v48, v49\n\t" \
  "v_cvt_pk_bf16_f32 v63, v50, v51\n\t" \
  "s_add_u32 s69, s74, 90112\n\t" \
  "buffer_store_dwordx4 v[60:63], v53, %3, s69 offen sc1\n\t" \
  "buffer_load_dwordx2 v[38:39], v35, %3, 0 offen offset:512 sc1\n\t" \
  "v_mov_b32 v86, v33\n\t" \
  "v_accvgpr_read_b32 v84, a255\n\t" \
  "v_accvgpr_read_b32 v93, a254\n\t" \
  "v_accvgpr_read_b32 v144, a242\n\t" \
  "v_accvgpr_read_b32 v145, a243\n\t" \
  "v_accvgpr_read_b32 v146, a244\n\t" \
  "v_accvgpr_read_b32 v147, a245\n\t" \
  "v_accvgpr_read_b32 v148, a246\n\t" \
  "v_accvgpr_read_b32 v149, a247\n\t" \
  "v_accvgpr_read_b32 v150, a248\n\t" \
  "v_accvgpr_read_b32 v151, a249\n\t" \
  "v_accvgpr_read_b32 v152, a250\n\t" \
  "v_accvgpr_read_b32 v153, a251\n\t" \
  "v_accvgpr_read_b32 v154, a252\n\t" \
  "v_accvgpr_read_b32 v155, a253\n\t" \
  "v_accvgpr_read_b32 v156, a216\n\t" \
  "v_accvgpr_read_b32 v157, a217\n\t" \
  "v_accvgpr_read_b32 v158, a228\n\t" \
  "v_accvgpr_read_b32 v159, a229\n\t" \
  "v_accvgpr_read_b32 v80, a218\n\t" \
  "v_accvgpr_read_b32 v81, a219\n\t" \
  "v_accvgpr_read_b32 v82, a230\n\t" \
  "v_accvgpr_read_b32 v83, a231\n\t" \
  "v_accvgpr_read_b32 v112, a220\n\t" \
  "v_accvgpr_read_b32 v113, a221\n\t" \
  "v_accvgpr_read_b32 v114, a232\n\t" \
  "v_accvgpr_read_b32 v115, a233\n\t" \
  "v_mov_b32 v42, 0x3f80\n\t" \
  "v_mov_b32 v43, 0x3f800000\n\t" \
  "s_mul_i32 s54, %7, 3072\n\t" \
  "v_add_u32 v33, s54, v32\n\t" \
  "v_lshrrev_b32 v85, 1, v32\n\t" \
  "v_add_u32 v85, s54, v85\n\t" \
  "s_or_b32 s54, s75, s76\n\t" \
  "s_cmp_eq_u32 s54, 0\n\t" \
  "s_cbranch_scc0 L_ownA1_%=\n\t" \
  "L_ownB1_%=:\n\t" \
  "s_lshl_b32 s54, %7, 11\n\t" \
  "s_add_u32 s54, s54, 24576\n\t" \
  "v_add_u32 v160, s54, v32\n\t" \
  "s_barrier\n\t" \
  "ds_read_b128 v[64:67], v160 offset:0\n\t" \
  "ds_read_b128 v[68:71], v160 offset:1024\n\t" \
  "ds_read_b128 v[72:75], v160 offset:8192\n\t" \
  "ds_read_b128 v[76:79], v160 offset:9216\n\t" \
  "ds_read_b128 v[44:47], v160 offset:16384\n\t" \
  "ds_read_b128 v[48:51], v160 offset:17408\n\t" \
  "s_waitcnt lgkmcnt(0)\n\t" \
  "s_waitcnt vmcnt(1)\n\t" \
  "buffer_store_dwordx2 v[36:37], v34, %3, s65 offen offset:1024 sc1\n\t" \
  "v_cmp_eq_u64 vcc, v[36:37], v[40:41]\n\t" \
  "s_nop 1\n\t" \
  "s_cmp_eq_u64 vcc, -1\n\t" \
  "s_cbranch_scc0 L_slow4_%=\n\t" \
  "L_back4_%=:\n\t" \
  "s_add_u32 s68, s70, 0\n\t" \
  "s_add_u32 s67, s70, 4096\n\t" \
  "buffer_load_dwordx4 v[160:163], v32, %3, s68 offen offset:0 sc1\n\t" \
  "buffer_load_dwordx4 v[164:167], v32, %3, s68 offen offset:1024 sc1\n\t" \
  "buffer_load_dwordx4 v[168:171], v32, %3, s68 offen offset:2048 sc1\n\t" \
  "buffer_load_dwordx4 v[172:175], v32, %3, s68 offen offset:3072 sc1\n\t" \
  "buffer_load_dwordx4 v[176:179], v32, %3, s67 offen offset:0 sc1\n\t" \
  "buffer_load_dwordx4 v[180:183], v32, %3, s67 offen offset:1024 sc1\n\t" \
  "buffer_load_dwordx4 v[184:187], v32, %3, s67 offen offset:2048 sc1\n\t" \
  "s_add_u32 s54, s97, 0\n\t" \
  "buffer_load_dwordx4 v[96:99], v86, %3, s54 offen sc1\n\t" \
  "s_add_u32 s54, s97, 262144\n\t" \
  "buffer_load_dwordx4 v[100:103], v86, %3, s54 offen sc1\n\t" \
  "s_add_u32 s54, s97, 524288\n\t" \
  "buffer_load_dwordx4 v[104:107], v86, %3, s54 offen sc1\n\t" \
  "s_waitcnt vmcnt(11)\n\t" \
  "v_cmp_eq_u64 vcc, v[36:37], v[38:39]\n\t" \
  "s_nop 1\n\t" \
  "s_cmp_eq_u64 vcc, -1\n\t" \
  "s_cbranch_scc0 L_slow5_%=\n\t" \
  "L_back5_%=:\n\t" \
  "s_add_u32 s68, s70, 32768\n\t" \
  "s_add_u32 s67, s70, 36864\n\t" \
  "buffer_load_dwordx4 v[192:195], v32, %3, s68 offen offset:0 sc1\n\t" \
  "buffer_load_dwordx4 v[196:199], v32, %3, s68 offen offset:1024 sc1\n\t" \
  "buffer_load_dwordx4 v[200:203], v32, %3, s68 offen offset:2048 sc1\n\t" \
  "buffer_load_dwordx4 v[204:207], v32, %3, s68 offen offset:3072 sc1\n\t" \
  "buffer_load_dwordx4 v[208:211], v32, %3, s67 offen offset:0 sc1\n\t" \
  "buffer_load_dwordx4 v[212:215], v32, %3, s67 offen offset:1024 sc1\n\t" \
  "buffer_load_dwordx4 v[216:219], v32, %3, s67 offen offset:2048 sc1\n\t" \
  "s_waitcnt vmcnt(16)\n\t" \
  "v_dot2c_f32_bf16 v64, v160, v42\n\t" \
  "v_dot2c_f32_bf16 v65, v160, v43\n\t" \
  "v_dot2c_f32_bf16 v66, v161, v42\n\t" \
  "v_dot2c_f32_bf16 v67, v161, v43\n\t" \
  "v_dot2c_f32_bf16 v68, v162, v42\n\t" \
  "v_dot2c_f32_bf16 v69, v162, v43\n\t" \
  "v_dot2c_f32_bf16 v70, v163, v42\n\t" \
  "v_dot2c_f32_bf16 v71, v163, v43\n\t" \
  "s_waitcnt vmcnt(15)\n\t" \
  "v_dot2c_f32_bf16 v64, v164, v42\n\t" \
  "v_dot2c_f32_bf16 v65, v164, v43\n\t" \
  "v_dot2c_f32_bf16 v66, v165, v42\n\t" \
  "v_dot2c_f32_bf16 v67, v165, v43\n\t" \
  "v_dot2c_f32_bf16 v68, v166, v42\n\t" \
  "v_dot2c_f32_bf16 v69, v166, v43\n\t" \
  "v_dot2c_f32_bf16 v70, v167, v42\n\t" \
  "v_dot2c_f32_bf16 v71, v167, v43\n\t" \
  "s_waitcnt vmcnt(14)\n\t" \
  "v_dot2c_f32_bf16 v64, v168, v42\n\t" \
  "v_dot2c_f32_bf16 v65, v168, v43\n\t" \
  "v_dot2c_f32_bf16 v66, v169, v42\n\t" \
  "v_dot2c_f32_bf16 v67, v169, v43\n\t" \
  "v_dot2c_f32_bf16 v68, v170, v42\n\t" \
  "v_dot2c_f32_bf16 v69, v170, v43\n\t" \
  "v_dot2c_f32_bf16 v70, v171, v42\n\t" \
  "v_dot2c_f32_bf16 v71, v171, v43\n\t" \
  "s_waitcnt vmcnt(13)\n\t" \
  "v_dot2c_f32_bf16 v64, v172, v42\n\t" \
  "v_dot2c_f32_bf16 v65, v172, v43\n\t" \
  "v_dot2c_f32_bf16 v66, v173, v42\n\t" \
  "v_dot2c_f32_bf16 v67, v173, v43\n\t" \
  "v_dot2c_f32_bf16 v68, v174, v42\n\t" \
  "v_dot2c_f32_bf16 v69, v174, v43\n\t" \
  "v_dot2c_f32_bf16 v70, v175, v42\n\t" \
  "v_dot2c_f32_bf16 v71, v175, v43\n\t" \
  "s_waitcnt vmcnt(12)\n\t" \
  "v_dot2c_f32_bf16 v64, v176, v42\n\t" \
  "v_dot2c_f32_bf16 v65, v176, v43\n\t" \
  "v_dot2c_f32_bf16 v66, v177, v42\n\t" \
  "v_dot2c_f32_bf16 v67, v177, v43\n\t" \
  "v_dot2c_f32_bf16 v68, v178, v42\n\t" \
  "v_dot2c_f32_bf16 v69, v178, v43\n\t" \
  "v_dot2c_f32_bf16 v70, v179, v42\n\t" \
  "v_dot2c_f32_bf16 v71, v179, v43\n\t" \
  "s_waitcnt vmcnt(11)\n\t" \
  "v_dot2c_f32_bf16 v64, v180, v42\n\t" \
  "v_dot2c_f32_bf16 v65, v180, v43\n\t" \
  "v_dot2c_f32_bf16 v66, v181, v42\n\t" \
  "v_dot2c_f32_bf16 v67, v181, v43\n\t" \
  "v_dot2c_f32_bf16 v68, v182, v42\n\t" \
  "v_dot2c_f32_bf16 v69, v182, v43\n\t" \
  "v_dot2c_f32_bf16 v70, v183, v42\n\t" \
  "v_dot2c_f32_bf16 v71, v183, v43\n\t" \
  "s_waitcnt vmcnt(10)\n\t" \
  "v_dot2c_f32_bf16 v64, v184, v42\n\t" \
  "v_dot2c_f32_bf16 v65, v184, v43\n\t" \
  "v_dot2c_f32_bf16 v66, v185, v42\n\t" \
  "v_dot2c_f32_bf16 v67, v185, v43\n\t" \
  "v_dot2c_f32_bf16 v68, v186, v42\n\t" \
  "v_dot2c_f32_bf16 v69, v186, v43\n\t" \
  "v_dot2c_f32_bf16 v70, v187, v42\n\t" \
  "v_dot2c_f32_bf16 v71, v187, v43\n\t" \
  "s_waitcnt vmcnt(7)\n\t" \
  "buffer_load_dwordx2 v[38:39], v35, %3, 0 offen offset:1024 sc1\n\t" \
  "ds_write_b128 v33, v[96:99] offset:0\n\t" \
  "ds_write_b128 v33, v[100:103] offset:1024\n\t" \
  "ds_write_b128 v33, v[104:107] offset:2048\n\t" \
  "ds_read_b64_tr_b16 v[128:129], v85 offset:0\n\t" \
  "ds_read_b64_tr_b16 v[130:131], v85 offset:512\n\t" \
  "ds_read_b64_tr_b16 v[132:133], v85 offset:1024\n\t" \
  "ds_read_b64_tr_b16 v[134:135], v85 offset:1536\n\t" \
  "ds_read_b64_tr_b16 v[136:137], v85 offset:2048\n\t" \
  "ds_read_b64_tr_b16 v[138:139], v85 offset:2560\n\t" \
  "s_waitcnt lgkmcnt(5)\n\t" \
  "v_dot2c_f32_bf16 v144, v128, v42\n\t" \
  "v_dot2c_f32_bf16 v145, v128, v43\n\t" \
  "v_dot2c_f32_bf16 v146, v129, v42\n\t" \
  "v_dot2c_f32_bf16 v147, v129, v43\n\t" \
  "s_waitcnt lgkmcnt(4)\n\t" \
  "v_dot2c_f32_bf16 v148, v130, v42\n\t" \
  "v_dot2c_f32_bf16 v149, v130, v43\n\t" \
  "v_dot2c_f32_bf16 v150, v131, v42\n\t" \
  "v_dot2c_f32_bf16 v151, v131, v43\n\t" \
  "s_nop 2\n\t" \
  "v_fmac_f32 v144, v64, v93\n\t" \
  "v_fmac_f32 v145, v65, v93\n\t" \
  "v_fmac_f32 v146, v66, v93\n\t" \
  "v_fmac_f32 v147, v67, v93\n\t" \
  "v_cvt_pk_bf16_f32 v144, v144, v145\n\t" \
  "v_cvt_pk_bf16_f32 v145, v146, v147\n\t" \
  "buffer_store_dwordx2 v[144:145], v84, %8, s96 offen offset:0\n\t" \
  "v_fmac_f32 v148, v68, v93\n\t" \
  "v_fmac_f32 v149, v69, v93\n\t" \
  "v_fmac_f32 v150, v70, v93\n\t" \
  "v_fmac_f32 v151, v71, v93\n\t" \
  "v_cvt_pk_bf16_f32 v148, v148, v149\n\t" \
  "v_cvt_pk_bf16_f32 v149, v150, v151\n\t" \
  "buffer_store_dwordx2 v[148:149], v84, %8, s96 offen offset:32\n\t" \
  "s_waitcnt lgkmcnt(3)\n\t" \
  "v_dot2c_f32_bf16 v152, v132, v42\n\t" \
  "v_dot2c_f32_bf16 v153, v132, v43\n\t" \
  "v_dot2c_f32_bf16 v154, v133, v42\n\t" \
  "v_dot2c_f32_bf16 v155, v133, v43\n\t" \
  "s_waitcnt lgkmcnt(2)\n\t" \
  "v_dot2c_f32_bf16 v156, v134, v42\n\t" \
  "v_dot2c_f32_bf16 v157, v134, v43\n\t" \
  "v_dot2c_f32_bf16 v158, v135, v42\n\t" \
  "v_dot2c_f32_bf16 v159, v135, v43\n\t" \
  "s_waitcnt lgkmcnt(1)\n\t" \
  "v_dot2c_f32_bf16 v80, v136, v42\n\t" \
  "v_dot2c_f32_bf16 v81, v136, v43\n\t" \
  "v_dot2c_f32_bf16 v82, v137, v42\n\t" \
  "v_dot2c_f32_bf16 v83, v137, v43\n\t" \
  "s_waitcnt lgkmcnt(0)\n\t" \
  "v_dot2c_f32_bf16 v112, v138, v42\n\t" \
  "v_dot2c_f32_bf16 v113, v138, v43\n\t" \
  "v_dot2c_f32_bf16 v114, v139, v42\n\t" \
  "v_dot2c_f32_bf16 v115, v139, v43\n\t" \
  "s_waitcnt vmcnt(9)\n\t" \
  "v_dot2c_f32_bf16 v72, v192, v42\n\t" \
  "v_dot2c_f32_bf16 v73, v192, v43\n\t" \
  "v_dot2c_f32_bf16 v74, v193, v42\n\t" \
  "v_dot2c_f32_bf16 v75, v193, v43\n\t" \
  "v_dot2c_f32_bf16 v76, v194, v42\n\t" \
  "v_dot2c_f32_bf16 v77, v194, v43\n\t" \
  "v_dot2c_f32_bf16 v78, v195, v42\n\t" \
  "v_dot2c_f32_bf16 v79, v195, v43\n\t" \
  "s_waitcnt vmcnt(8)\n\t" \
  "v_dot2c_f32_bf16 v72, v196, v42\n\t" \
  "v_dot2c_f32_bf16 v73, v196, v43\n\t" \
  "v_dot2c_f32_bf16 v74, v197, v42\n\t" \
  "v_dot2c_f32_bf16 v75, v197, v43\n\t" \
  "v_dot2c_f32_bf16 v76, v198, v42\n\t" \
  "v_dot2c_f32_bf16 v77, v198, v43\n\t" \
  "v_dot2c_f32_bf16 v78, v199, v42\n\t" \
  "v_dot2c_f32_bf16 v79, v199, v43\n\t" \
  "s_waitcnt vmcnt(7)\n\t" \
  "v_dot2c_f32_bf16 v72, v200, v42\n\t" \
  "v_dot2c_f32_bf16 v73, v200, v43\n\t" \
  "v_dot2c_f32_bf16 v74, v201, v42\n\t" \
  "v_dot2c_f32_bf16 v75, v201, v43\n\t" \
  "v_dot2c_f32_bf16 v76, v202, v42\n\t" \
  "v_dot2c_f32_bf16 v77, v202, v43\n\t" \
  "v_dot2c_f32_bf16 v78, v203, v42\n\t" \
  "v_dot2c_f32_bf16 v79, v203, v43\n\t" \
  "s_waitcnt vmcnt(6)\n\t" \
  "v_dot2c_f32_bf16 v72, v204, v42\n\t" \
  "v_dot2c_f32_bf16 v73, v204, v43\n\t" \
  "v_dot2c_f32_bf16 v74, v205, v42\n\t" \
  "v_dot2c_f32_bf16 v75, v205, v43\n\t" \
  "v_dot2c_f32_bf16 v76, v206, v42\n\t" \
  "v_dot2c_f32_bf16 v77, v206, v43\n\t" \
  "v_dot2c_f32_bf16 v78, v207, v42\n\t" \
  "v_dot2c_f32_bf16 v79, v207, v43\n\t" \
  "s_waitcnt vmcnt(5)\n\t" \
  "v_dot2c_f32_bf16 v72, v208, v42\n\t" \
  "v_dot2c_f32_bf16 v73, v208, v43\n\t" \
  "v_dot2c_f32_bf16 v74, v209, v42\n\t" \
  "v_dot2c_f32_bf16 v75, v209, v43\n\t" \
  "v_dot2c_f32_bf16 v76, v210, v42\n\t" \
  "v_dot2c_f32_bf16 v77, v210, v43\n\t" \
  "v_dot2c_f32_bf16 v78, v211, v42\n\t" \
  "v_dot2c_f32_bf16 v79, v211, v43\n\t" \
  "s_waitcnt vmcnt(4)\n\t" \
  "v_dot2c_f32_bf16 v72, v212, v42\n\t" \
  "v_dot2c_f32_bf16 v73, v212, v43\n\t" \
  "v_dot2c_f32_bf16 v74, v213, v42\n\t" \
  "v_dot2c_f32_bf16 v75, v213, v43\n\t" \
  "v_dot2c_f32_bf16 v76, v214, v42\n\t" \
  "v_dot2c_f32_bf16 v77, v214, v43\n\t" \
  "v_dot2c_f32_bf16 v78, v215, v42\n\t" \
  "v_dot2c_f32_bf16 v79, v215, v43\n\t" \
  "s_waitcnt vmcnt(3)\n\t" \
  "v_dot2c_f32_bf16 v72, v216, v42\n\t" \
  "v_dot2c_f32_bf16 v73, v216, v43\n\t" \
  "v_dot2c_f32_bf16 v74, v217, v42\n\t" \
  "v_dot2c_f32_bf16 v75, v217, v43\n\t" \
  "v_dot2c_f32_bf16 v76, v218, v42\n\t" \
  "v_dot2c_f32_bf16 v77, v218, v43\n\t" \
  "v_dot2c_f32_bf16 v78, v219, v42\n\t" \
  "v_dot2c_f32_bf16 v79, v219, v43\n\t" \
  "s_waitcnt vmcnt(2)\n\t" \
  "v_cmp_eq_u64 vcc, v[36:37], v[38:39]\n\t" \
  "s_nop 1\n\t" \
  "s_cmp_eq_u64 vcc, -1\n\t" \
  "s_cbranch_scc0 L_slow6_%=\n\t" \
  "L_back6_%=:\n\t" \
  "s_add_u32 s68, s70, 65536\n\t" \
  "s_add_u32 s67, s70, 69632\n\t" \
  "buffer_load_dwordx4 v[224:227], v32, %3, s68 offen offset:0 sc1\n\t" \
  "buffer_load_dwordx4 v[228:231], v32, %3, s68 offen offset:1024 sc1\n\t" \
  "buffer_load_dwordx4 v[232:235], v32, %3, s68 offen offset:2048 sc1\n\t" \
  "buffer_load_dwordx4 v[236:239], v32, %3, s68 offen offset:3072 sc1\n\t" \
  "buffer_load_dwordx4 v[240:243], v32, %3, s67 offen offset:0 sc1\n\t" \
  "buffer_load_dwordx4 v[244:247], v32, %3, s67 offen offset:1024 sc1\n\t" \
  "buffer_load_dwordx4 v[248:251], v32, %3, s67 offen offset:2048 sc1\n\t" \
  "s_nop 2\n\t" \
  "v_fmac_f32 v152, v72, v93\n\t" \
  "v_fmac_f32 v153, v73, v93\n\t" \
  "v_fmac_f32 v154, v74, v93\n\t" \
  "v_fmac_f32 v155, v75, v93\n\t" \
  "v_cvt_pk_bf16_f32 v152, v152, v153\n\t" \
  "v_cvt_pk_bf16_f32 v153, v154, v155\n\t" \
  "buffer_store_dwordx2 v[152:153], v84, %8, s96 offen offset:128\n\t" \
  "v_fmac_f32 v156, v76, v93\n\t" \
  "v_fmac_f32 v157, v77, v93\n\t" \
  "v_fmac_f32 v158, v78, v93\n\t" \
  "v_fmac_f32 v159, v79, v93\n\t" \
  "v_cvt_pk_bf16_f32 v156, v156, v157\n\t" \
  "v_cvt_pk_bf16_f32 v157, v158, v159\n\t" \
  "buffer_store_dwordx2 v[156:157], v84, %8, s96 offen offset:160\n\t" \
  "s_waitcnt vmcnt(8)\n\t" \
  "v_dot2c_f32_bf16 v44, v224, v42\n\t" \
  "v_dot2c_f32_bf16 v45, v224, v43\n\t" \
  "v_dot2c_f32_bf16 v46, v225, v42\n\t" \
  "v_dot2c_f32_bf16 v47, v225, v43\n\t" \
  "v_dot2c_f32_bf16 v48, v226, v42\n\t" \
  "v_dot2c_f32_bf16 v49, v226, v43\n\t" \
  "v_dot2c_f32_bf16 v50, v227, v42\n\t" \
  "v_dot2c_f32_bf16 v51, v227, v43\n\t" \
  "s_waitcnt vmcnt(7)\n\t" \
  "v_dot2c_f32_bf16 v44, v228, v42\n\t" \
  "v_dot2c_f32_bf16 v45, v228, v43\n\t" \
  "v_dot2c_f32_bf16 v46, v229, v42\n\t" \
  "v_dot2c_f32_bf16 v47, v229, v43\n\t" \
  "v_dot2c_f32_bf16 v48, v230, v42\n\t" \
  "v_dot2c_f32_bf16 v49, v230, v43\n\t" \
  "v_dot2c_f32_bf16 v50, v231, v42\n\t" \
  "v_dot2c_f32_bf16 v51, v231, v43\n\t" \
  "s_waitcnt vmcnt(6)\n\t" \
  "v_dot2c_f32_bf16 v44, v232, v42\n\t" \
  "v_dot2c_f32_bf16 v45, v232, v43\n\t" \
  "v_dot2c_f32_bf16 v46, v233, v42\n\t" \
  "v_dot2c_f32_bf16 v47, v233, v43\n\t" \
  "v_dot2c_f32_bf16 v48, v234, v42\n\t" \
  "v_dot2c_f32_bf16 v49, v234, v43\n\t" \
  "v_dot2c_f32_bf16 v50, v235, v42\n\t" \
  "v_dot2c_f32_bf16 v51, v235, v43\n\t" \
  "s_waitcnt vmcnt(5)\n\t" \
  "v_dot2c_f32_bf16 v44, v236, v42\n\t" \
  "v_dot2c_f32_bf16 v45, v236, v43\n\t" \
  "v_dot2c_f32_bf16 v46, v237, v42\n\t" \
  "v_dot2c_f32_bf16 v47, v237, v43\n\t" \
  "v_dot2c_f32_bf16 v48, v238, v42\n\t" \
  "v_dot2c_f32_bf16 v49, v238, v43\n\t" \
  "v_dot2c_f32_bf16 v50, v239, v42\n\t" \
  "v_dot2c_f32_bf16 v51, v239, v43\n\t" \
  "s_waitcnt vmcnt(4)\n\t" \
  "v_dot2c_f32_bf16 v44, v240, v42\n\t" \
  "v_dot2c_f32_bf16 v45, v240, v43\n\t" \
  "v_dot2c_f32_bf16 v46, v241, v42\n\t" \
  "v_dot2c_f32_bf16 v47, v241, v43\n\t" \
  "v_dot2c_f32_bf16 v48, v242, v42\n\t" \
  "v_dot2c_f32_bf16 v49, v242, v43\n\t" \
  "v_dot2c_f32_bf16 v50, v243, v42\n\t" \
  "v_dot2c_f32_bf16 v51, v243, v43\n\t" \
  "s_waitcnt vmcnt(3)\n\t" \
  "v_dot2c_f32_bf16 v44, v244, v42\n\t" \
  "v_dot2c_f32_bf16 v45, v244, v43\n\t" \
  "v_dot2c_f32_bf16 v46, v245, v42\n\t" \
  "v_dot2c_f32_bf16 v47, v245, v43\n\t" \
  "v_dot2c_f32_bf16 v48, v246, v42\n\t" \
  "v_dot2c_f32_bf16 v49, v246, v43\n\t" \
  "v_dot2c_f32_bf16 v50, v247, v42\n\t" \
  "v_dot2c_f32_bf16 v51, v247, v43\n\t" \
  "s_waitcnt vmcnt(2)\n\t" \
  "v_dot2c_f32_bf16 v44, v248, v42\n\t" \
  "v_dot2c_f32_bf16 v45, v248, v43\n\t" \
  "v_dot2c_f32_bf16 v46, v249, v42\n\t" \
  "v_dot2c_f32_bf16 v47, v249, v43\n\t" \
  "v_dot2c_f32_bf16 v48, v250, v42\n\t" \
  "v_dot2c_f32_bf16 v49, v250, v43\n\t" \
  "v_dot2c_f32_bf16 v50, v251, v42\n\t" \
  "v_dot2c_f32_bf16 v51, v251, v43\n\t" \
  "s_nop 2\n\t" \
  "v_fmac_f32 v80, v44, v93\n\t" \
  "v_fmac_f32 v81, v45, v93\n\t" \
  "v_fmac_f32 v82, v46, v93\n\t" \
  "v_fmac_f32 v83, v47, v93\n\t" \
  "v_cvt_pk_bf16_f32 v80, v80, v81\n\t" \
  "v_cvt_pk_bf16_f32 v81, v82, v83\n\t" \
  "buffer_store_dwordx2 v[80:81], v84, %8, s96 offen offset:256\n\t" \
  "v_fmac_f32 v112, v48, v93\n\t" \
  "v_fmac_f32 v113, v49, v93\n\t" \
  "v_fmac_f32 v114, v50, v93\n\t" \
  "v_fmac_f32 v115, v51, v93\n\t" \
  "v_cvt_pk_bf16_f32 v112, v112, v113\n\t" \
  "v_cvt_pk_bf16_f32 v113, v114, v115\n\t" \
  "buffer_store_dwordx2 v[112:113], v84, %8, s96 offen offset:288\n\t" \
  "s_waitcnt vmcnt(0) lgkmcnt(0)\n\t" \
  "s_branch L_end_%=\n\t" \
  "L_slow0_%=:\n\t" \
  "s_mov_b32 s66, 0\n\t" \
  "L_spin0_%=:\n\t" \
  "s_sleep 1\n\t" \
  "buffer_load_dwordx2 v[40:41], v58, %3, 0 offen offset:0 sc1\n\t" \
  "s_waitcnt vmcnt(0)\n\t" \
  "v_cmp_eq_u64 vcc, v[36:37], v[40:41]\n\t" \
  "s_add_u32 s66, s66, 1\n\t" \
  "s_nop 1\n\t" \
  "s_cmp_eq_u64 vcc, -1\n\t" \
  "s_cbranch_scc1 L_back0_%=\n\t" \
  "s_cmp_lt_u32 s66, 0x40000\n\t" \
  "s_cbranch_scc1 L_spin0_%=\n\t" \
  "s_branch L_back0_%=\n\t" \
  "L_slow1_%=:\n\t" \
  "s_mov_b32 s66, 0\n\t" \
  "L_spin1_%=:\n\t" \
  "s_sleep 1\n\t" \
  "buffer_load_dwordx2 v[38:39], v59, %3, 0 offen offset:0 sc1\n\t" \
  "s_waitcnt vmcnt(0)\n\t" \
  "v_cmp_eq_u64 vcc, v[36:37], v[38:39]\n\t" \
  "s_add_u32 s66, s66, 1\n\t" \
  "s_nop 1\n\t" \
  "s_cmp_eq_u64 vcc, -1\n\t" \
  "s_cbranch_scc1 L_back1_%=\n\t" \
  "s_cmp_lt_u32 s66, 0x40000\n\t" \
  "s_cbranch_scc1 L_spin1_%=\n\t" \
  "s_branch L_back1_%=\n\t" \
  "L_slow2_%=:\n\t" \
  "s_mov_b32 s66, 0\n\t" \
  "L_spin2_%=:\n\t" \
  "s_sleep 1\n\t" \
  "buffer_load_dwordx2 v[38:39], v30, %3, 0 offen offset:0 sc1\n\t" \
  "s_waitcnt vmcnt(0)\n\t" \
  "v_cmp_eq_u64 vcc, v[36:37], v[38:39]\n\t" \
  "s_add_u32 s66, s66, 1\n\t" \
  "s_nop 1\n\t" \
  "s_cmp_eq_u64 vcc, -1\n\t" \
  "s_cbranch_scc1 L_back2_%=\n\t" \
  "s_cmp_lt_u32 s66, 0x40000\n\t" \
  "s_cbranch_scc1 L_spin2_%=\n\t" \
  "s_branch L_back2_%=\n\t" \
  "L_slow3_%=:\n\t" \
  "s_mov_b32 s66, 0\n\t" \
  "L_spin3_%=:\n\t" \
  "s_sleep 1\n\t" \
  "buffer_load_dwordx2 v[38:39], v30, %3, 0 offen offset:64 sc1\n\t" \
  "s_waitcnt vmcnt(0)\n\t" \
  "v_cmp_eq_u64 vcc, v[36:37], v[38:39]\n\t" \
  "s_add_u32 s66, s66, 1\n\t" \
  "s_nop 1\n\t" \
  "s_cmp_eq_u64 vcc, -1\n\t" \
  "s_cbranch_scc1 L_back3_%=\n\t" \
  "s_cmp_lt_u32 s66, 0x40000\n\t" \
  "s_cbranch_scc1 L_spin3_%=\n\t" \
  "s_branch L_back3_%=\n\t" \
  "L_ownA1_%=:\n\t" \
  "v_add_u32 v160, 24576, v32\n\t" \
  "s_cmp_lg_u32 s76, 0\n\t" \
  "s_cbranch_scc1 L_ownC1_%=\n\t" \
  "ds_write_b128 v160, a[0:3] offset:0\n\t" \
  "ds_write_b128 v160, a[16:19] offset:1024\n\t" \
  "ds_write_b128 v160, a[32:35] offset:2048\n\t" \
  "ds_write_b128 v160, a[48:51] offset:3072\n\t" \
  "ds_write_b128 v160, a[4:7] offset:4096\n\t" \
  "ds_write_b128 v160, a[20:23] offset:5120\n\t" \
  "ds_write_b128 v160, a[36:39] offset:6144\n\t" \
  "ds_write_b128 v160, a[52:55] offset:7168\n\t" \
  "ds_write_b128 v160, a[64:67] offset:8192\n\t" \
  "ds_write_b128 v160, a[80:83] offset:9216\n\t" \
  "ds_write_b128 v160, a[96:99] offset:10240\n\t" \
  "ds_write_b128 v160, a[112:115] offset:11264\n\t" \
  "ds_write_b128 v160, a[68:71] offset:12288\n\t" \
  "ds_write_b128 v160, a[84:87] offset:13312\n\t" \
  "ds_write_b128 v160, a[100:103] offset:14336\n\t" \
  "ds_write_b128 v160, a[116:119] offset:15360\n\t" \
  "ds_write_b128 v160, a[128:131] offset:16384\n\t" \
  "ds_write_b128 v160, a[144:147] offset:17408\n\t" \
  "ds_write_b128 v160, a[160:163] offset:18432\n\t" \
  "ds_write_b128 v160, a[176:179] offset:19456\n\t" \
  "ds_write_b128 v160, a[132:135] offset:20480\n\t" \
  "ds_write_b128 v160, a[148:151] offset:21504\n\t" \
  "ds_write_b128 v160, a[164:167] offset:22528\n\t" \
  "ds_write_b128 v160, a[180:183] offset:23552\n\t" \
  "s_waitcnt lgkmcnt(0)\n\t" \
  "s_branch L_ownB1_%=\n\t" \
  "L_ownC1_%=:\n\t" \
  "ds_write_b128 v160, a[8:11] offset:0\n\t" \
  "ds_write_b128 v160, a[24:27] offset:1024\n\t" \
  "ds_write_b128 v160, a[40:43] offset:2048\n\t" \
  "ds_write_b128 v160, a[56:59] offset:3072\n\t" \
  "ds_write_b128 v160, a[12:15] offset:4096\n\t" \
  "ds_write_b128 v160, a[28:31] offset:5120\n\t" \
  "ds_write_b128 v160, a[44:47] offset:6144\n\t" \
  "ds_write_b128 v160, a[60:63] offset:7168\n\t" \
  "ds_write_b128 v160, a[72:75] offset:8192\n\t" \
  "ds_write_b128 v160, a[88:91] offset:9216\n\t" \
  "ds_write_b128 v160, a[104:107] offset:10240\n\t" \
  "ds_write_b128 v160, a[120:123] offset:11264\n\t" \
  "ds_write_b128 v160, a[76:79] offset:12288\n\t" \
  "ds_write_b128 v160, a[92:95] offset:13312\n\t" \
  "ds_write_b128 v160, a[108:111] offset:14336\n\t" \
  "ds_write_b128 v160, a[124:127] offset:15360\n\t" \
  "ds_write_b128 v160, a[136:139] offset:16384\n\t" \
  "ds_write_b128 v160, a[152:155] offset:17408\n\t" \
  "ds_write_b128 v160, a[168:171] offset:18432\n\t" \
  "ds_write_b128 v160, a[184:187] offset:19456\n\t" \
  "ds_write_b128 v160, a[140:143] offset:20480\n\t" \
  "ds_write_b128 v160, a[156:159] offset:21504\n\t" \
  "ds_write_b128 v160, a[172:175] offset:22528\n\t" \
  "ds_write_b128 v160, a[188:191] offset:23552\n\t" \
  "s_waitcnt lgkmcnt(0)\n\t" \
  "s_branch L_ownB1_%=\n\t" \
  "L_slow4_%=:\n\t" \
  "s_mov_b32 s66, 0\n\t" \
  "L_spin4_%=:\n\t" \
  "s_sleep 1\n\t" \
  "buffer_load_dwordx2 v[40:41], v31, %3, 0 offen offset:0 sc1\n\t" \
  "s_waitcnt vmcnt(0)\n\t" \
  "v_cmp_eq_u64 vcc, v[36:37], v[40:41]\n\t" \
  "s_add_u32 s66, s66, 1\n\t" \
  "s_nop 1\n\t" \
  "s_cmp_eq_u64 vcc, -1\n\t" \
  "s_cbranch_scc1 L_back4_%=\n\t" \
  "s_cmp_lt_u32 s66, 0x40000\n\t" \
  "s_cbranch_scc1 L_spin4_%=\n\t" \
  "s_branch L_back4_%=\n\t" \
  "L_slow5_%=:\n\t" \
  "s_mov_b32 s66, 0\n\t" \
  "L_spin5_%=:\n\t" \
  "s_sleep 1\n\t" \
  "buffer_load_dwordx2 v[38:39], v35, %3, 0 offen offset:512 sc1\n\t" \
  "s_waitcnt vmcnt(0)\n\t" \
  "v_cmp_eq_u64 vcc, v[36:37], v[38:39]\n\t" \
  "s_add_u32 s66, s66, 1\n\t" \
  "s_nop 1\n\t" \
  "s_cmp_eq_u64 vcc, -1\n\t" \
  "s_cbranch_scc1 L_back5_%=\n\t" \
  "s_cmp_lt_u32 s66, 0x40000\n\t" \
  "s_cbranch_scc1 L_spin5_%=\n\t" \
  "s_branch L_back5_%=\n\t" \
  "L_slow6_%=:\n\t" \
  "s_mov_b32 s66, 0\n\t" \
  "L_spin6_%=:\n\t" \
  "s_sleep 1\n\t" \
  "buffer_load_dwordx2 v[38:39], v35, %3, 0 offen offset:1024 sc1\n\t" \
  "s_waitcnt vmcnt(0)\n\t" \
  "v_cmp_eq_u64 vcc, v[36:37], v[38:39]\n\t" \
  "s_add_u32 s66, s66, 1\n\t" \
  "s_nop 1\n\t" \
  "s_cmp_eq_u64 vcc, -1\n\t" \
  "s_cbranch_scc1 L_back6_%=\n\t" \
  "s_cmp_lt_u32 s66, 0x40000\n\t" \
  "s_cbranch_scc1 L_spin6_%=\n\t" \
  "s_branch L_back6_%=\n\t" \
  "L_end_%=:\n\t"
#define MLP_CLOB "v16","v17","v18","v19","v20","v21","v22","v23","v24","v25","v26","v27","v28","v29","v30","v31","v32","v33","v34","v35","v36","v37","v38","v39","v40","v41","v42","v43","v44","v45","v46","v47","v48","v49","v50","v51","v52","v53","v54","v55","v56","v57","v58","v59","v60","v61","v62","v63","v64","v65","v66","v67","v68","v69","v70","v71","v72","v73","v74","v75","v76","v77","v78","v79","v80","v81","v82","v83","v84","v85","v86","v87","v88","v89","v90","v91","v92","v93","v94","v95","v96","v97","v98","v99","v100","v101","v102","v103","v104","v105","v106","v107","v108","v109","v110","v111","v112","v113","v114","v115","v116","v117","v118","v119","v120","v121","v122","v123","v124","v125","v126","v127","v128","v129","v130","v131","v132","v133","v134","v135","v136","v137","v138","v139","v140","v141","v142","v143","v144","v145","v146","v147","v148","v149","v150","v151","v152","v153","v154","v155","v156","v157","v158","v159","v160","v161","v162","v163","v164","v165","v166","v167","v168","v169","v170","v171","v172","v173","v174","v175","v176","v177","v178","v179","v180","v181","v182","v183","v184","v185","v186","v187","v188","v189","v190","v191","v192","v193","v194","v195","v196","v197","v198","v199","v200","v201","v202","v203","v204","v205","v206","v207","v208","v209","v210","v211","v212","v213","v214","v215","v216","v217","v218","v219","v220","v221","v222","v223","v224","v225","v226","v227","v228","v229","v230","v231","v232","v233","v234","v235","v236","v237","v238","v239","v240","v241","v242","v243","v244","v245","v246","v247","v248","v249","v250","v251","v252","v253","v254","v255","a0","a1","a2","a3","a4","a5","a6","a7","a8","a9","a10","a11","a12","a13","a14","a15","a16","a17","a18","a19","a20","a21","a22","a23","a24","a25","a26","a27","a28","a29","a30","a31","a32","a33","a34","a35","a36","a37","a38","a39","a40","a41","a42","a43","a44","a45","a46","a47","a48","a49","a50","a51","a52","a53","a54","a55","a56","a57","a58","a59","a60","a61","a62","a63","a64","a65","a66","a67","a68","a69","a70","a71","a72","a73","a74","a75","a76","a77","a78","a79","a80","a81","a82","a83","a84","a85","a86","a87","a88","a89","a90","a91","a92","a93","a94","a95","a96","a97","a98","a99","a100","a101","a102","a103","a104","a105","a106","a107","a108","a109","a110","a111","a112","a113","a114","a115","a116","a117","a118","a119","a120","a121","a122","a123","a124","a125","a126","a127","a128","a129","a130","a131","a132","a133","a134","a135","a136","a137","a138","a139","a140","a141","a142","a143","a144","a145","a146","a147","a148","a149","a150","a151","a152","a153","a154","a155","a156","a157","a158","a159","a160","a161","a162","a163","a164","a165","a166","a167","a168","a169","a170","a171","a172","a173","a174","a175","a176","a177","a178","a179","a180","a181","a182","a183","a184","a185","a186","a187","a188","a189","a190","a191","a192","a193","a194","a195","a196","a197","a198","a199","a200","a201","a202","a203","a204","a205","a206","a207","a208","a209","a210","a211","a212","a213","a214","a215","a216","a217","a218","a219","a220","a221","a222","a223","a224","a225","a226","a227","a228","a229","a230","a231","a232","a233","a234","a235","a236","a237","a238","a239","a240","a241","a242","a243","a244","a245","a246","a247","a248","a249","a250","a251","a252","a253","a254","a255","s40","s41","s42","s43","s44","s45","s46","s47","s48","s49","s50","s51","s52","s53","s54","s55","s56","s57","s58","s59","s60","s61","s62","s63","s64","s65","s66","s67","s68","s69","s70","s71","s72","s73","s74","s75","s76","s77","s78","s79","s80","s81","s82","s83","s84","s85","s86","s87","s88","s89","s90","s91","s92","s93","s94","s95","s96","s97","s98","s99","vcc","m0","scc","memory"
#define SSM_ASM \
  "v_mbcnt_lo_u32_b32 v16, -1, 0\n\t" \
  "v_mbcnt_hi_u32_b32 v16, -1, v16\n\t" \
  "s_lshl_b32 s40, %12, 3\n\t" \
  "s_add_u32 s40, s40, %11\n\t" \
  "s_lshr_b32 s41, %13, 1\n\t" \
  "s_and_b32 s42, %13, 1\n\t" \
  "s_lshl_b32 s43, %13, 6\n\t" \
  "v_add_u32 v19, s43, v16\n\t" \
  "v_and_b32 v31, 127, v19\n\t" \
  "v_lshlrev_b32 v31, 1, v31\n\t" \
  "v_mov_b32 v35, 0\n\t" \
  "v_mov_b32 v36, 0\n\t" \
  "v_mov_b32 v37, 0\n\t" \
  "v_mov_b32 v38, 0\n\t" \
  "v_mov_b32 v39, 0\n\t" \
  "v_mov_b32 v40, 0\n\t" \
  "v_mov_b32 v41, 0\n\t" \
  "s_mul_i32 s48, s40, 9\n\t" \
  "s_add_u32 s48, s48, s41\n\t" \
  "s_lshl_b32 s43, s48, 8\n\t" \
  "v_add_u32 v54, s43, v31\n\t" \
  "s_lshl_b32 s43, s48, 1\n\t" \
  "v_mov_b32 v55, s43\n\t" \
  "global_load_short_d16_hi v35, v54, %4\n\t" \
  "global_load_short_d16_hi v36, v54, %5\n\t" \
  "global_load_short_d16_hi v37, v54, %6\n\t" \
  "global_load_short_d16_hi v38, v54, %7\n\t" \
  "global_load_short_d16_hi v39, v54, %8\n\t" \
  "global_load_short_d16_hi v40, v54, %9\n\t" \
  "global_load_short_d16_hi v41, v55, %10\n\t" \
  "s_getpc_b64 s[64:65]\n\t" \
  "L_spc_%=:\n\t" \
  "v_lshlrev_b32 v56, 7, v19\n\t" \
  "v_min_u32 v56, L_send_%=-L_spc_%=-4, v56\n\t" \
  "global_load_dword v63, v56, s[64:65]\n\t" \
  "buffer_load_dwordx2 v[32:33], off, %3, 0 offset:0\n\t" \
  "s_mul_i32 s44, %13, 9\n\t" \
  "s_add_u32 s44, s44, 1\n\t" \
  "s_lshr_b32 s44, s44, 1\n\t" \
  "v_and_b32 v17, 15, v16\n\t" \
  "v_lshrrev_b32 v18, 4, v16\n\t" \
  "v_and_b32 v52, 12, v17\n\t" \
  "v_add_u32 v52, v52, v17\n\t" \
  "v_mul_u32_u24 v52, 1152, v52\n\t" \
  "v_lshl_add_u32 v52, v18, 4, v52\n\t" \
  "s_mul_i32 s60, s40, 36864\n\t" \
  "s_lshl_b32 s45, s44, 6\n\t" \
  "s_add_u32 s60, s60, s45\n\t" \
  "v_add_u32 v52, s60, v52\n\t" \
  "v_add_u32 v53, 4608, v52\n\t" \
  "buffer_load_dwordx4 a[0:3], v52, %0, 0 offen offset:0\n\t" \
  "buffer_load_dwordx4 a[4:7], v53, %0, 0 offen offset:0\n\t" \
  "v_mov_b32 v220, 0\n\t" \
  "v_mov_b32 v221, 0\n\t" \
  "v_mov_b32 v222, 0\n\t" \
  "v_mov_b32 v223, 0\n\t" \
  "v_mov_b32 v224, 0\n\t" \
  "v_mov_b32 v225, 0\n\t" \
  "v_mov_b32 v226, 0\n\t" \
  "v_mov_b32 v227, 0\n\t" \
  "v_mov_b32 v228, 0\n\t" \
  "v_mov_b32 v229, 0\n\t" \
  "v_mov_b32 v230, 0\n\t" \
  "v_mov_b32 v231, 0\n\t" \
  "v_mov_b32 v232, 0\n\t" \
  "v_mov_b32 v233, 0\n\t" \
  "v_mov_b32 v42, 0x3f80\n\t" \
  "v_mov_b32 v43, 0x3f800000\n\t" \
  "v_mov_b32 v58, 0x3f803f80\n\t" \
  "v_and_b32 v54, 1, v17\n\t" \
  "v_lshlrev_b32 v54, 4, v54\n\t" \
  "v_lshlrev_b32 v54, v54, v42\n\t" \
  "v_lshlrev_b32 v55, 2, v18\n\t" \
  "v_lshrrev_b32 v57, 1, v17\n\t" \
  "v_sub_u32 v55, v57, v55\n\t" \
  "v_cmp_eq_u32 vcc, 0, v55\n\t" \
  "v_cmp_eq_u32 s[52:53], -8, v55\n\t" \
  "v_accvgpr_write_b32 a48, v58\n\t" \
  "v_cndmask_b32 v59, 0, v54, vcc\n\t" \
  "v_cndmask_b32 v60, 0, v54, s[52:53]\n\t" \
  "v_accvgpr_write_b32 a40, v59\n\t" \
  "v_accvgpr_write_b32 a44, v60\n\t" \
  "v_cmp_eq_u32 vcc, 1, v55\n\t" \
  "v_cmp_eq_u32 s[52:53], -7, v55\n\t" \
  "v_accvgpr_write_b32 a49, v58\n\t" \
  "v_cndmask_b32 v59, 0, v54, vcc\n\t" \
  "v_cndmask_b32 v60, 0, v54, s[52:53]\n\t" \
  "v_accvgpr_write_b32 a41, v59\n\t" \
  "v_accvgpr_write_b32 a45, v60\n\t" \
  "v_cmp_eq_u32 vcc, 2, v55\n\t" \
  "v_cmp_eq_u32 s[52:53], -6, v55\n\t" \
  "v_accvgpr_write_b32 a50, v58\n\t" \
  "v_cndmask_b32 v59, 0, v54, vcc\n\t" \
  "v_cndmask_b32 v60, 0, v54, s[52:53]\n\t" \
  "v_accvgpr_write_b32 a42, v59\n\t" \
  "v_accvgpr_write_b32 a46, v60\n\t" \
  "v_cmp_eq_u32 vcc, 3, v55\n\t" \
  "v_cmp_eq_u32 s[52:53], -5, v55\n\t" \
  "v_accvgpr_write_b32 a51, v58\n\t" \
  "v_cndmask_b32 v59, 0, v54, vcc\n\t" \
  "v_cndmask_b32 v60, 0, v54, s[52:53]\n\t" \
  "v_accvgpr_write_b32 a43, v59\n\t" \
  "v_accvgpr_write_b32 a47, v60\n\t" \
  "buffer_load_dwordx4 a[8:11], v52, %0, 0 offen offset:64\n\t" \
  "v_lshlrev_b32 v54, 3, v19\n\t" \
  "s_mov_b32 s63, 0x0E0C0800\n\t" \
  "v_add_u32 v20, -1, v54\n\t" \
  "v_max_i32 v20, 0, v20\n\t" \
  "v_add_u32 v21, 0, v54\n\t" \
  "v_add_u32 v22, 1, v54\n\t" \
  "v_add_u32 v23, 2, v54\n\t" \
  "v_add_u32 v24, 3, v54\n\t" \
  "v_add_u32 v25, 4, v54\n\t" \
  "v_add_u32 v26, 5, v54\n\t" \
  "v_add_u32 v27, 6, v54\n\t" \
  "v_add_u32 v28, 7, v54\n\t" \
  "v_cvt_f32_u32 v20, v20\n\t" \
  "v_cvt_f32_u32 v21, v21\n\t" \
  "v_cvt_f32_u32 v22, v22\n\t" \
  "v_cvt_f32_u32 v23, v23\n\t" \
  "v_cvt_f32_u32 v24, v24\n\t" \
  "v_cvt_f32_u32 v25, v25\n\t" \
  "v_cvt_f32_u32 v26, v26\n\t" \
  "v_cvt_f32_u32 v27, v27\n\t" \
  "v_cvt_f32_u32 v28, v28\n\t" \
  "v_cvt_pk_bf16_f32 v20, v20, v20\n\t" \
  "v_cvt_pk_bf16_f32 v21, v21, v21\n\t" \
  "v_cvt_pk_bf16_f32 v22, v22, v22\n\t" \
  "v_cvt_pk_bf16_f32 v23, v23, v23\n\t" \
  "v_cvt_pk_bf16_f32 v24, v24, v24\n\t" \
  "v_cvt_pk_bf16_f32 v25, v25, v25\n\t" \
  "v_cvt_pk_bf16_f32 v26, v26, v26\n\t" \
  "v_cvt_pk_bf16_f32 v27, v27, v27\n\t" \
  "v_cvt_pk_bf16_f32 v28, v28, v28\n\t" \
  "buffer_load_dwordx4 a[12:15], v53, %0, 0 offen offset:64\n\t" \
  "v_and_b32 v20, 0xffff0000, v20\n\t" \
  "v_and_b32 v21, 0xffff0000, v21\n\t" \
  "v_and_b32 v22, 0xffff0000, v22\n\t" \
  "v_and_b32 v23, 0xffff0000, v23\n\t" \
  "v_and_b32 v24, 0xffff0000, v24\n\t" \
  "v_and_b32 v25, 0xffff0000, v25\n\t" \
  "v_and_b32 v26, 0xffff0000, v26\n\t" \
  "v_and_b32 v27, 0xffff0000, v27\n\t" \
  "v_and_b32 v28, 0xffff0000, v28\n\t" \
  "v_cvt_u32_f32 v20, v20\n\t" \
  "v_cvt_u32_f32 v21, v21\n\t" \
  "v_cvt_u32_f32 v22, v22\n\t" \
  "v_cvt_u32_f32 v23, v23\n\t" \
  "v_cvt_u32_f32 v24, v24\n\t" \
  "v_cvt_u32_f32 v25, v25\n\t" \
  "v_cvt_u32_f32 v26, v26\n\t" \
  "v_cvt_u32_f32 v27, v27\n\t" \
  "v_cvt_u32_f32 v28, v28\n\t" \
  "v_lshrrev_b32 v54, 8, v20\n\t" \
  "v_lshlrev_b32 v55, 3, v54\n\t" \
  "v_bfe_u32 v55, s63, v55, 8\n\t" \
  "v_add_u32 v57, 12, v54\n\t" \
  "v_cmp_gt_u32 vcc, 4, v54\n\t" \
  "v_min_u32 v54, 2, v54\n\t" \
  "v_and_b32 v58, 7, v20\n\t" \
  "v_cndmask_b32 v55, v57, v55, vcc\n\t" \
  "v_lshrrev_b32 v58, v54, v58\n\t" \
  "v_bfe_u32 v20, v20, 3, 5\n\t" \
  "v_add_u32 v55, v55, v58\n\t" \
  "v_lshrrev_b32 v54, 4, v20\n\t" \
  "v_and_b32 v20, 15, v20\n\t" \
  "v_lshlrev_b32 v20, 4, v20\n\t" \
  "v_lshl_add_u32 v20, v54, 10, v20\n\t" \
  "v_bfe_u32 v54, v55, 2, 2\n\t" \
  "v_lshl_add_u32 v20, v54, 8, v20\n\t" \
  "v_lshrrev_b32 v54, 4, v55\n\t" \
  "v_lshl_add_u32 v20, v54, 3, v20\n\t" \
  "v_and_b32 v54, 3, v55\n\t" \
  "v_lshl_add_u32 v20, v54, 1, v20\n\t" \
  "v_add_u32 v20, 139872, v20\n\t" \
  "v_lshrrev_b32 v54, 8, v21\n\t" \
  "v_lshlrev_b32 v55, 3, v54\n\t" \
  "v_bfe_u32 v55, s63, v55, 8\n\t" \
  "v_add_u32 v57, 12, v54\n\t" \
  "v_cmp_gt_u32 vcc, 4, v54\n\t" \
  "v_min_u32 v54, 2, v54\n\t" \
  "v_and_b32 v58, 7, v21\n\t" \
  "v_cndmask_b32 v55, v57, v55, vcc\n\t" \
  "v_lshrrev_b32 v58, v54, v58\n\t" \
  "v_bfe_u32 v21, v21, 3, 5\n\t" \
  "v_add_u32 v55, v55, v58\n\t" \
  "v_lshrrev_b32 v54, 4, v21\n\t" \
  "v_and_b32 v21, 15, v21\n\t" \
  "v_lshlrev_b32 v21, 4, v21\n\t" \
  "v_lshl_add_u32 v21, v54, 10, v21\n\t" \
  "v_bfe_u32 v54, v55, 2, 2\n\t" \
  "v_lshl_add_u32 v21, v54, 8, v21\n\t" \
  "v_lshrrev_b32 v54, 4, v55\n\t" \
  "v_lshl_add_u32 v21, v54, 3, v21\n\t" \
  "v_and_b32 v54, 3, v55\n\t" \
  "v_lshl_add_u32 v21, v54, 1, v21\n\t" \
  "v_add_u32 v21, 139872, v21\n\t" \
  "v_lshrrev_b32 v54, 8, v22\n\t" \
  "v_lshlrev_b32 v55, 3, v54\n\t" \
  "v_bfe_u32 v55, s63, v55, 8\n\t" \
  "v_add_u32 v57, 12, v54\n\t" \
  "v_cmp_gt_u32 vcc, 4, v54\n\t" \
  "v_min_u32 v54, 2, v54\n\t" \
  "v_and_b32 v58, 7, v22\n\t" \
  "v_cndmask_b32 v55, v57, v55, vcc\n\t" \
  "v_lshrrev_b32 v58, v54, v58\n\t" \
  "v_bfe_u32 v22, v22, 3, 5\n\t" \
  "v_add_u32 v55, v55, v58\n\t" \
  "buffer_load_dwordx4 a[16:19], v52, %0, 0 offen offset:128\n\t" \
  "v_lshrrev_b32 v54, 4, v22\n\t" \
  "v_and_b32 v22, 15, v22\n\t" \
  "v_lshlrev_b32 v22, 4, v22\n\t" \
  "v_lshl_add_u32 v22, v54, 10, v22\n\t" \
  "v_bfe_u32 v54, v55, 2, 2\n\t" \
  "v_lshl_add_u32 v22, v54, 8, v22\n\t" \
  "v_lshrrev_b32 v54, 4, v55\n\t" \
  "v_lshl_add_u32 v22, v54, 3, v22\n\t" \
  "v_and_b32 v54, 3, v55\n\t" \
  "v_lshl_add_u32 v22, v54, 1, v22\n\t" \
  "v_add_u32 v22, 139872, v22\n\t" \
  "v_lshrrev_b32 v54, 8, v23\n\t" \
  "v_lshlrev_b32 v55, 3, v54\n\t" \
  "v_bfe_u32 v55, s63, v55, 8\n\t" \
  "v_add_u32 v57, 12, v54\n\t" \
  "v_cmp_gt_u32 vcc, 4, v54\n\t" \
  "v_min_u32 v54, 2, v54\n\t" \
  "v_and_b32 v58, 7, v23\n\t" \
  "v_cndmask_b32 v55, v57, v55, vcc\n\t" \
  "v_lshrrev_b32 v58, v54, v58\n\t" \
  "v_bfe_u32 v23, v23, 3, 5\n\t" \
  "v_add_u32 v55, v55, v58\n\t" \
  "v_lshrrev_b32 v54, 4, v23\n\t" \
  "v_and_b32 v23, 15, v23\n\t" \
  "v_lshlrev_b32 v23, 4, v23\n\t" \
  "v_lshl_add_u32 v23, v54, 10, v23\n\t" \
  "v_bfe_u32 v54, v55, 2, 2\n\t" \
  "v_lshl_add_u32 v23, v54, 8, v23\n\t" \
  "v_lshrrev_b32 v54, 4, v55\n\t" \
  "v_lshl_add_u32 v23, v54, 3, v23\n\t" \
  "v_and_b32 v54, 3, v55\n\t" \
  "v_lshl_add_u32 v23, v54, 1, v23\n\t" \
  "v_add_u32 v23, 139872, v23\n\t" \
  "v_lshrrev_b32 v54, 8, v24\n\t" \
  "v_lshlrev_b32 v55, 3, v54\n\t" \
  "v_bfe_u32 v55, s63, v55, 8\n\t" \
  "v_add_u32 v57, 12, v54\n\t" \
  "v_cmp_gt_u32 vcc, 4, v54\n\t" \
  "v_min_u32 v54, 2, v54\n\t" \
  "v_and_b32 v58, 7, v24\n\t" \
  "v_cndmask_b32 v55, v57, v55, vcc\n\t" \
  "v_lshrrev_b32 v58, v54, v58\n\t" \
  "v_bfe_u32 v24, v24, 3, 5\n\t" \
  "v_add_u32 v55, v55, v58\n\t" \
  "v_lshrrev_b32 v54, 4, v24\n\t" \
  "v_and_b32 v24, 15, v24\n\t" \
  "v_lshlrev_b32 v24, 4, v24\n\t" \
  "v_lshl_add_u32 v24, v54, 10, v24\n\t" \
  "v_bfe_u32 v54, v55, 2, 2\n\t" \
  "v_lshl_add_u32 v24, v54, 8, v24\n\t" \
  "v_lshrrev_b32 v54, 4, v55\n\t" \
  "v_lshl_add_u32 v24, v54, 3, v24\n\t" \
  "v_and_b32 v54, 3, v55\n\t" \
  "v_lshl_add_u32 v24, v54, 1, v24\n\t" \
  "v_add_u32 v24, 139872, v24\n\t" \
  "v_lshrrev_b32 v54, 8, v25\n\t" \
  "v_lshlrev_b32 v55, 3, v54\n\t" \
  "v_bfe_u32 v55, s63, v55, 8\n\t" \
  "v_add_u32 v57, 12, v54\n\t" \
  "v_cmp_gt_u32 vcc, 4, v54\n\t" \
  "v_min_u32 v54, 2, v54\n\t" \
  "v_and_b32 v58, 7, v25\n\t" \
  "v_cndmask_b32 v55, v57, v55, vcc\n\t" \
  "v_lshrrev_b32 v58, v54, v58\n\t" \
  "v_bfe_u32 v25, v25, 3, 5\n\t" \
  "v_add_u32 v55, v55, v58\n\t" \
  "v_lshrrev_b32 v54, 4, v25\n\t" \
  "v_and_b32 v25, 15, v25\n\t" \
  "v_lshlrev_b32 v25, 4, v25\n\t" \
  "v_lshl_add_u32 v25, v54, 10, v25\n\t" \
  "v_bfe_u32 v54, v55, 2, 2\n\t" \
  "v_lshl_add_u32 v25, v54, 8, v25\n\t" \
  "v_lshrrev_b32 v54, 4, v55\n\t" \
  "v_lshl_add_u32 v25, v54, 3, v25\n\t" \
  "v_and_b32 v54, 3, v55\n\t" \
  "v_lshl_add_u32 v25, v54, 1, v25\n\t" \
  "v_add_u32 v25, 139872, v25\n\t" \
  "v_lshrrev_b32 v54, 8, v26\n\t" \
  "v_lshlrev_b32 v55, 3, v54\n\t" \
  "v_bfe_u32 v55, s63, v55, 8\n\t" \
  "v_add_u32 v57, 12, v54\n\t" \
  "v_cmp_gt_u32 vcc, 4, v54\n\t" \
  "v_min_u32 v54, 2, v54\n\t" \
  "v_and_b32 v58, 7, v26\n\t" \
  "v_cndmask_b32 v55, v57, v55, vcc\n\t" \
  "v_lshrrev_b32 v58, v54, v58\n\t" \
  "v_bfe_u32 v26, v26, 3, 5\n\t" \
  "v_add_u32 v55, v55, v58\n\t" \
  "buffer_load_dwordx4 a[20:23], v53, %0, 0 offen offset:128\n\t" \
  "v_lshrrev_b32 v54, 4, v26\n\t" \
  "v_and_b32 v26, 15, v26\n\t" \
  "v_lshlrev_b32 v26, 4, v26\n\t" \
  "v_lshl_add_u32 v26, v54, 10, v26\n\t" \
  "v_bfe_u32 v54, v55, 2, 2\n\t" \
  "v_lshl_add_u32 v26, v54, 8, v26\n\t" \
  "v_lshrrev_b32 v54, 4, v55\n\t" \
  "v_lshl_add_u32 v26, v54, 3, v26\n\t" \
  "v_and_b32 v54, 3, v55\n\t" \
  "v_lshl_add_u32 v26, v54, 1, v26\n\t" \
  "v_add_u32 v26, 139872, v26\n\t" \
  "v_lshrrev_b32 v54, 8, v27\n\t" \
  "v_lshlrev_b32 v55, 3, v54\n\t" \
  "v_bfe_u32 v55, s63, v55, 8\n\t" \
  "v_add_u32 v57, 12, v54\n\t" \
  "v_cmp_gt_u32 vcc, 4, v54\n\t" \
  "v_min_u32 v54, 2, v54\n\t" \
  "v_and_b32 v58, 7, v27\n\t" \
  "v_cndmask_b32 v55, v57, v55, vcc\n\t" \
  "v_lshrrev_b32 v58, v54, v58\n\t" \
  "v_bfe_u32 v27, v27, 3, 5\n\t" \
  "v_add_u32 v55, v55, v58\n\t" \
  "v_lshrrev_b32 v54, 4, v27\n\t" \
  "v_and_b32 v27, 15, v27\n\t" \
  "v_lshlrev_b32 v27, 4, v27\n\t" \
  "v_lshl_add_u32 v27, v54, 10, v27\n\t" \
  "v_bfe_u32 v54, v55, 2, 2\n\t" \
  "v_lshl_add_u32 v27, v54, 8, v27\n\t" \
  "v_lshrrev_b32 v54, 4, v55\n\t" \
  "v_lshl_add_u32 v27, v54, 3, v27\n\t" \
  "v_and_b32 v54, 3, v55\n\t" \
  "v_lshl_add_u32 v27, v54, 1, v27\n\t" \
  "v_add_u32 v27, 139872, v27\n\t" \
  "v_lshrrev_b32 v54, 8, v28\n\t" \
  "v_lshlrev_b32 v55, 3, v54\n\t" \
  "v_bfe_u32 v55, s63, v55, 8\n\t" \
  "v_add_u32 v57, 12, v54\n\t" \
  "v_cmp_gt_u32 vcc, 4, v54\n\t" \
  "v_min_u32 v54, 2, v54\n\t" \
  "v_and_b32 v58, 7, v28\n\t" \
  "v_cndmask_b32 v55, v57, v55, vcc\n\t" \
  "v_lshrrev_b32 v58, v54, v58\n\t" \
  "v_bfe_u32 v28, v28, 3, 5\n\t" \
  "v_add_u32 v55, v55, v58\n\t" \
  "v_lshrrev_b32 v54, 4, v28\n\t" \
  "v_and_b32 v28, 15, v28\n\t" \
  "v_lshlrev_b32 v28, 4, v28\n\t" \
  "v_lshl_add_u32 v28, v54, 10, v28\n\t" \
  "v_bfe_u32 v54, v55, 2, 2\n\t" \
  "v_lshl_add_u32 v28, v54, 8, v28\n\t" \
  "v_lshrrev_b32 v54, 4, v55\n\t" \
  "v_lshl_add_u32 v28, v54, 3, v28\n\t" \
  "v_and_b32 v54, 3, v55\n\t" \
  "v_lshl_add_u32 v28, v54, 1, v28\n\t" \
  "v_add_u32 v28, 139872, v28\n\t" \
  "s_mov_b32 s63, 0x3fb8aa3b\n\t" \
  "s_mul_i32 s47, s41, 35840\n\t" \
  "s_lshl_b32 s43, s42, 8\n\t" \
  "s_add_u32 s47, s47, s43\n\t" \
  "s_add_u32 s47, s47, 76032\n\t" \
  "s_waitcnt vmcnt(8)\n\t" \
  "v_mul_f32 v194, 0x3fb8aa3b, v41\n\t" \
  "v_rndne_f32 v195, v194\n\t" \
  "v_fma_f32 v196, v41, s63, -v194\n\t" \
  "v_sub_f32 v194, v194, v195\n\t" \
  "v_fmac_f32 v196, 0x32a5705f, v41\n\t" \
  "v_cvt_i32_f32 v195, v195\n\t" \
  "v_add_f32 v194, v194, v196\n\t" \
  "v_exp_f32 v194, v194\n\t" \
  "s_nop 0\n\t" \
  "v_ldexp_f32 v194, v194, v195\n\t" \
  "v_cvt_pk_bf16_f32 v194, v194, v194\n\t" \
  "v_and_b32 v194, 0xffff0000, v194\n\t" \
  "v_mul_f32 v48, v194, v35\n\t" \
  "v_mul_f32 v49, v194, v36\n\t" \
  "v_mul_f32 v195, v194, v37\n\t" \
  "v_mul_f32 v196, v194, v38\n\t" \
  "v_mul_f32 v48, 0x3fb8aa3b, v48\n\t" \
  "buffer_load_dwordx4 a[24:27], v52, %0, 0 offen offset:192\n\t" \
  "v_mul_f32 v49, 0.15915494, v49\n\t" \
  "v_mul_f32 v50, v39, v195\n\t" \
  "v_mul_f32 v51, v39, v196\n\t" \
  "v_fma_f32 v50, -v40, v196, v50\n\t" \
  "v_fmac_f32 v51, v40, v195\n\t" \
  "v_exp_f32 v194, v48\n\t" \
  "v_fract_f32 v198, v49\n\t" \
  "v_mul_f32 v195, 0x41000000, v48\n\t" \
  "v_mul_f32 v199, 0xc1000000, v49\n\t" \
  "v_mul_f32 v196, 0x42000000, v48\n\t" \
  "v_mul_f32 v200, 0xc2000000, v49\n\t" \
  "v_mul_f32 v197, 0x43800000, v48\n\t" \
  "v_mul_f32 v201, 0x43800000, v49\n\t" \
  "v_fract_f32 v199, v199\n\t" \
  "v_fract_f32 v200, v200\n\t" \
  "v_fract_f32 v201, v201\n\t" \
  "v_exp_f32 v195, v195\n\t" \
  "v_exp_f32 v196, v196\n\t" \
  "v_exp_f32 v197, v197\n\t" \
  "v_cos_f32 v178, v198\n\t" \
  "v_sin_f32 v179, v198\n\t" \
  "v_cos_f32 v180, v199\n\t" \
  "v_sin_f32 v181, v199\n\t" \
  "v_cos_f32 v182, v200\n\t" \
  "buffer_load_dwordx4 a[28:31], v53, %0, 0 offen offset:192\n\t" \
  "v_sin_f32 v183, v200\n\t" \
  "v_cos_f32 v184, v201\n\t" \
  "v_sin_f32 v185, v201\n\t" \
  "s_nop 0\n\t" \
  "v_mul_f32 v178, v178, v194\n\t" \
  "v_mul_f32 v179, v179, v194\n\t" \
  "v_mul_f32 v180, v180, v195\n\t" \
  "v_mul_f32 v181, v181, v195\n\t" \
  "v_mul_f32 v182, v182, v196\n\t" \
  "v_mul_f32 v183, v183, v196\n\t" \
  "v_mul_f32 v184, v184, v197\n\t" \
  "v_mul_f32 v185, v185, v197\n\t" \
  "v_xor_b32 v186, 0x80000000, v179\n\t" \
  "v_mov_b32 v187, v178\n\t" \
  "v_xor_b32 v188, 0x80000000, v181\n\t" \
  "v_mov_b32 v189, v180\n\t" \
  "v_xor_b32 v190, 0x80000000, v183\n\t" \
  "v_mov_b32 v191, v182\n\t" \
  "v_xor_b32 v192, 0x80000000, v185\n\t" \
  "v_mov_b32 v193, v184\n\t" \
  "s_mov_b32 m0, s47\n\t" \
  "v_mov_b32 v122, 0x3f80\n\t" \
  "v_cvt_pk_bf16_f32 v123, v50, v51\n\t" \
  "v_cvt_pk_bf16_f32 v124, v180, v181\n\t" \
  "v_pk_mul_f32 v[106:107], v[180:181], v[180:181] op_sel:[0,0] op_sel_hi:[1,0]\n\t" \
  "ds_write_addtid_b32 v122 offset:11088\n\t" \
  "v_pk_mul_f32 v[108:109], v[178:179], v[50:51] op_sel:[0,0] op_sel_hi:[1,0]\n\t" \
  "v_pk_mul_f32 v[110:111], v[184:185], v[50:51] op_sel:[0,0] op_sel_hi:[1,0]\n\t" \
  "ds_write_addtid_b32 v123 offset:0\n\t" \
  "v_pk_mul_f32 v[112:113], v[182:183], v[180:181] op_sel:[0,0] op_sel_hi:[1,0]\n\t" \
  "v_pk_fma_f32 v[92:93], v[188:189], v[180:181], v[106:107] op_sel:[0,1,0] op_sel_hi:[1,1,1]\n\t" \
  "v_pk_fma_f32 v[96:97], v[186:187], v[50:51], v[108:109] op_sel:[0,1,0] op_sel_hi:[1,1,1]\n\t" \
  "ds_write_addtid_b32 v124 offset:11616\n\t" \
  "v_pk_fma_f32 v[98:99], v[192:193], v[50:51], v[110:111] op_sel:[0,1,0] op_sel_hi:[1,1,1]\n\t" \
  "v_pk_fma_f32 v[90:91], v[190:191], v[180:181], v[112:113] op_sel:[0,1,0] op_sel_hi:[1,1,1]\n\t" \
  "buffer_load_dwordx4 a[32:35], v52, %0, 0 offen offset:256\n\t" \
  "v_cvt_pk_bf16_f32 v125, v92, v93\n\t" \
  "v_cvt_pk_bf16_f32 v126, v96, v97\n\t" \
  "v_cvt_pk_bf16_f32 v127, v98, v99\n\t" \
  "v_cvt_pk_bf16_f32 v128, v182, v183\n\t" \
  "v_cvt_pk_bf16_f32 v129, v90, v91\n\t" \
  "v_pk_mul_f32 v[106:107], v[180:181], v[92:93] op_sel:[0,0] op_sel_hi:[1,0]\n\t" \
  "ds_write_addtid_b32 v125 offset:12144\n\t" \
  "v_pk_mul_f32 v[108:109], v[178:179], v[96:97] op_sel:[0,0] op_sel_hi:[1,0]\n\t" \
  "ds_write_addtid_b32 v126 offset:528\n\t" \
  "v_pk_mul_f32 v[110:111], v[184:185], v[98:99] op_sel:[0,0] op_sel_hi:[1,0]\n\t" \
  "v_pk_mul_f32 v[112:113], v[182:183], v[92:93] op_sel:[0,0] op_sel_hi:[1,0]\n\t" \
  "ds_write_addtid_b32 v127 offset:4224\n\t" \
  "v_pk_fma_f32 v[94:95], v[188:189], v[92:93], v[106:107] op_sel:[0,1,0] op_sel_hi:[1,1,1]\n\t" \
  "ds_write_addtid_b32 v128 offset:13200\n\t" \
  "v_pk_fma_f32 v[100:101], v[186:187], v[96:97], v[108:109] op_sel:[0,1,0] op_sel_hi:[1,1,1]\n\t" \
  "v_pk_fma_f32 v[98:99], v[192:193], v[98:99], v[110:111] op_sel:[0,1,0] op_sel_hi:[1,1,1]\n\t" \
  "ds_write_addtid_b32 v129 offset:13728\n\t" \
  "v_pk_fma_f32 v[92:93], v[190:191], v[92:93], v[112:113] op_sel:[0,1,0] op_sel_hi:[1,1,1]\n\t" \
  "buffer_load_dwordx4 a[36:39], v53, %0, 0 offen offset:256\n\t" \
  "v_cvt_pk_bf16_f32 v122, v94, v95\n\t" \
  "v_cvt_pk_bf16_f32 v123, v100, v101\n\t" \
  "v_cvt_pk_bf16_f32 v124, v98, v99\n\t" \
  "v_cvt_pk_bf16_f32 v125, v92, v93\n\t" \
  "v_pk_mul_f32 v[106:107], v[182:183], v[94:95] op_sel:[0,0] op_sel_hi:[1,0]\n\t" \
  "ds_write_addtid_b32 v122 offset:12672\n\t" \
  "v_pk_mul_f32 v[108:109], v[182:183], v[182:183] op_sel:[0,0] op_sel_hi:[1,0]\n\t" \
  "v_pk_mul_f32 v[110:111], v[182:183], v[90:91] op_sel:[0,0] op_sel_hi:[1,0]\n\t" \
  "v_pk_mul_f32 v[112:113], v[182:183], v[92:93] op_sel:[0,0] op_sel_hi:[1,0]\n\t" \
  "ds_write_addtid_b32 v123 offset:1056\n\t" \
  "v_pk_mul_f32 v[114:115], v[184:185], v[98:99] op_sel:[0,0] op_sel_hi:[1,0]\n\t" \
  "v_pk_mul_f32 v[116:117], v[178:179], v[100:101] op_sel:[0,0] op_sel_hi:[1,0]\n\t" \
  "v_pk_fma_f32 v[94:95], v[190:191], v[94:95], v[106:107] op_sel:[0,1,0] op_sel_hi:[1,1,1]\n\t" \
  "ds_write_addtid_b32 v124 offset:6336\n\t" \
  "v_pk_fma_f32 v[88:89], v[190:191], v[182:183], v[108:109] op_sel:[0,1,0] op_sel_hi:[1,1,1]\n\t" \
  "v_pk_fma_f32 v[90:91], v[190:191], v[90:91], v[110:111] op_sel:[0,1,0] op_sel_hi:[1,1,1]\n\t" \
  "v_pk_fma_f32 v[92:93], v[190:191], v[92:93], v[112:113] op_sel:[0,1,0] op_sel_hi:[1,1,1]\n\t" \
  "ds_write_addtid_b32 v125 offset:14256\n\t" \
  "v_pk_fma_f32 v[98:99], v[192:193], v[98:99], v[114:115] op_sel:[0,1,0] op_sel_hi:[1,1,1]\n\t" \
  "v_pk_fma_f32 v[96:97], v[186:187], v[100:101], v[116:117] op_sel:[0,1,0] op_sel_hi:[1,1,1]\n\t" \
  "s_add_u32 s48, s48, 2\n\t" \
  "s_lshl_b32 s43, s48, 8\n\t" \
  "v_add_u32 v54, s43, v31\n\t" \
  "s_lshl_b32 s43, s48, 1\n\t" \
  "v_mov_b32 v55, s43\n\t" \
  "global_load_short_d16_hi v220, v54, %4\n\t" \
  "global_load_short_d16_hi v221, v54, %5\n\t" \
  "global_load_short_d16_hi v222, v54, %6\n\t" \
  "global_load_short_d16_hi v223, v54, %7\n\t" \
  "global_load_short_d16_hi v224, v54, %8\n\t" \
  "global_load_short_d16_hi v225, v54, %9\n\t" \
  "global_load_short_d16_hi v226, v55, %10\n\t" \
  "v_cvt_pk_bf16_f32 v126, v94, v95\n\t" \
  "v_cvt_pk_bf16_f32 v127, v88, v89\n\t" \
  "v_cvt_pk_bf16_f32 v128, v90, v91\n\t" \
  "v_cvt_pk_bf16_f32 v129, v92, v93\n\t" \
  "v_cvt_pk_bf16_f32 v122, v98, v99\n\t" \
  "v_cvt_pk_bf16_f32 v123, v96, v97\n\t" \
  "v_pk_mul_f32 v[106:107], v[182:183], v[94:95] op_sel:[0,0] op_sel_hi:[1,0]\n\t" \
  "ds_write_addtid_b32 v126 offset:14784\n\t" \
  "v_pk_mul_f32 v[108:109], v[182:183], v[88:89] op_sel:[0,0] op_sel_hi:[1,0]\n\t" \
  "v_pk_mul_f32 v[110:111], v[182:183], v[90:91] op_sel:[0,0] op_sel_hi:[1,0]\n\t" \
  "ds_write_addtid_b32 v127 offset:15312\n\t" \
  "v_pk_mul_f32 v[112:113], v[182:183], v[92:93] op_sel:[0,0] op_sel_hi:[1,0]\n\t" \
  "v_pk_mul_f32 v[114:115], v[184:185], v[98:99] op_sel:[0,0] op_sel_hi:[1,0]\n\t" \
  "ds_write_addtid_b32 v128 offset:15840\n\t" \
  "v_pk_mul_f32 v[116:117], v[178:179], v[96:97] op_sel:[0,0] op_sel_hi:[1,0]\n\t" \
  "v_pk_mul_f32 v[118:119], v[184:185], v[100:101] op_sel:[0,0] op_sel_hi:[1,0]\n\t" \
  "v_pk_fma_f32 v[94:95], v[190:191], v[94:95], v[106:107] op_sel:[0,1,0] op_sel_hi:[1,1,1]\n\t" \
  "ds_write_addtid_b32 v129 offset:16368\n\t" \
  "v_pk_fma_f32 v[88:89], v[190:191], v[88:89], v[108:109] op_sel:[0,1,0] op_sel_hi:[1,1,1]\n\t" \
  "v_pk_fma_f32 v[90:91], v[190:191], v[90:91], v[110:111] op_sel:[0,1,0] op_sel_hi:[1,1,1]\n\t" \
  "ds_write_addtid_b32 v122 offset:7392\n\t" \
  "v_pk_fma_f32 v[92:93], v[190:191], v[92:93], v[112:113] op_sel:[0,1,0] op_sel_hi:[1,1,1]\n\t" \
  "v_pk_fma_f32 v[98:99], v[192:193], v[98:99], v[114:115] op_sel:[0,1,0] op_sel_hi:[1,1,1]\n\t" \
  "ds_write_addtid_b32 v123 offset:1584\n\t" \
  "v_pk_fma_f32 v[102:103], v[186:187], v[96:97], v[116:117] op_sel:[0,1,0] op_sel_hi:[1,1,1]\n\t" \
  "v_pk_fma_f32 v[100:101], v[192:193], v[100:101], v[118:119] op_sel:[0,1,0] op_sel_hi:[1,1,1]\n\t" \
  "s_add_u32 s48, s48, 2\n\t" \
  "s_lshl_b32 s43, s48, 8\n\t" \
  "v_add_u32 v54, s43, v31\n\t" \
  "s_lshl_b32 s43, s48, 1\n\t" \
  "v_mov_b32 v55, s43\n\t" \
  "global_load_short_d16_hi v227, v54, %4\n\t" \
  "global_load_short_d16_hi v228, v54, %5\n\t" \
  "global_load_short_d16_hi v229, v54, %6\n\t" \
  "global_load_short_d16_hi v230, v54, %7\n\t" \
  "global_load_short_d16_hi v231, v54, %8\n\t" \
  "global_load_short_d16_hi v232, v54, %9\n\t" \
  "global_load_short_d16_hi v233, v55, %10\n\t" \
  "v_cvt_pk_bf16_f32 v124, v94, v95\n\t" \
  "v_cvt_pk_bf16_f32 v125, v88, v89\n\t" \
  "v_cvt_pk_bf16_f32 v126, v90, v91\n\t" \
  "v_cvt_pk_bf16_f32 v127, v92, v93\n\t" \
  "v_cvt_pk_bf16_f32 v128, v98, v99\n\t" \
  "v_cvt_pk_bf16_f32 v129, v102, v103\n\t" \
  "v_cvt_pk_bf16_f32 v122, v100, v101\n\t" \
  "v_pk_mul_f32 v[106:107], v[182:183], v[94:95] op_sel:[0,0] op_sel_hi:[1,0]\n\t" \
  "ds_write_addtid_b32 v124 offset:16896\n\t" \
  "v_pk_mul_f32 v[108:109], v[182:183], v[88:89] op_sel:[0,0] op_sel_hi:[1,0]\n\t" \
  "ds_write_addtid_b32 v125 offset:17424\n\t" \
  "v_pk_mul_f32 v[110:111], v[182:183], v[90:91] op_sel:[0,0] op_sel_hi:[1,0]\n\t" \
  "v_pk_mul_f32 v[112:113], v[182:183], v[92:93] op_sel:[0,0] op_sel_hi:[1,0]\n\t" \
  "ds_write_addtid_b32 v126 offset:17952\n\t" \
  "v_pk_mul_f32 v[114:115], v[184:185], v[98:99] op_sel:[0,0] op_sel_hi:[1,0]\n\t" \
  "v_pk_mul_f32 v[116:117], v[178:179], v[102:103] op_sel:[0,0] op_sel_hi:[1,0]\n\t" \
  "ds_write_addtid_b32 v127 offset:18480\n\t" \
  "v_pk_fma_f32 v[94:95], v[190:191], v[94:95], v[106:107] op_sel:[0,1,0] op_sel_hi:[1,1,1]\n\t" \
  "ds_write_addtid_b32 v128 offset:8448\n\t" \
  "v_pk_fma_f32 v[88:89], v[190:191], v[88:89], v[108:109] op_sel:[0,1,0] op_sel_hi:[1,1,1]\n\t" \
  "v_pk_fma_f32 v[90:91], v[190:191], v[90:91], v[110:111] op_sel:[0,1,0] op_sel_hi:[1,1,1]\n\t" \
  "ds_write_addtid_b32 v129 offset:2112\n\t" \
  "v_pk_fma_f32 v[92:93], v[190:191], v[92:93], v[112:113] op_sel:[0,1,0] op_sel_hi:[1,1,1]\n\t" \
  "v_pk_fma_f32 v[98:99], v[192:193], v[98:99], v[114:115] op_sel:[0,1,0] op_sel_hi:[1,1,1]\n\t" \
  "ds_write_addtid_b32 v122 offset:4752\n\t" \
  "v_pk_fma_f32 v[96:97], v[186:187], v[102:103], v[116:117] op_sel:[0,1,0] op_sel_hi:[1,1,1]\n\t" \
  "v_cvt_pk_bf16_f32 v123, v94, v95\n\t" \
  "v_cvt_pk_bf16_f32 v124, v88, v89\n\t" \
  "v_cvt_pk_bf16_f32 v125, v90, v91\n\t" \
  "v_cvt_pk_bf16_f32 v126, v92, v93\n\t" \
  "v_cvt_pk_bf16_f32 v127, v98, v99\n\t" \
  "v_cvt_pk_bf16_f32 v128, v96, v97\n\t" \
  "v_pk_mul_f32 v[106:107], v[182:183], v[94:95] op_sel:[0,0] op_sel_hi:[1,0]\n\t" \
  "ds_write_addtid_b32 v123 offset:19008\n\t" \
  "v_pk_mul_f32 v[108:109], v[182:183], v[88:89] op_sel:[0,0] op_sel_hi:[1,0]\n\t" \
  "v_pk_mul_f32 v[110:111], v[182:183], v[90:91] op_sel:[0,0] op_sel_hi:[1,0]\n\t" \
  "ds_write_addtid_b32 v124 offset:19536\n\t" \
  "v_pk_mul_f32 v[112:113], v[182:183], v[92:93] op_sel:[0,0] op_sel_hi:[1,0]\n\t" \
  "v_pk_mul_f32 v[114:115], v[184:185], v[98:99] op_sel:[0,0] op_sel_hi:[1,0]\n\t" \
  "ds_write_addtid_b32 v125 offset:20064\n\t" \
  "v_pk_mul_f32 v[116:117], v[178:179], v[96:97] op_sel:[0,0] op_sel_hi:[1,0]\n\t" \
  "v_pk_mul_f32 v[118:119], v[184:185], v[102:103] op_sel:[0,0] op_sel_hi:[1,0]\n\t" \
  "v_pk_fma_f32 v[94:95], v[190:191], v[94:95], v[106:107] op_sel:[0,1,0] op_sel_hi:[1,1,1]\n\t" \
  "ds_write_addtid_b32 v126 offset:20592\n\t" \
  "v_pk_fma_f32 v[88:89], v[190:191], v[88:89], v[108:109] op_sel:[0,1,0] op_sel_hi:[1,1,1]\n\t" \
  "v_pk_fma_f32 v[90:91], v[190:191], v[90:91], v[110:111] op_sel:[0,1,0] op_sel_hi:[1,1,1]\n\t" \
  "ds_write_addtid_b32 v127 offset:8976\n\t" \
  "v_pk_fma_f32 v[92:93], v[190:191], v[92:93], v[112:113] op_sel:[0,1,0] op_sel_hi:[1,1,1]\n\t" \
  "v_pk_fma_f32 v[98:99], v[192:193], v[98:99], v[114:115] op_sel:[0,1,0] op_sel_hi:[1,1,1]\n\t" \
  "ds_write_addtid_b32 v128 offset:2640\n\t" \
  "v_pk_fma_f32 v[104:105], v[186:187], v[96:97], v[116:117] op_sel:[0,1,0] op_sel_hi:[1,1,1]\n\t" \
  "v_pk_fma_f32 v[102:103], v[192:193], v[102:103], v[118:119] op_sel:[0,1,0] op_sel_hi:[1,1,1]\n\t" \
  "v_cvt_pk_bf16_f32 v129, v94, v95\n\t" \
  "v_cvt_pk_bf16_f32 v122, v88, v89\n\t" \
  "v_cvt_pk_bf16_f32 v123, v90, v91\n\t" \
  "v_cvt_pk_bf16_f32 v124, v92, v93\n\t" \
  "v_cvt_pk_bf16_f32 v125, v98, v99\n\t" \
  "v_cvt_pk_bf16_f32 v126, v104, v105\n\t" \
  "v_cvt_pk_bf16_f32 v127, v102, v103\n\t" \
  "v_pk_mul_f32 v[106:107], v[182:183], v[94:95] op_sel:[0,0] op_sel_hi:[1,0]\n\t" \
  "ds_write_addtid_b32 v129 offset:21120\n\t" \
  "v_pk_mul_f32 v[108:109], v[182:183], v[88:89] op_sel:[0,0] op_sel_hi:[1,0]\n\t" \
  "v_pk_mul_f32 v[110:111], v[182:183], v[90:91] op_sel:[0,0] op_sel_hi:[1,0]\n\t" \
  "ds_write_addtid_b32 v122 offset:21648\n\t" \
  "v_pk_mul_f32 v[112:113], v[182:183], v[92:93] op_sel:[0,0] op_sel_hi:[1,0]\n\t" \
  "v_pk_mul_f32 v[114:115], v[184:185], v[98:99] op_sel:[0,0] op_sel_hi:[1,0]\n\t" \
  "ds_write_addtid_b32 v123 offset:22176\n\t" \
  "v_pk_mul_f32 v[116:117], v[178:179], v[104:105] op_sel:[0,0] op_sel_hi:[1,0]\n\t" \
  "v_pk_mul_f32 v[118:119], v[184:185], v[102:103] op_sel:[0,0] op_sel_hi:[1,0]\n\t" \
  "ds_write_addtid_b32 v124 offset:22704\n\t" \
  "v_pk_fma_f32 v[94:95], v[190:191], v[94:95], v[106:107] op_sel:[0,1,0] op_sel_hi:[1,1,1]\n\t" \
  "v_pk_fma_f32 v[88:89], v[190:191], v[88:89], v[108:109] op_sel:[0,1,0] op_sel_hi:[1,1,1]\n\t" \
  "ds_write_addtid_b32 v125 offset:9504\n\t" \
  "v_pk_fma_f32 v[90:91], v[190:191], v[90:91], v[110:111] op_sel:[0,1,0] op_sel_hi:[1,1,1]\n\t" \
  "v_pk_fma_f32 v[92:93], v[190:191], v[92:93], v[112:113] op_sel:[0,1,0] op_sel_hi:[1,1,1]\n\t" \
  "ds_write_addtid_b32 v126 offset:3168\n\t" \
  "v_pk_fma_f32 v[98:99], v[192:193], v[98:99], v[114:115] op_sel:[0,1,0] op_sel_hi:[1,1,1]\n\t" \
  "v_pk_fma_f32 v[96:97], v[186:187], v[104:105], v[116:117] op_sel:[0,1,0] op_sel_hi:[1,1,1]\n\t" \
  "ds_write_addtid_b32 v127 offset:5280\n\t" \
  "v_pk_fma_f32 v[102:103], v[192:193], v[102:103], v[118:119] op_sel:[0,1,0] op_sel_hi:[1,1,1]\n\t" \
  "v_cvt_pk_bf16_f32 v128, v94, v95\n\t" \
  "v_cvt_pk_bf16_f32 v129, v88, v89\n\t" \
  "v_cvt_pk_bf16_f32 v122, v90, v91\n\t" \
  "v_cvt_pk_bf16_f32 v123, v92, v93\n\t" \
  "v_cvt_pk_bf16_f32 v124, v98, v99\n\t" \
  "v_cvt_pk_bf16_f32 v125, v96, v97\n\t" \
  "v_cvt_pk_bf16_f32 v126, v102, v103\n\t" \
  "v_pk_mul_f32 v[106:107], v[182:183], v[94:95] op_sel:[0,0] op_sel_hi:[1,0]\n\t" \
  "ds_write_addtid_b32 v128 offset:23232\n\t" \
  "v_pk_mul_f32 v[108:109], v[182:183], v[88:89] op_sel:[0,0] op_sel_hi:[1,0]\n\t" \
  "v_pk_mul_f32 v[110:111], v[182:183], v[90:91] op_sel:[0,0] op_sel_hi:[1,0]\n\t" \
  "ds_write_addtid_b32 v129 offset:23760\n\t" \
  "v_pk_mul_f32 v[112:113], v[182:183], v[92:93] op_sel:[0,0] op_sel_hi:[1,0]\n\t" \
  "v_pk_mul_f32 v[114:115], v[184:185], v[98:99] op_sel:[0,0] op_sel_hi:[1,0]\n\t" \
  "ds_write_addtid_b32 v122 offset:24288\n\t" \
  "v_pk_mul_f32 v[116:117], v[184:185], v[102:103] op_sel:[0,0] op_sel_hi:[1,0]\n\t" \
  "v_pk_mul_f32 v[118:119], v[184:185], v[104:105] op_sel:[0,0] op_sel_hi:[1,0]\n\t" \
  "ds_write_addtid_b32 v123 offset:24816\n\t" \
  "v_pk_fma_f32 v[94:95], v[190:191], v[94:95], v[106:107] op_sel:[0,1,0] op_sel_hi:[1,1,1]\n\t" \
  "v_pk_fma_f32 v[88:89], v[190:191], v[88:89], v[108:109] op_sel:[0,1,0] op_sel_hi:[1,1,1]\n\t" \
  "ds_write_addtid_b32 v124 offset:10032\n\t" \
  "v_pk_fma_f32 v[90:91], v[190:191], v[90:91], v[110:111] op_sel:[0,1,0] op_sel_hi:[1,1,1]\n\t" \
  "v_pk_fma_f32 v[92:93], v[190:191], v[92:93], v[112:113] op_sel:[0,1,0] op_sel_hi:[1,1,1]\n\t" \
  "ds_write_addtid_b32 v125 offset:3696\n\t" \
  "v_pk_fma_f32 v[98:99], v[192:193], v[98:99], v[114:115] op_sel:[0,1,0] op_sel_hi:[1,1,1]\n\t" \
  "v_pk_fma_f32 v[102:103], v[192:193], v[102:103], v[116:117] op_sel:[0,1,0] op_sel_hi:[1,1,1]\n\t" \
  "ds_write_addtid_b32 v126 offset:6864\n\t" \
  "v_pk_fma_f32 v[104:105], v[192:193], v[104:105], v[118:119] op_sel:[0,1,0] op_sel_hi:[1,1,1]\n\t" \
  "v_cvt_pk_bf16_f32 v127, v94, v95\n\t" \
  "v_cvt_pk_bf16_f32 v128, v88, v89\n\t" \
  "v_cvt_pk_bf16_f32 v129, v90, v91\n\t" \
  "v_cvt_pk_bf16_f32 v122, v92, v93\n\t" \
  "v_cvt_pk_bf16_f32 v123, v98, v99\n\t" \
  "v_cvt_pk_bf16_f32 v124, v102, v103\n\t" \
  "v_cvt_pk_bf16_f32 v125, v104, v105\n\t" \
  "v_pk_mul_f32 v[106:107], v[182:183], v[94:95] op_sel:[0,0] op_sel_hi:[1,0]\n\t" \
  "ds_write_addtid_b32 v127 offset:25344\n\t" \
  "ds_write_addtid_b32 v128 offset:25872\n\t" \
  "ds_write_addtid_b32 v129 offset:26400\n\t" \
  "ds_write_addtid_b32 v122 offset:26928\n\t" \
  "v_pk_fma_f32 v[94:95], v[190:191], v[94:95], v[106:107] op_sel:[0,1,0] op_sel_hi:[1,1,1]\n\t" \
  "ds_write_addtid_b32 v123 offset:10560\n\t" \
  "ds_write_addtid_b32 v124 offset:7920\n\t" \
  "ds_write_addtid_b32 v125 offset:5808\n\t" \
  "v_cvt_pk_bf16_f32 v126, v94, v95\n\t" \
  "s_nop 0\n\t" \
  "ds_write_addtid_b32 v126 offset:27456\n\t" \
  "v_and_b32 v54, 3, v17\n\t" \
  "v_cmp_eq_u32 s[54:55], 1, v54\n\t" \
  "v_cmp_eq_u32 s[56:57], 2, v54\n\t" \
  "v_cmp_eq_u32 s[58:59], 3, v54\n\t" \
  "v_lshrrev_b32 v55, 2, v17\n\t" \
  "v_cmp_eq_u32 vcc, v55, v18\n\t" \
  "v_lshl_add_u32 v54, v18, 3, v54\n\t" \
  "v_lshlrev_b32 v54, 5, v54\n\t" \
  "s_lshl_b32 s43, %13, 3\n\t" \
  "s_add_u32 s43, s43, 147712\n\t" \
  "v_add_u32 v54, s43, v54\n\t" \
  "v_mov_b32 v55, 155904\n\t" \
  "v_cndmask_b32 v61, v55, v54, vcc\n\t" \
  "v_mov_b32 v55, 0x7ffffff0\n\t" \
  "v_cmp_eq_u32 vcc, 0, v19\n\t" \
  "s_nop 1\n\t" \
  "v_cndmask_b32 v34, v55, 0, vcc\n\t" \
  "s_waitcnt vmcnt(22)\n\t" \
  "v_mfma_f32_16x16x32_bf16 v[144:147], a[0:3], a[48:51], 0\n\t" \
  "v_mfma_f32_16x16x32_bf16 v[152:155], a[0:3], a[0:3], 0\n\t" \
  "v_mfma_f32_16x16x32_bf16 v[148:151], a[4:7], a[48:51], 0\n\t" \
  "v_mfma_f32_16x16x32_bf16 v[156:159], a[4:7], a[4:7], 0\n\t" \
  "s_waitcnt vmcnt(20)\n\t" \
  "v_mfma_f32_16x16x32_bf16 v[144:147], a[8:11], a[48:51], v[144:147]\n\t" \
  "v_mfma_f32_16x16x32_bf16 v[152:155], a[8:11], a[8:11], v[152:155]\n\t" \
  "v_mfma_f32_16x16x32_bf16 v[148:151], a[12:15], a[48:51], v[148:151]\n\t" \
  "v_mfma_f32_16x16x32_bf16 v[156:159], a[12:15], a[12:15], v[156:159]\n\t" \
  "s_waitcnt vmcnt(18)\n\t" \
  "v_mfma_f32_16x16x32_bf16 v[144:147], a[16:19], a[48:51], v[144:147]\n\t" \
  "v_mfma_f32_16x16x32_bf16 v[152:155], a[16:19], a[16:19], v[152:155]\n\t" \
  "v_mfma_f32_16x16x32_bf16 v[148:151], a[20:23], a[48:51], v[148:151]\n\t" \
  "v_mfma_f32_16x16x32_bf16 v[156:159], a[20:23], a[20:23], v[156:159]\n\t" \
  "s_waitcnt vmcnt(16)\n\t" \
  "v_mfma_f32_16x16x32_bf16 v[144:147], a[24:27], a[48:51], v[144:147]\n\t" \
  "v_mfma_f32_16x16x32_bf16 v[152:155], a[24:27], a[24:27], v[152:155]\n\t" \
  "v_mfma_f32_16x16x32_bf16 v[148:151], a[28:31], a[48:51], v[148:151]\n\t" \
  "v_mfma_f32_16x16x32_bf16 v[156:159], a[28:31], a[28:31], v[156:159]\n\t" \
  "s_waitcnt vmcnt(14)\n\t" \
  "s_cmp_eq_u32 s42, 1\n\t" \
  "s_cbranch_scc1 L_x4_%=\n\t" \
  "v_mfma_f32_16x16x32_bf16 v[144:147], a[32:35], a[48:51], v[144:147]\n\t" \
  "v_mfma_f32_16x16x32_bf16 v[152:155], a[32:35], a[32:35], v[152:155]\n\t" \
  "v_mfma_f32_16x16x32_bf16 v[148:151], a[36:39], a[48:51], v[148:151]\n\t" \
  "v_mfma_f32_16x16x32_bf16 v[156:159], a[36:39], a[36:39], v[156:159]\n\t" \
  "L_x4_%=:\n\t" \
  "v_mfma_f32_16x16x32_bf16 v[64:67], a[0:3], a[40:43], 0\n\t" \
  "v_mfma_f32_16x16x32_bf16 v[72:75], a[0:3], a[44:47], 0\n\t" \
  "v_mfma_f32_16x16x32_bf16 v[68:71], a[4:7], a[40:43], 0\n\t" \
  "v_mfma_f32_16x16x32_bf16 v[76:79], a[4:7], a[44:47], 0\n\t" \
  "v_mfma_f32_16x16x32_bf16 v[80:83], a[8:11], a[40:43], 0\n\t" \
  "v_mfma_f32_16x16x32_bf16 v[88:91], a[8:11], a[44:47], 0\n\t" \
  "v_cndmask_b32 v54, v144, v145, s[54:55]\n\t" \
  "v_cndmask_b32 v54, v54, v146, s[56:57]\n\t" \
  "v_cndmask_b32 v54, v54, v147, s[58:59]\n\t" \
  "v_mfma_f32_16x16x32_bf16 v[84:87], a[12:15], a[40:43], 0\n\t" \
  "v_cndmask_b32 v56, v148, v149, s[54:55]\n\t" \
  "v_cndmask_b32 v56, v56, v150, s[56:57]\n\t" \
  "v_cndmask_b32 v56, v56, v151, s[58:59]\n\t" \
  "v_mfma_f32_16x16x32_bf16 v[92:95], a[12:15], a[44:47], 0\n\t" \
  "v_cndmask_b32 v55, v152, v153, s[54:55]\n\t" \
  "v_cndmask_b32 v55, v55, v154, s[56:57]\n\t" \
  "v_cndmask_b32 v55, v55, v155, s[58:59]\n\t" \
  "v_mfma_f32_16x16x32_bf16 v[96:99], a[16:19], a[40:43], 0\n\t" \
  "v_cndmask_b32 v57, v156, v157, s[54:55]\n\t" \
  "v_cndmask_b32 v57, v57, v158, s[56:57]\n\t" \
  "v_cndmask_b32 v57, v57, v159, s[58:59]\n\t" \
  "v_mfma_f32_16x16x32_bf16 v[104:107], a[16:19], a[44:47], 0\n\t" \
  "ds_write_b64 v61, v[54:55]\n\t" \
  "ds_write_b64 v61, v[56:57] offset:128\n\t" \
  "v_mfma_f32_16x16x32_bf16 v[100:103], a[20:23], a[40:43], 0\n\t" \
  "v_mfma_f32_16x16x32_bf16 v[108:111], a[20:23], a[44:47], 0\n\t" \
  "v_mfma_f32_16x16x32_bf16 v[112:115], a[24:27], a[40:43], 0\n\t" \
  "v_mfma_f32_16x16x32_bf16 v[120:123], a[24:27], a[44:47], 0\n\t" \
  "v_mfma_f32_16x16x32_bf16 v[116:119], a[28:31], a[40:43], 0\n\t" \
  "v_mfma_f32_16x16x32_bf16 v[124:127], a[28:31], a[44:47], 0\n\t" \
  "v_mfma_f32_16x16x32_bf16 v[128:131], a[32:35], a[40:43], 0\n\t" \
  "v_mfma_f32_16x16x32_bf16 v[136:139], a[32:35], a[44:47], 0\n\t" \
  "v_mfma_f32_16x16x32_bf16 v[132:135], a[36:39], a[40:43], 0\n\t" \
  "v_mfma_f32_16x16x32_bf16 v[140:143], a[36:39], a[44:47], 0\n\t" \
  "v_and_b32 v58, 31, v16\n\t" \
  "v_lshlrev_b32 v59, 5, v58\n\t" \
  "v_add_u32 v59, 147712, v59\n\t" \
  "s_waitcnt lgkmcnt(0)\n\t" \
  "s_barrier\n\t" \
  "ds_read_b128 v[144:147], v59\n\t" \
  "ds_read_b128 v[148:151], v59 offset:16\n\t" \
  "s_lshl_b32 s43, %13, 8\n\t" \
  "s_add_u32 s43, s43, 148736\n\t" \
  "v_lshl_add_u32 v59, v58, 2, s43\n\t" \
  "v_lshl_add_u32 v60, v18, 5, s43\n\t" \
  "v_lshlrev_b32 v61, 3, v19\n\t" \
  "v_mov_b32 v62, 0x7ffffff0\n\t" \
  "v_cmp_gt_u32 vcc, 32, v19\n\t" \
  "s_lshl_b32 s43, s40, 8\n\t" \
  "s_add_u32 s43, s43, 327680\n\t" \
  "v_cndmask_b32 v61, v62, v61, vcc\n\t" \
  "s_waitcnt lgkmcnt(0)\n\t" \
  "v_pk_add_f32 v[144:145], v[144:145], v[146:147]\n\t" \
  "v_pk_add_f32 v[148:149], v[148:149], v[150:151]\n\t" \
  "v_pk_add_f32 v[144:145], v[144:145], v[148:149]\n\t" \
  "v_mul_f32 v144, 0x3ae38e39, v144\n\t" \
  "v_mul_f32 v145, 0x3ae38e39, v145\n\t" \
  "v_fma_f32 v145, -v144, v144, v145\n\t" \
  "v_max_f32 v145, 0, v145\n\t" \
  "v_cvt_pk_bf16_f32 v144, v144, v145\n\t" \
  "v_and_b32 v145, 0xffff0000, v144\n\t" \
  "v_lshlrev_b32 v144, 16, v144\n\t" \
  "v_add_f32 v145, 0x3727c5ac, v145\n\t" \
  "v_cvt_pk_bf16_f32 v145, v145, v145\n\t" \
  "v_and_b32 v145, 0xffff0000, v145\n\t" \
  "v_sqrt_f32 v145, v145\n\t" \
  "v_xor_b32 v144, 0x80000000, v144\n\t" \
  "v_cvt_pk_bf16_f32 v145, v145, v145\n\t" \
  "v_and_b32 v145, 0xffff0000, v145\n\t" \
  "v_rcp_f32 v146, v145\n\t" \
  "buffer_store_dwordx2 v[144:145], v61, %3, s43 offen sc1\n\t" \
  "v_mul_f32 v147, v144, v146\n\t" \
  "ds_write_b32 v59, v146\n\t" \
  "ds_write_b32 v59, v147 offset:128\n\t" \
  "ds_read_b128 v[144:147], v60 offset:0\n\t" \
  "ds_read_b128 v[148:151], v60 offset:16\n\t" \
  "ds_read_b128 v[152:155], v60 offset:128\n\t" \
  "ds_read_b128 v[156:159], v60 offset:144\n\t" \
  "s_mul_i32 s43, %13, 12800\n\t" \
  "v_mul_u32_u24 v54, 80, v17\n\t" \
  "v_xor_b32 v55, 3, v18\n\t" \
  "v_lshl_add_u32 v54, v55, 4, v54\n\t" \
  "v_add_u32 v54, s43, v54\n\t" \
  "v_lshrrev_b32 v56, 2, v16\n\t" \
  "v_and_b32 v57, 3, v16\n\t" \
  "v_lshlrev_b32 v57, 4, v57\n\t" \
  "v_mul_u32_u24 v55, 80, v56\n\t" \
  "v_add3_u32 v55, v55, v57, s43\n\t" \
  "v_mul_u32_u24 v56, 4608, v56\n\t" \
  "v_add_u32 v56, v56, v57\n\t" \
  "s_mul_i32 s60, s44, 147456\n\t" \
  "s_lshl_b32 s43, s40, 6\n\t" \
  "s_sub_u32 s60, s60, s43\n\t" \
  "s_add_u32 s60, s60, 1052608\n\t" \
  "s_waitcnt lgkmcnt(0)\n\t" \
  "v_pk_fma_f32 v[64:65], v[64:65], v[144:145], v[152:153]\n\t" \
  "v_pk_fma_f32 v[66:67], v[66:67], v[146:147], v[154:155]\n\t" \
  "v_pk_fma_f32 v[68:69], v[68:69], v[148:149], v[156:157]\n\t" \
  "v_pk_fma_f32 v[70:71], v[70:71], v[150:151], v[158:159]\n\t" \
  "v_pk_fma_f32 v[72:73], v[72:73], v[144:145], v[152:153]\n\t" \
  "v_pk_fma_f32 v[74:75], v[74:75], v[146:147], v[154:155]\n\t" \
  "v_pk_fma_f32 v[76:77], v[76:77], v[148:149], v[156:157]\n\t" \
  "v_pk_fma_f32 v[78:79], v[78:79], v[150:151], v[158:159]\n\t" \
  "v_cvt_pk_bf16_f32 v44, v71, v70\n\t" \
  "v_cvt_pk_bf16_f32 v45, v69, v68\n\t" \
  "v_cvt_pk_bf16_f32 v46, v67, v66\n\t" \
  "v_cvt_pk_bf16_f32 v47, v65, v64\n\t" \
  "ds_write_b128 v54, v[44:47] offset:0\n\t" \
  "v_pk_fma_f32 v[80:81], v[80:81], v[144:145], v[152:153]\n\t" \
  "v_pk_fma_f32 v[82:83], v[82:83], v[146:147], v[154:155]\n\t" \
  "v_pk_fma_f32 v[84:85], v[84:85], v[148:149], v[156:157]\n\t" \
  "v_pk_fma_f32 v[86:87], v[86:87], v[150:151], v[158:159]\n\t" \
  "v_cvt_pk_bf16_f32 v48, v79, v78\n\t" \
  "v_cvt_pk_bf16_f32 v49, v77, v76\n\t" \
  "v_cvt_pk_bf16_f32 v50, v75, v74\n\t" \
  "v_cvt_pk_bf16_f32 v51, v73, v72\n\t" \
  "ds_write_b128 v54, v[48:51] offset:1280\n\t" \
  "v_pk_fma_f32 v[88:89], v[88:89], v[144:145], v[152:153]\n\t" \
  "v_pk_fma_f32 v[90:91], v[90:91], v[146:147], v[154:155]\n\t" \
  "v_pk_fma_f32 v[92:93], v[92:93], v[148:149], v[156:157]\n\t" \
  "v_pk_fma_f32 v[94:95], v[94:95], v[150:151], v[158:159]\n\t" \
  "v_cvt_pk_bf16_f32 v44, v87, v86\n\t" \
  "v_cvt_pk_bf16_f32 v45, v85, v84\n\t" \
  "v_cvt_pk_bf16_f32 v46, v83, v82\n\t" \
  "v_cvt_pk_bf16_f32 v47, v81, v80\n\t" \
  "ds_write_b128 v54, v[44:47] offset:2560\n\t" \
  "v_pk_fma_f32 v[96:97], v[96:97], v[144:145], v[152:153]\n\t" \
  "v_pk_fma_f32 v[98:99], v[98:99], v[146:147], v[154:155]\n\t" \
  "v_pk_fma_f32 v[100:101], v[100:101], v[148:149], v[156:157]\n\t" \
  "v_pk_fma_f32 v[102:103], v[102:103], v[150:151], v[158:159]\n\t" \
  "v_cvt_pk_bf16_f32 v48, v95, v94\n\t" \
  "v_cvt_pk_bf16_f32 v49, v93, v92\n\t" \
  "v_cvt_pk_bf16_f32 v50, v91, v90\n\t" \
  "v_cvt_pk_bf16_f32 v51, v89, v88\n\t" \
  "ds_write_b128 v54, v[48:51] offset:3840\n\t" \
  "v_pk_fma_f32 v[104:105], v[104:105], v[144:145], v[152:153]\n\t" \
  "v_pk_fma_f32 v[106:107], v[106:107], v[146:147], v[154:155]\n\t" \
  "v_pk_fma_f32 v[108:109], v[108:109], v[148:149], v[156:157]\n\t" \
  "v_pk_fma_f32 v[110:111], v[110:111], v[150:151], v[158:159]\n\t" \
  "v_cvt_pk_bf16_f32 v44, v103, v102\n\t" \
  "v_cvt_pk_bf16_f32 v45, v101, v100\n\t" \
  "v_cvt_pk_bf16_f32 v46, v99, v98\n\t" \
  "v_cvt_pk_bf16_f32 v47, v97, v96\n\t" \
  "ds_write_b128 v54, v[44:47] offset:5120\n\t" \
  "v_pk_fma_f32 v[112:113], v[112:113], v[144:145], v[152:153]\n\t" \
  "v_pk_fma_f32 v[114:115], v[114:115], v[146:147], v[154:155]\n\t" \
  "v_pk_fma_f32 v[116:117], v[116:117], v[148:149], v[156:157]\n\t" \
  "v_pk_fma_f32 v[118:119], v[118:119], v[150:151], v[158:159]\n\t" \
  "v_cvt_pk_bf16_f32 v48, v111, v110\n\t" \
  "v_cvt_pk_bf16_f32 v49, v109, v108\n\t" \
  "v_cvt_pk_bf16_f32 v50, v107, v106\n\t" \
  "v_cvt_pk_bf16_f32 v51, v105, v104\n\t" \
  "ds_write_b128 v54, v[48:51] offset:6400\n\t" \
  "v_pk_fma_f32 v[120:121], v[120:121], v[144:145], v[152:153]\n\t" \
  "v_pk_fma_f32 v[122:123], v[122:123], v[146:147], v[154:155]\n\t" \
  "v_pk_fma_f32 v[124:125], v[124:125], v[148:149], v[156:157]\n\t" \
  "v_pk_fma_f32 v[126:127], v[126:127], v[150:151], v[158:159]\n\t" \
  "v_cvt_pk_bf16_f32 v44, v119, v118\n\t" \
  "v_cvt_pk_bf16_f32 v45, v117, v116\n\t" \
  "v_cvt_pk_bf16_f32 v46, v115, v114\n\t" \
  "v_cvt_pk_bf16_f32 v47, v113, v112\n\t" \
  "ds_write_b128 v54, v[44:47] offset:7680\n\t" \
  "v_pk_fma_f32 v[128:129], v[128:129], v[144:145], v[152:153]\n\t" \
  "v_pk_fma_f32 v[130:131], v[130:131], v[146:147], v[154:155]\n\t" \
  "v_pk_fma_f32 v[132:133], v[132:133], v[148:149], v[156:157]\n\t" \
  "v_pk_fma_f32 v[134:135], v[134:135], v[150:151], v[158:159]\n\t" \
  "v_cvt_pk_bf16_f32 v48, v127, v126\n\t" \
  "v_cvt_pk_bf16_f32 v49, v125, v124\n\t" \
  "v_cvt_pk_bf16_f32 v50, v123, v122\n\t" \
  "v_cvt_pk_bf16_f32 v51, v121, v120\n\t" \
  "ds_write_b128 v54, v[48:51] offset:8960\n\t" \
  "v_pk_fma_f32 v[136:137], v[136:137], v[144:145], v[152:153]\n\t" \
  "v_pk_fma_f32 v[138:139], v[138:139], v[146:147], v[154:155]\n\t" \
  "v_pk_fma_f32 v[140:141], v[140:141], v[148:149], v[156:157]\n\t" \
  "v_pk_fma_f32 v[142:143], v[142:143], v[150:151], v[158:159]\n\t" \
  "v_cvt_pk_bf16_f32 v44, v135, v134\n\t" \
  "v_cvt_pk_bf16_f32 v45, v133, v132\n\t" \
  "v_cvt_pk_bf16_f32 v46, v131, v130\n\t" \
  "v_cvt_pk_bf16_f32 v47, v129, v128\n\t" \
  "ds_write_b128 v54, v[44:47] offset:10240\n\t" \
  "v_cvt_pk_bf16_f32 v48, v143, v142\n\t" \
  "v_cvt_pk_bf16_f32 v49, v141, v140\n\t" \
  "v_cvt_pk_bf16_f32 v50, v139, v138\n\t" \
  "v_cvt_pk_bf16_f32 v51, v137, v136\n\t" \
  "ds_write_b128 v54, v[48:51] offset:11520\n\t" \
  "ds_read_b128 a[64:67], v55 offset:0\n\t" \
  "ds_read_b128 a[68:71], v55 offset:1280\n\t" \
  "ds_read_b128 a[72:75], v55 offset:2560\n\t" \
  "ds_read_b128 a[76:79], v55 offset:3840\n\t" \
  "ds_read_b128 a[80:83], v55 offset:5120\n\t" \
  "ds_read_b128 a[84:87], v55 offset:6400\n\t" \
  "ds_read_b128 a[88:91], v55 offset:7680\n\t" \
  "ds_read_b128 a[92:95], v55 offset:8960\n\t" \
  "ds_read_b128 a[96:99], v55 offset:10240\n\t" \
  "ds_read_b128 a[100:103], v55 offset:11520\n\t" \
  "s_cmp_eq_u32 s42, 1\n\t" \
  "s_cselect_b32 s45, 0x70000000, 0\n\t" \
  "v_mov_b32 v52, v56\n\t" \
  "v_add_u32 v53, s45, v56\n\t" \
  "v_mul_u32_u24 v54, 4608, v19\n\t" \
  "v_mov_b32 v55, 0x7ffffff0\n\t" \
  "v_cmp_gt_u32 vcc, 9, v19\n\t" \
  "s_mul_i32 s43, s40, 41472\n\t" \
  "s_add_u32 s43, s43, 1052672\n\t" \
  "v_cndmask_b32 v54, v55, v54, vcc\n\t" \
  "v_mov_b32 v104, 0\n\t" \
  "v_mov_b32 v105, 0\n\t" \
  "v_mov_b32 v106, 0\n\t" \
  "v_mov_b32 v107, 0\n\t" \
  "buffer_store_dwordx4 v[104:107], v54, %3, s43 offen sc1\n\t" \
  "s_waitcnt vmcnt(2)\n\t" \
  "v_add_co_u32 v32, vcc, 1, v32\n\t" \
  "s_lshl_b32 s43, s40, 6\n\t" \
  "s_add_u32 s43, s43, 4096\n\t" \
  "v_addc_co_u32 v33, vcc, 0, v33, vcc\n\t" \
  "buffer_store_dwordx2 v[32:33], v34, %3, s43 offen sc1\n\t" \
  "v_mov_b32 v35, v220\n\t" \
  "v_mov_b32 v36, v221\n\t" \
  "v_mov_b32 v37, v222\n\t" \
  "v_mov_b32 v38, v223\n\t" \
  "v_mov_b32 v39, v224\n\t" \
  "v_mov_b32 v40, v225\n\t" \
  "v_mov_b32 v41, v226\n\t" \
  "v_lshlrev_b32 v55, 4, v19\n\t" \
  "v_mul_u32_u24 v44, 528, v17\n\t" \
  "v_lshrrev_b32 v57, 1, v18\n\t" \
  "v_lshl_add_u32 v44, v57, 7, v44\n\t" \
  "v_and_b32 v57, 1, v18\n\t" \
  "v_lshl_add_u32 v44, v57, 8, v44\n\t" \
  "s_mul_i32 s43, s41, 35840\n\t" \
  "s_add_u32 s43, s43, 76032\n\t" \
  "v_add_u32 v44, s43, v44\n\t" \
  "s_mul_i32 s43, s42, 8448\n\t" \
  "s_add_u32 s43, s43, 11088\n\t" \
  "v_add_u32 v45, s43, v44\n\t" \
  "s_lshl_b32 s43, %13, 10\n\t" \
  "s_add_u32 s43, s43, 147712\n\t" \
  "v_lshl_add_u32 v46, v16, 4, s43\n\t" \
  "v_add_u32 v47, 112, v55\n\t" \
  "s_waitcnt lgkmcnt(0)\n\t" \
  "s_barrier\n\t" \
  "v_lshrrev_b32 v57, 3, v19\n\t" \
  "v_mul_u32_u24 v57, 4224, v57\n\t" \
  "v_and_b32 v58, 7, v19\n\t" \
  "v_min_u32 v58, 6, v58\n\t" \
  "v_lshl_add_u32 v57, v58, 4, v57\n\t" \
  "v_min_u32 v57, 71904, v57\n\t" \
  "v_mov_b32 v76, 0\n\t" \
  "v_mov_b32 v77, 0\n\t" \
  "v_mov_b32 v78, 0\n\t" \
  "v_mov_b32 v79, 0\n\t" \
  "ds_write_b128 v57, v[76:79]\n\t" \
  "v_lshrrev_b32 v54, 5, v16\n\t" \
  "v_lshlrev_b32 v56, 1, v54\n\t" \
  "v_xor_b32 v56, v56, v16\n\t" \
  "v_and_b32 v56, 7, v56\n\t" \
  "v_lshlrev_b32 v56, 4, v56\n\t" \
  "v_lshl_add_u32 v56, v54, 7, v56\n\t" \
  "v_bfe_u32 v54, v16, 3, 2\n\t" \
  "v_lshlrev_b32 v54, 2, v54\n\t" \
  "v_sub_u32 v54, 12, v54\n\t" \
  "v_add_u32 v29, v56, v54\n\t" \
  "v_xor_b32 v56, 64, v56\n\t" \
  "v_add_u32 v30, v56, v54\n\t" \
  "s_mov_b32 s46, 0\n\t" \
  "v_cmp_ne_u32 vcc, 0, v19\n\t" \
  "v_add_u32 v169, 50688, v47\n\t" \
  "s_nop 0\n\t" \
  "v_cndmask_b32 v168, 0, -1, vcc\n\t" \
  "ds_read_b128 v[64:67], v45 offset:0\n\t" \
  "v_mul_f32 v194, 0x3fb8aa3b, v41\n\t" \
  "v_rndne_f32 v195, v194\n\t" \
  "v_fma_f32 v196, v41, s63, -v194\n\t" \
  "ds_read_b128 v[68:71], v44 offset:0\n\t" \
  "v_sub_f32 v194, v194, v195\n\t" \
  "v_fmac_f32 v196, 0x32a5705f, v41\n\t" \
  "v_cvt_i32_f32 v195, v195\n\t" \
  "s_add_u32 s49, s60, 0\n\t" \
  "buffer_store_dwordx4 a[64:67], v52, %3, s49 offen sc1\n\t" \
  "ds_read_b128 v[72:75], v44 offset:8448\n\t" \
  "v_add_f32 v194, v194, v196\n\t" \
  "v_exp_f32 v194, v194\n\t" \
  "s_nop 0\n\t" \
  "ds_read_b128 v[76:79], v45 offset:16\n\t" \
  "v_ldexp_f32 v194, v194, v195\n\t" \
  "v_cvt_pk_bf16_f32 v194, v194, v194\n\t" \
  "v_and_b32 v194, 0xffff0000, v194\n\t" \
  "ds_read_b128 v[80:83], v44 offset:16\n\t" \
  "v_mul_f32 v48, v194, v35\n\t" \
  "v_mul_f32 v49, v194, v36\n\t" \
  "v_mul_f32 v195, v194, v37\n\t" \
  "s_add_u32 s49, s60, 73728\n\t" \
  "buffer_store_dwordx4 a[68:71], v52, %3, s49 offen sc1\n\t" \
  "ds_read_b128 v[84:87], v44 offset:8464\n\t" \
  "v_mul_f32 v196, v194, v38\n\t" \
  "v_mul_f32 v48, 0x3fb8aa3b, v48\n\t" \
  "v_mul_f32 v49, 0.15915494, v49\n\t" \
  "ds_read_b128 v[88:91], v45 offset:32\n\t" \
  "v_mul_f32 v50, v39, v195\n\t" \
  "v_mul_f32 v51, v39, v196\n\t" \
  "v_fma_f32 v50, -v40, v196, v50\n\t" \
  "ds_read_b128 v[92:95], v44 offset:32\n\t" \
  "v_fmac_f32 v51, v40, v195\n\t" \
  "v_exp_f32 v194, v48\n\t" \
  "v_fract_f32 v198, v49\n\t" \
  "s_add_u32 s49, s60, 147456\n\t" \
  "buffer_store_dwordx4 a[72:75], v52, %3, s49 offen sc1\n\t" \
  "ds_read_b128 v[96:99], v44 offset:8480\n\t" \
  "v_mul_f32 v195, 0x41000000, v48\n\t" \
  "v_mul_f32 v199, 0xc1000000, v49\n\t" \
  "v_mul_f32 v196, 0x42000000, v48\n\t" \
  "s_waitcnt lgkmcnt(6)\n\t" \
  "v_mfma_f32_16x16x32_bf16 v[170:173], v[68:71], v[64:67], 0\n\t" \
  "v_mfma_f32_16x16x32_bf16 v[174:177], v[72:75], v[64:67], 0\n\t" \
  "ds_read_b128 v[100:103], v45 offset:48\n\t" \
  "v_mul_f32 v200, 0xc2000000, v49\n\t" \
  "v_mul_f32 v197, 0x43800000, v48\n\t" \
  "v_mul_f32 v201, 0x43800000, v49\n\t" \
  "ds_read_b128 v[104:107], v44 offset:48\n\t" \
  "v_fract_f32 v199, v199\n\t" \
  "v_fract_f32 v200, v200\n\t" \
  "v_fract_f32 v201, v201\n\t" \
  "s_add_u32 s49, s60, 221184\n\t" \
  "buffer_store_dwordx4 a[76:79], v52, %3, s49 offen sc1\n\t" \
  "ds_read_b128 v[108:111], v44 offset:8496\n\t" \
  "v_exp_f32 v195, v195\n\t" \
  "v_exp_f32 v196, v196\n\t" \
  "v_exp_f32 v197, v197\n\t" \
  "s_waitcnt lgkmcnt(6)\n\t" \
  "v_mfma_f32_16x16x32_bf16 v[170:173], v[80:83], v[76:79], v[170:173]\n\t" \
  "v_mfma_f32_16x16x32_bf16 v[174:177], v[84:87], v[76:79], v[174:177]\n\t" \
  "ds_read_b128 v[112:115], v45 offset:64\n\t" \
  "v_cos_f32 v178, v198\n\t" \
  "v_sin_f32 v179, v198\n\t" \
  "v_cos_f32 v180, v199\n\t" \
  "ds_read_b128 v[116:119], v44 offset:64\n\t" \
  "v_sin_f32 v181, v199\n\t" \
  "v_cos_f32 v182, v200\n\t" \
  "v_sin_f32 v183, v200\n\t" \
  "s_add_u32 s49, s60, 294912\n\t" \
  "buffer_store_dwordx4 a[80:83], v52, %3, s49 offen sc1\n\t" \
  "ds_read_b128 v[120:123], v44 offset:8512\n\t" \
  "v_cos_f32 v184, v201\n\t" \
  "v_sin_f32 v185, v201\n\t" \
  "s_nop 0\n\t" \
  "s_waitcnt lgkmcnt(6)\n\t" \
  "v_mfma_f32_16x16x32_bf16 v[170:173], v[92:95], v[88:91], v[170:173]\n\t" \
  "v_mfma_f32_16x16x32_bf16 v[174:177], v[96:99], v[88:91], v[174:177]\n\t" \
  "ds_read_b128 v[124:127], v45 offset:80\n\t" \
  "v_mul_f32 v178, v178, v194\n\t" \
  "v_mul_f32 v179, v179, v194\n\t" \
  "v_mul_f32 v180, v180, v195\n\t" \
  "ds_read_b128 v[128:131], v44 offset:80\n\t" \
  "v_mul_f32 v181, v181, v195\n\t" \
  "v_mul_f32 v182, v182, v196\n\t" \
  "v_mul_f32 v183, v183, v196\n\t" \
  "s_add_u32 s49, s60, 368640\n\t" \
  "buffer_store_dwordx4 a[84:87], v52, %3, s49 offen sc1\n\t" \
  "ds_read_b128 v[132:135], v44 offset:8528\n\t" \
  "v_mul_f32 v184, v184, v197\n\t" \
  "v_mul_f32 v185, v185, v197\n\t" \
  "v_xor_b32 v186, 0x80000000, v179\n\t" \
  "s_waitcnt lgkmcnt(6)\n\t" \
  "v_mfma_f32_16x16x32_bf16 v[170:173], v[104:107], v[100:103], v[170:173]\n\t" \
  "v_mfma_f32_16x16x32_bf16 v[174:177], v[108:111], v[100:103], v[174:177]\n\t" \
  "ds_read_b128 v[136:139], v45 offset:96\n\t" \
  "v_mov_b32 v187, v178\n\t" \
  "v_xor_b32 v188, 0x80000000, v181\n\t" \
  "v_mov_b32 v189, v180\n\t" \
  "ds_read_b128 v[140:143], v44 offset:96\n\t" \
  "v_xor_b32 v190, 0x80000000, v183\n\t" \
  "v_mov_b32 v191, v182\n\t" \
  "v_xor_b32 v192, 0x80000000, v185\n\t" \
  "s_add_u32 s49, s60, 442368\n\t" \
  "buffer_store_dwordx4 a[88:91], v52, %3, s49 offen sc1\n\t" \
  "ds_read_b128 v[144:147], v44 offset:8544\n\t" \
  "v_mov_b32 v193, v184\n\t" \
  "s_waitcnt lgkmcnt(6)\n\t" \
  "v_mfma_f32_16x16x32_bf16 v[170:173], v[116:119], v[112:115], v[170:173]\n\t" \
  "v_mfma_f32_16x16x32_bf16 v[174:177], v[120:123], v[112:115], v[174:177]\n\t" \
  "ds_read_b128 v[148:151], v45 offset:112\n\t" \
  "ds_read_b128 v[152:155], v44 offset:112\n\t" \
  "ds_read_b128 v[156:159], v44 offset:8560\n\t" \
  "s_waitcnt lgkmcnt(6)\n\t" \
  "v_mfma_f32_16x16x32_bf16 v[170:173], v[128:131], v[124:127], v[170:173]\n\t" \
  "v_mfma_f32_16x16x32_bf16 v[174:177], v[132:135], v[124:127], v[174:177]\n\t" \
  "s_waitcnt lgkmcnt(3)\n\t" \
  "v_mfma_f32_16x16x32_bf16 v[170:173], v[140:143], v[136:139], v[170:173]\n\t" \
  "v_mfma_f32_16x16x32_bf16 v[174:177], v[144:147], v[136:139], v[174:177]\n\t" \
  "s_waitcnt lgkmcnt(0)\n\t" \
  "v_mfma_f32_16x16x32_bf16 v[170:173], v[152:155], v[148:151], v[170:173]\n\t" \
  "v_mfma_f32_16x16x32_bf16 v[174:177], v[156:159], v[148:151], v[174:177]\n\t" \
  "s_nop 7\n\t" \
  "v_cvt_pk_bf16_f32 v244, v170, v171\n\t" \
  "v_cvt_pk_bf16_f32 v245, v172, v173\n\t" \
  "v_cvt_pk_bf16_f32 v246, v174, v175\n\t" \
  "v_cvt_pk_bf16_f32 v247, v176, v177\n\t" \
  "ds_write_b128 v46, v[244:247]\n\t" \
  "s_waitcnt lgkmcnt(0)\n\t" \
  "s_barrier\n\t" \
  "s_mov_b32 m0, s47\n\t" \
  "v_mov_b32 v122, 0x3f80\n\t" \
  "v_cvt_pk_bf16_f32 v123, v50, v51\n\t" \
  "v_cvt_pk_bf16_f32 v124, v180, v181\n\t" \
  "v_pk_mul_f32 v[106:107], v[180:181], v[180:181] op_sel:[0,0] op_sel_hi:[1,0]\n\t" \
  "ds_write_addtid_b32 v122 offset:11088\n\t" \
  "v_pk_mul_f32 v[108:109], v[178:179], v[50:51] op_sel:[0,0] op_sel_hi:[1,0]\n\t" \
  "v_pk_mul_f32 v[110:111], v[184:185], v[50:51] op_sel:[0,0] op_sel_hi:[1,0]\n\t" \
  "ds_write_addtid_b32 v123 offset:0\n\t" \
  "v_pk_mul_f32 v[112:113], v[182:183], v[180:181] op_sel:[0,0] op_sel_hi:[1,0]\n\t" \
  "v_pk_fma_f32 v[92:93], v[188:189], v[180:181], v[106:107] op_sel:[0,1,0] op_sel_hi:[1,1,1]\n\t" \
  "v_pk_fma_f32 v[96:97], v[186:187], v[50:51], v[108:109] op_sel:[0,1,0] op_sel_hi:[1,1,1]\n\t" \
  "ds_write_addtid_b32 v124 offset:11616\n\t" \
  "v_pk_fma_f32 v[98:99], v[192:193], v[50:51], v[110:111] op_sel:[0,1,0] op_sel_hi:[1,1,1]\n\t" \
  "v_pk_fma_f32 v[90:91], v[190:191], v[180:181], v[112:113] op_sel:[0,1,0] op_sel_hi:[1,1,1]\n\t" \
  "ds_read_u16 v202, v20 offset:7840\n\t" \
  "ds_read_u16 v203, v21 offset:7840\n\t" \
  "s_add_u32 s49, s60, 516096\n\t" \
  "buffer_store_dwordx4 a[92:95], v52, %3, s49 offen sc1\n\t" \
  "v_cvt_pk_bf16_f32 v125, v92, v93\n\t" \
  "v_cvt_pk_bf16_f32 v126, v96, v97\n\t" \
  "v_cvt_pk_bf16_f32 v127, v98, v99\n\t" \
  "v_cvt_pk_bf16_f32 v128, v182, v183\n\t" \
  "v_cvt_pk_bf16_f32 v129, v90, v91\n\t" \
  "v_pk_mul_f32 v[106:107], v[180:181], v[92:93] op_sel:[0,0] op_sel_hi:[1,0]\n\t" \
  "ds_write_addtid_b32 v125 offset:12144\n\t" \
  "v_pk_mul_f32 v[108:109], v[178:179], v[96:97] op_sel:[0,0] op_sel_hi:[1,0]\n\t" \
  "ds_write_addtid_b32 v126 offset:528\n\t" \
  "v_pk_mul_f32 v[110:111], v[184:185], v[98:99] op_sel:[0,0] op_sel_hi:[1,0]\n\t" \
  "v_pk_mul_f32 v[112:113], v[182:183], v[92:93] op_sel:[0,0] op_sel_hi:[1,0]\n\t" \
  "ds_write_addtid_b32 v127 offset:4224\n\t" \
  "v_pk_fma_f32 v[94:95], v[188:189], v[92:93], v[106:107] op_sel:[0,1,0] op_sel_hi:[1,1,1]\n\t" \
  "ds_write_addtid_b32 v128 offset:13200\n\t" \
  "v_pk_fma_f32 v[100:101], v[186:187], v[96:97], v[108:109] op_sel:[0,1,0] op_sel_hi:[1,1,1]\n\t" \
  "v_pk_fma_f32 v[98:99], v[192:193], v[98:99], v[110:111] op_sel:[0,1,0] op_sel_hi:[1,1,1]\n\t" \
  "ds_write_addtid_b32 v129 offset:13728\n\t" \
  "v_pk_fma_f32 v[92:93], v[190:191], v[92:93], v[112:113] op_sel:[0,1,0] op_sel_hi:[1,1,1]\n\t" \
  "ds_read_u16 v204, v22 offset:7840\n\t" \
  "ds_read_u16 v205, v23 offset:7840\n\t" \
  "s_add_u32 s49, s60, 589824\n\t" \
  "buffer_store_dwordx4 a[96:99], v53, %3, s49 offen sc1\n\t" \
  "v_cvt_pk_bf16_f32 v122, v94, v95\n\t" \
  "v_cvt_pk_bf16_f32 v123, v100, v101\n\t" \
  "v_cvt_pk_bf16_f32 v124, v98, v99\n\t" \
  "v_cvt_pk_bf16_f32 v125, v92, v93\n\t" \
  "v_pk_mul_f32 v[106:107], v[182:183], v[94:95] op_sel:[0,0] op_sel_hi:[1,0]\n\t" \
  "ds_write_addtid_b32 v122 offset:12672\n\t" \
  "v_pk_mul_f32 v[108:109], v[182:183], v[182:183] op_sel:[0,0] op_sel_hi:[1,0]\n\t" \
  "v_pk_mul_f32 v[110:111], v[182:183], v[90:91] op_sel:[0,0] op_sel_hi:[1,0]\n\t" \
  "v_pk_mul_f32 v[112:113], v[182:183], v[92:93] op_sel:[0,0] op_sel_hi:[1,0]\n\t" \
  "ds_write_addtid_b32 v123 offset:1056\n\t" \
  "v_pk_mul_f32 v[114:115], v[184:185], v[98:99] op_sel:[0,0] op_sel_hi:[1,0]\n\t" \
  "v_pk_mul_f32 v[116:117], v[178:179], v[100:101] op_sel:[0,0] op_sel_hi:[1,0]\n\t" \
  "v_pk_fma_f32 v[94:95], v[190:191], v[94:95], v[106:107] op_sel:[0,1,0] op_sel_hi:[1,1,1]\n\t" \
  "ds_write_addtid_b32 v124 offset:6336\n\t" \
  "v_pk_fma_f32 v[88:89], v[190:191], v[182:183], v[108:109] op_sel:[0,1,0] op_sel_hi:[1,1,1]\n\t" \
  "v_pk_fma_f32 v[90:91], v[190:191], v[90:91], v[110:111] op_sel:[0,1,0] op_sel_hi:[1,1,1]\n\t" \
  "v_pk_fma_f32 v[92:93], v[190:191], v[92:93], v[112:113] op_sel:[0,1,0] op_sel_hi:[1,1,1]\n\t" \
  "ds_write_addtid_b32 v125 offset:14256\n\t" \
  "v_pk_fma_f32 v[98:99], v[192:193], v[98:99], v[114:115] op_sel:[0,1,0] op_sel_hi:[1,1,1]\n\t" \
  "v_pk_fma_f32 v[96:97], v[186:187], v[100:101], v[116:117] op_sel:[0,1,0] op_sel_hi:[1,1,1]\n\t" \
  "ds_read_u16 v206, v24 offset:7840\n\t" \
  "ds_read_u16 v207, v25 offset:7840\n\t" \
  "s_add_u32 s49, s60, 663552\n\t" \
  "buffer_store_dwordx4 a[100:103], v53, %3, s49 offen sc1\n\t" \
  "v_cvt_pk_bf16_f32 v126, v94, v95\n\t" \
  "v_cvt_pk_bf16_f32 v127, v88, v89\n\t" \
  "v_cvt_pk_bf16_f32 v128, v90, v91\n\t" \
  "v_cvt_pk_bf16_f32 v129, v92, v93\n\t" \
  "v_cvt_pk_bf16_f32 v122, v98, v99\n\t" \
  "v_cvt_pk_bf16_f32 v123, v96, v97\n\t" \
  "v_pk_mul_f32 v[106:107], v[182:183], v[94:95] op_sel:[0,0] op_sel_hi:[1,0]\n\t" \
  "ds_write_addtid_b32 v126 offset:14784\n\t" \
  "v_pk_mul_f32 v[108:109], v[182:183], v[88:89] op_sel:[0,0] op_sel_hi:[1,0]\n\t" \
  "v_pk_mul_f32 v[110:111], v[182:183], v[90:91] op_sel:[0,0] op_sel_hi:[1,0]\n\t" \
  "ds_write_addtid_b32 v127 offset:15312\n\t" \
  "v_pk_mul_f32 v[112:113], v[182:183], v[92:93] op_sel:[0,0] op_sel_hi:[1,0]\n\t" \
  "v_pk_mul_f32 v[114:115], v[184:185], v[98:99] op_sel:[0,0] op_sel_hi:[1,0]\n\t" \
  "ds_write_addtid_b32 v128 offset:15840\n\t" \
  "v_pk_mul_f32 v[116:117], v[178:179], v[96:97] op_sel:[0,0] op_sel_hi:[1,0]\n\t" \
  "v_pk_mul_f32 v[118:119], v[184:185], v[100:101] op_sel:[0,0] op_sel_hi:[1,0]\n\t" \
  "v_pk_fma_f32 v[94:95], v[190:191], v[94:95], v[106:107] op_sel:[0,1,0] op_sel_hi:[1,1,1]\n\t" \
  "ds_write_addtid_b32 v129 offset:16368\n\t" \
  "v_pk_fma_f32 v[88:89], v[190:191], v[88:89], v[108:109] op_sel:[0,1,0] op_sel_hi:[1,1,1]\n\t" \
  "v_pk_fma_f32 v[90:91], v[190:191], v[90:91], v[110:111] op_sel:[0,1,0] op_sel_hi:[1,1,1]\n\t" \
  "ds_write_addtid_b32 v122 offset:7392\n\t" \
  "v_pk_fma_f32 v[92:93], v[190:191], v[92:93], v[112:113] op_sel:[0,1,0] op_sel_hi:[1,1,1]\n\t" \
  "v_pk_fma_f32 v[98:99], v[192:193], v[98:99], v[114:115] op_sel:[0,1,0] op_sel_hi:[1,1,1]\n\t" \
  "ds_write_addtid_b32 v123 offset:1584\n\t" \
  "v_pk_fma_f32 v[102:103], v[186:187], v[96:97], v[116:117] op_sel:[0,1,0] op_sel_hi:[1,1,1]\n\t" \
  "v_pk_fma_f32 v[100:101], v[192:193], v[100:101], v[118:119] op_sel:[0,1,0] op_sel_hi:[1,1,1]\n\t" \
  "ds_read_u16 v208, v26 offset:7840\n\t" \
  "ds_read_u16 v209, v27 offset:7840\n\t" \
  "v_cvt_pk_bf16_f32 v124, v94, v95\n\t" \
  "v_cvt_pk_bf16_f32 v125, v88, v89\n\t" \
  "v_cvt_pk_bf16_f32 v126, v90, v91\n\t" \
  "v_cvt_pk_bf16_f32 v127, v92, v93\n\t" \
  "v_cvt_pk_bf16_f32 v128, v98, v99\n\t" \
  "v_cvt_pk_bf16_f32 v129, v102, v103\n\t" \
  "v_cvt_pk_bf16_f32 v122, v100, v101\n\t" \
  "v_pk_mul_f32 v[106:107], v[182:183], v[94:95] op_sel:[0,0] op_sel_hi:[1,0]\n\t" \
  "ds_write_addtid_b32 v124 offset:16896\n\t" \
  "v_pk_mul_f32 v[108:109], v[182:183], v[88:89] op_sel:[0,0] op_sel_hi:[1,0]\n\t" \
  "ds_write_addtid_b32 v125 offset:17424\n\t" \
  "v_pk_mul_f32 v[110:111], v[182:183], v[90:91] op_sel:[0,0] op_sel_hi:[1,0]\n\t" \
  "v_pk_mul_f32 v[112:113], v[182:183], v[92:93] op_sel:[0,0] op_sel_hi:[1,0]\n\t" \
  "ds_write_addtid_b32 v126 offset:17952\n\t" \
  "v_pk_mul_f32 v[114:115], v[184:185], v[98:99] op_sel:[0,0] op_sel_hi:[1,0]\n\t" \
  "v_pk_mul_f32 v[116:117], v[178:179], v[102:103] op_sel:[0,0] op_sel_hi:[1,0]\n\t" \
  "ds_write_addtid_b32 v127 offset:18480\n\t" \
  "v_pk_fma_f32 v[94:95], v[190:191], v[94:95], v[106:107] op_sel:[0,1,0] op_sel_hi:[1,1,1]\n\t" \
  "ds_write_addtid_b32 v128 offset:8448\n\t" \
  "v_pk_fma_f32 v[88:89], v[190:191], v[88:89], v[108:109] op_sel:[0,1,0] op_sel_hi:[1,1,1]\n\t" \
  "v_pk_fma_f32 v[90:91], v[190:191], v[90:91], v[110:111] op_sel:[0,1,0] op_sel_hi:[1,1,1]\n\t" \
  "ds_write_addtid_b32 v129 offset:2112\n\t" \
  "v_pk_fma_f32 v[92:93], v[190:191], v[92:93], v[112:113] op_sel:[0,1,0] op_sel_hi:[1,1,1]\n\t" \
  "v_pk_fma_f32 v[98:99], v[192:193], v[98:99], v[114:115] op_sel:[0,1,0] op_sel_hi:[1,1,1]\n\t" \
  "ds_write_addtid_b32 v122 offset:4752\n\t" \
  "v_pk_fma_f32 v[96:97], v[186:187], v[102:103], v[116:117] op_sel:[0,1,0] op_sel_hi:[1,1,1]\n\t" \
  "ds_read_u16 v210, v28 offset:7840\n\t" \
  "ds_read_u16 v211, v20 offset:9888\n\t" \
  "v_cvt_pk_bf16_f32 v123, v94, v95\n\t" \
  "v_cvt_pk_bf16_f32 v124, v88, v89\n\t" \
  "v_cvt_pk_bf16_f32 v125, v90, v91\n\t" \
  "v_cvt_pk_bf16_f32 v126, v92, v93\n\t" \
  "v_cvt_pk_bf16_f32 v127, v98, v99\n\t" \
  "v_cvt_pk_bf16_f32 v128, v96, v97\n\t" \
  "v_pk_mul_f32 v[106:107], v[182:183], v[94:95] op_sel:[0,0] op_sel_hi:[1,0]\n\t" \
  "ds_write_addtid_b32 v123 offset:19008\n\t" \
  "v_pk_mul_f32 v[108:109], v[182:183], v[88:89] op_sel:[0,0] op_sel_hi:[1,0]\n\t" \
  "v_pk_mul_f32 v[110:111], v[182:183], v[90:91] op_sel:[0,0] op_sel_hi:[1,0]\n\t" \
  "ds_write_addtid_b32 v124 offset:19536\n\t" \
  "v_pk_mul_f32 v[112:113], v[182:183], v[92:93] op_sel:[0,0] op_sel_hi:[1,0]\n\t" \
  "v_pk_mul_f32 v[114:115], v[184:185], v[98:99] op_sel:[0,0] op_sel_hi:[1,0]\n\t" \
  "ds_write_addtid_b32 v125 offset:20064\n\t" \
  "v_pk_mul_f32 v[116:117], v[178:179], v[96:97] op_sel:[0,0] op_sel_hi:[1,0]\n\t" \
  "v_pk_mul_f32 v[118:119], v[184:185], v[102:103] op_sel:[0,0] op_sel_hi:[1,0]\n\t" \
  "v_pk_fma_f32 v[94:95], v[190:191], v[94:95], v[106:107] op_sel:[0,1,0] op_sel_hi:[1,1,1]\n\t" \
  "ds_write_addtid_b32 v126 offset:20592\n\t" \
  "v_pk_fma_f32 v[88:89], v[190:191], v[88:89], v[108:109] op_sel:[0,1,0] op_sel_hi:[1,1,1]\n\t" \
  "v_pk_fma_f32 v[90:91], v[190:191], v[90:91], v[110:111] op_sel:[0,1,0] op_sel_hi:[1,1,1]\n\t" \
  "ds_write_addtid_b32 v127 offset:8976\n\t" \
  "v_pk_fma_f32 v[92:93], v[190:191], v[92:93], v[112:113] op_sel:[0,1,0] op_sel_hi:[1,1,1]\n\t" \
  "v_pk_fma_f32 v[98:99], v[192:193], v[98:99], v[114:115] op_sel:[0,1,0] op_sel_hi:[1,1,1]\n\t" \
  "ds_write_addtid_b32 v128 offset:2640\n\t" \
  "v_pk_fma_f32 v[104:105], v[186:187], v[96:97], v[116:117] op_sel:[0,1,0] op_sel_hi:[1,1,1]\n\t" \
  "v_pk_fma_f32 v[102:103], v[192:193], v[102:103], v[118:119] op_sel:[0,1,0] op_sel_hi:[1,1,1]\n\t" \
  "ds_read_u16 v212, v21 offset:9888\n\t" \
  "ds_read_u16 v213, v22 offset:9888\n\t" \
  "v_cvt_pk_bf16_f32 v129, v94, v95\n\t" \
  "v_cvt_pk_bf16_f32 v122, v88, v89\n\t" \
  "v_cvt_pk_bf16_f32 v123, v90, v91\n\t" \
  "v_cvt_pk_bf16_f32 v124, v92, v93\n\t" \
  "v_cvt_pk_bf16_f32 v125, v98, v99\n\t" \
  "v_cvt_pk_bf16_f32 v126, v104, v105\n\t" \
  "v_cvt_pk_bf16_f32 v127, v102, v103\n\t" \
  "v_pk_mul_f32 v[106:107], v[182:183], v[94:95] op_sel:[0,0] op_sel_hi:[1,0]\n\t" \
  "ds_write_addtid_b32 v129 offset:21120\n\t" \
  "v_pk_mul_f32 v[108:109], v[182:183], v[88:89] op_sel:[0,0] op_sel_hi:[1,0]\n\t" \
  "v_pk_mul_f32 v[110:111], v[182:183], v[90:91] op_sel:[0,0] op_sel_hi:[1,0]\n\t" \
  "ds_write_addtid_b32 v122 offset:21648\n\t" \
  "v_pk_mul_f32 v[112:113], v[182:183], v[92:93] op_sel:[0,0] op_sel_hi:[1,0]\n\t" \
  "v_pk_mul_f32 v[114:115], v[184:185], v[98:99] op_sel:[0,0] op_sel_hi:[1,0]\n\t" \
  "ds_write_addtid_b32 v123 offset:22176\n\t" \
  "v_pk_mul_f32 v[116:117], v[178:179], v[104:105] op_sel:[0,0] op_sel_hi:[1,0]\n\t" \
  "v_pk_mul_f32 v[118:119], v[184:185], v[102:103] op_sel:[0,0] op_sel_hi:[1,0]\n\t" \
  "ds_write_addtid_b32 v124 offset:22704\n\t" \
  "v_pk_fma_f32 v[94:95], v[190:191], v[94:95], v[106:107] op_sel:[0,1,0] op_sel_hi:[1,1,1]\n\t" \
  "v_pk_fma_f32 v[88:89], v[190:191], v[88:89], v[108:109] op_sel:[0,1,0] op_sel_hi:[1,1,1]\n\t" \
  "ds_write_addtid_b32 v125 offset:9504\n\t" \
  "v_pk_fma_f32 v[90:91], v[190:191], v[90:91], v[110:111] op_sel:[0,1,0] op_sel_hi:[1,1,1]\n\t" \
  "v_pk_fma_f32 v[92:93], v[190:191], v[92:93], v[112:113] op_sel:[0,1,0] op_sel_hi:[1,1,1]\n\t" \
  "ds_write_addtid_b32 v126 offset:3168\n\t" \
  "v_pk_fma_f32 v[98:99], v[192:193], v[98:99], v[114:115] op_sel:[0,1,0] op_sel_hi:[1,1,1]\n\t" \
  "v_pk_fma_f32 v[96:97], v[186:187], v[104:105], v[116:117] op_sel:[0,1,0] op_sel_hi:[1,1,1]\n\t" \
  "ds_write_addtid_b32 v127 offset:5280\n\t" \
  "v_pk_fma_f32 v[102:103], v[192:193], v[102:103], v[118:119] op_sel:[0,1,0] op_sel_hi:[1,1,1]\n\t" \
  "ds_read_u16 v214, v23 offset:9888\n\t" \
  "ds_read_u16 v215, v24 offset:9888\n\t" \
  "v_cvt_pk_bf16_f32 v128, v94, v95\n\t" \
  "v_cvt_pk_bf16_f32 v129, v88, v89\n\t" \
  "v_cvt_pk_bf16_f32 v122, v90, v91\n\t" \
  "v_cvt_pk_bf16_f32 v123, v92, v93\n\t" \
  "v_cvt_pk_bf16_f32 v124, v98, v99\n\t" \
  "v_cvt_pk_bf16_f32 v125, v96, v97\n\t" \
  "v_cvt_pk_bf16_f32 v126, v102, v103\n\t" \
  "v_pk_mul_f32 v[106:107], v[182:183], v[94:95] op_sel:[0,0] op_sel_hi:[1,0]\n\t" \
  "ds_write_addtid_b32 v128 offset:23232\n\t" \
  "v_pk_mul_f32 v[108:109], v[182:183], v[88:89] op_sel:[0,0] op_sel_hi:[1,0]\n\t" \
  "v_pk_mul_f32 v[110:111], v[182:183], v[90:91] op_sel:[0,0] op_sel_hi:[1,0]\n\t" \
  "ds_write_addtid_b32 v129 offset:23760\n\t" \
  "v_pk_mul_f32 v[112:113], v[182:183], v[92:93] op_sel:[0,0] op_sel_hi:[1,0]\n\t" \
  "v_pk_mul_f32 v[114:115], v[184:185], v[98:99] op_sel:[0,0] op_sel_hi:[1,0]\n\t" \
  "ds_write_addtid_b32 v122 offset:24288\n\t" \
  "v_pk_mul_f32 v[116:117], v[184:185], v[102:103] op_sel:[0,0] op_sel_hi:[1,0]\n\t" \
  "v_pk_mul_f32 v[118:119], v[184:185], v[104:105] op_sel:[0,0] op_sel_hi:[1,0]\n\t" \
  "ds_write_addtid_b32 v123 offset:24816\n\t" \
  "v_pk_fma_f32 v[94:95], v[190:191], v[94:95], v[106:107] op_sel:[0,1,0] op_sel_hi:[1,1,1]\n\t" \
  "v_pk_fma_f32 v[88:89], v[190:191], v[88:89], v[108:109] op_sel:[0,1,0] op_sel_hi:[1,1,1]\n\t" \
  "ds_write_addtid_b32 v124 offset:10032\n\t" \
  "v_pk_fma_f32 v[90:91], v[190:191], v[90:91], v[110:111] op_sel:[0,1,0] op_sel_hi:[1,1,1]\n\t" \
  "v_pk_fma_f32 v[92:93], v[190:191], v[92:93], v[112:113] op_sel:[0,1,0] op_sel_hi:[1,1,1]\n\t" \
  "ds_write_addtid_b32 v125 offset:3696\n\t" \
  "v_pk_fma_f32 v[98:99], v[192:193], v[98:99], v[114:115] op_sel:[0,1,0] op_sel_hi:[1,1,1]\n\t" \
  "v_pk_fma_f32 v[102:103], v[192:193], v[102:103], v[116:117] op_sel:[0,1,0] op_sel_hi:[1,1,1]\n\t" \
  "ds_write_addtid_b32 v126 offset:6864\n\t" \
  "v_pk_fma_f32 v[104:105], v[192:193], v[104:105], v[118:119] op_sel:[0,1,0] op_sel_hi:[1,1,1]\n\t" \
  "ds_read_u16 v216, v25 offset:9888\n\t" \
  "ds_read_u16 v217, v26 offset:9888\n\t" \
  "v_cvt_pk_bf16_f32 v127, v94, v95\n\t" \
  "v_cvt_pk_bf16_f32 v128, v88, v89\n\t" \
  "v_cvt_pk_bf16_f32 v129, v90, v91\n\t" \
  "v_cvt_pk_bf16_f32 v122, v92, v93\n\t" \
  "v_cvt_pk_bf16_f32 v123, v98, v99\n\t" \
  "v_cvt_pk_bf16_f32 v124, v102, v103\n\t" \
  "v_cvt_pk_bf16_f32 v125, v104, v105\n\t" \
  "v_pk_mul_f32 v[106:107], v[182:183], v[94:95] op_sel:[0,0] op_sel_hi:[1,0]\n\t" \
  "ds_write_addtid_b32 v127 offset:25344\n\t" \
  "ds_write_addtid_b32 v128 offset:25872\n\t" \
  "ds_write_addtid_b32 v129 offset:26400\n\t" \
  "ds_write_addtid_b32 v122 offset:26928\n\t" \
  "v_pk_fma_f32 v[94:95], v[190:191], v[94:95], v[106:107] op_sel:[0,1,0] op_sel_hi:[1,1,1]\n\t" \
  "ds_write_addtid_b32 v123 offset:10560\n\t" \
  "ds_write_addtid_b32 v124 offset:7920\n\t" \
  "ds_write_addtid_b32 v125 offset:5808\n\t" \
  "ds_read_u16 v218, v27 offset:9888\n\t" \
  "ds_read_u16 v219, v28 offset:9888\n\t" \
  "v_cvt_pk_bf16_f32 v126, v94, v95\n\t" \
  "s_nop 0\n\t" \
  "ds_write_addtid_b32 v126 offset:27456\n\t" \
  "v_mov_b32 v35, v227\n\t" \
  "v_mov_b32 v36, v228\n\t" \
  "v_mov_b32 v37, v229\n\t" \
  "v_mov_b32 v38, v230\n\t" \
  "v_mov_b32 v39, v231\n\t" \
  "v_mov_b32 v40, v232\n\t" \
  "v_mov_b32 v41, v233\n\t" \
  "s_lshl_b32 s43, s40, 7\n\t" \
  "s_add_u32 s43, s43, 32768\n\t" \
  "s_waitcnt vmcnt(0)\n\t" \
  "s_waitcnt lgkmcnt(0)\n\t" \
  "s_barrier\n\t" \
  "buffer_store_dwordx2 v[32:33], v34, %3, s43 offen sc1\n\t" \
  "s_mul_i32 s43, s40, 12\n\t" \
  "v_min_u32 v240, 11, v16\n\t" \
  "v_add_u32 v240, s43, v240\n\t" \
  "v_lshlrev_b32 v240, 6, v240\n\t" \
  "v_add_u32 v240, 917504, v240\n\t" \
  "buffer_load_dwordx2 v[236:237], v240, %3, 0 offen sc1\n\t" \
  "s_mul_i32 s43, %12, 768\n\t" \
  "v_add_u32 v54, s43, v19\n\t" \
  "s_mul_i32 s43, %11, 663552\n\t" \
  "v_min_u32 v55, 5183, v54\n\t" \
  "v_lshlrev_b32 v55, 7, v55\n\t" \
  "buffer_load_dword v63, v55, %2, s43 offen\n\t" \
  "v_add_u32 v54, 256, v54\n\t" \
  "v_min_u32 v55, 5183, v54\n\t" \
  "v_lshlrev_b32 v55, 7, v55\n\t" \
  "buffer_load_dword v63, v55, %2, s43 offen\n\t" \
  "v_add_u32 v54, 256, v54\n\t" \
  "v_min_u32 v55, 5183, v54\n\t" \
  "v_lshlrev_b32 v55, 7, v55\n\t" \
  "buffer_load_dword v63, v55, %2, s43 offen\n\t" \
  "v_add_u32 v54, 256, v54\n\t" \
  "ds_read_b128 v[64:67], v45 offset:0\n\t" \
  "v_lshl_or_b32 v164, v204, 16, v203\n\t" \
  "v_lshl_or_b32 v165, v206, 16, v205\n\t" \
  "v_lshl_or_b32 v166, v208, 16, v207\n\t" \
  "v_lshl_or_b32 v167, v210, 16, v209\n\t" \
  "ds_read_b128 v[68:71], v44 offset:0\n\t" \
  "v_and_b32 v202, v202, v168\n\t" \
  "ds_write_b128 v47, v[164:167] offset:4224\n\t" \
  "v_mul_f32 v194, 0x3fb8aa3b, v41\n\t" \
  "v_rndne_f32 v195, v194\n\t" \
  "ds_read_b128 v[72:75], v44 offset:8448\n\t" \
  "v_fma_f32 v196, v41, s63, -v194\n\t" \
  "v_sub_f32 v194, v194, v195\n\t" \
  "v_fmac_f32 v196, 0x32a5705f, v41\n\t" \
  "v_cvt_i32_f32 v195, v195\n\t" \
  "ds_read_b128 v[76:79], v45 offset:16\n\t" \
  "v_add_f32 v194, v194, v196\n\t" \
  "v_exp_f32 v194, v194\n\t" \
  "s_nop 0\n\t" \
  "v_ldexp_f32 v194, v194, v195\n\t" \
  "ds_read_b128 v[80:83], v44 offset:16\n\t" \
  "v_cvt_pk_bf16_f32 v194, v194, v194\n\t" \
  "v_and_b32 v194, 0xffff0000, v194\n\t" \
  "v_alignbit_b32 v161, v165, v164, 16\n\t" \
  "v_alignbit_b32 v162, v166, v165, 16\n\t" \
  "ds_read_b128 v[84:87], v44 offset:8464\n\t" \
  "v_alignbit_b32 v163, v167, v166, 16\n\t" \
  "v_lshl_or_b32 v160, v203, 16, v202\n\t" \
  "ds_write_b128 v47, v[160:163] offset:0\n\t" \
  "v_mul_f32 v48, v194, v35\n\t" \
  "ds_read_b128 v[88:91], v45 offset:32\n\t" \
  "v_mul_f32 v49, v194, v36\n\t" \
  "v_mul_f32 v195, v194, v37\n\t" \
  "v_mul_f32 v196, v194, v38\n\t" \
  "v_mul_f32 v48, 0x3fb8aa3b, v48\n\t" \
  "ds_read_b128 v[92:95], v44 offset:32\n\t" \
  "v_mul_f32 v49, 0.15915494, v49\n\t" \
  "v_mul_f32 v50, v39, v195\n\t" \
  "v_mul_f32 v51, v39, v196\n\t" \
  "v_fma_f32 v50, -v40, v196, v50\n\t" \
  "ds_read_b128 v[96:99], v44 offset:8480\n\t" \
  "v_fmac_f32 v51, v40, v195\n\t" \
  "v_exp_f32 v194, v48\n\t" \
  "v_fract_f32 v198, v49\n\t" \
  "v_lshl_or_b32 v164, v213, 16, v212\n\t" \
  "s_waitcnt lgkmcnt(3)\n\t" \
  "v_mfma_f32_16x16x32_bf16 v[170:173], v[68:71], v[64:67], 0\n\t" \
  "v_mfma_f32_16x16x32_bf16 v[174:177], v[72:75], v[64:67], 0\n\t" \
  "ds_read_b128 v[100:103], v45 offset:48\n\t" \
  "v_lshl_or_b32 v165, v215, 16, v214\n\t" \
  "v_lshl_or_b32 v166, v217, 16, v216\n\t" \
  "v_lshl_or_b32 v167, v219, 16, v218\n\t" \
  "v_and_b32 v211, v211, v168\n\t" \
  "ds_read_b128 v[104:107], v44 offset:48\n\t" \
  "ds_write_b128 v47, v[164:167] offset:12672\n\t" \
  "v_mul_f32 v195, 0x41000000, v48\n\t" \
  "v_mul_f32 v199, 0xc1000000, v49\n\t" \
  "v_mul_f32 v196, 0x42000000, v48\n\t" \
  "ds_read_b128 v[108:111], v44 offset:8496\n\t" \
  "v_mul_f32 v200, 0xc2000000, v49\n\t" \
  "v_mul_f32 v197, 0x43800000, v48\n\t" \
  "v_mul_f32 v201, 0x43800000, v49\n\t" \
  "v_fract_f32 v199, v199\n\t" \
  "s_waitcnt lgkmcnt(1)\n\t" \
  "v_mfma_f32_16x16x32_bf16 v[170:173], v[80:83], v[76:79], v[170:173]\n\t" \
  "v_mfma_f32_16x16x32_bf16 v[174:177], v[84:87], v[76:79], v[174:177]\n\t" \
  "ds_read_b128 v[112:115], v45 offset:64\n\t" \
  "v_fract_f32 v200, v200\n\t" \
  "v_fract_f32 v201, v201\n\t" \
  "v_exp_f32 v195, v195\n\t" \
  "v_exp_f32 v196, v196\n\t" \
  "ds_read_b128 v[116:119], v44 offset:64\n\t" \
  "v_exp_f32 v197, v197\n\t" \
  "v_alignbit_b32 v161, v165, v164, 16\n\t" \
  "v_alignbit_b32 v162, v166, v165, 16\n\t" \
  "v_alignbit_b32 v163, v167, v166, 16\n\t" \
  "ds_read_b128 v[120:123], v44 offset:8512\n\t" \
  "v_lshl_or_b32 v160, v212, 16, v211\n\t" \
  "ds_write_b128 v47, v[160:163] offset:8448\n\t" \
  "v_cos_f32 v178, v198\n\t" \
  "v_sin_f32 v179, v198\n\t" \
  "s_waitcnt lgkmcnt(0)\n\t" \
  "v_mfma_f32_16x16x32_bf16 v[170:173], v[92:95], v[88:91], v[170:173]\n\t" \
  "v_mfma_f32_16x16x32_bf16 v[174:177], v[96:99], v[88:91], v[174:177]\n\t" \
  "ds_read_b128 v[124:127], v45 offset:80\n\t" \
  "v_cos_f32 v180, v199\n\t" \
  "v_sin_f32 v181, v199\n\t" \
  "v_cos_f32 v182, v200\n\t" \
  "v_sin_f32 v183, v200\n\t" \
  "ds_read_b128 v[128:131], v44 offset:80\n\t" \
  "v_cos_f32 v184, v201\n\t" \
  "v_sin_f32 v185, v201\n\t" \
  "s_nop 0\n\t" \
  "v_mul_f32 v178, v178, v194\n\t" \
  "ds_read_b128 v[132:135], v44 offset:8528\n\t" \
  "v_mul_f32 v179, v179, v194\n\t" \
  "v_mul_f32 v180, v180, v195\n\t" \
  "v_mul_f32 v181, v181, v195\n\t" \
  "v_mul_f32 v182, v182, v196\n\t" \
  "v_mfma_f32_16x16x32_bf16 v[170:173], v[104:107], v[100:103], v[170:173]\n\t" \
  "v_mfma_f32_16x16x32_bf16 v[174:177], v[108:111], v[100:103], v[174:177]\n\t" \
  "ds_read_b128 v[136:139], v45 offset:96\n\t" \
  "v_mul_f32 v183, v183, v196\n\t" \
  "v_mul_f32 v184, v184, v197\n\t" \
  "v_mul_f32 v185, v185, v197\n\t" \
  "v_xor_b32 v186, 0x80000000, v179\n\t" \
  "ds_read_b128 v[140:143], v44 offset:96\n\t" \
  "v_mov_b32 v187, v178\n\t" \
  "v_xor_b32 v188, 0x80000000, v181\n\t" \
  "v_mov_b32 v189, v180\n\t" \
  "v_xor_b32 v190, 0x80000000, v183\n\t" \
  "ds_read_b128 v[144:147], v44 offset:8544\n\t" \
  "v_mov_b32 v191, v182\n\t" \
  "v_xor_b32 v192, 0x80000000, v185\n\t" \
  "v_mov_b32 v193, v184\n\t" \
  "v_mfma_f32_16x16x32_bf16 v[170:173], v[116:119], v[112:115], v[170:173]\n\t" \
  "v_mfma_f32_16x16x32_bf16 v[174:177], v[120:123], v[112:115], v[174:177]\n\t" \
  "ds_read_b128 v[148:151], v45 offset:112\n\t" \
  "ds_read_b128 v[152:155], v44 offset:112\n\t" \
  "ds_read_b128 v[156:159], v44 offset:8560\n\t" \
  "s_waitcnt lgkmcnt(6)\n\t" \
  "v_mfma_f32_16x16x32_bf16 v[170:173], v[128:131], v[124:127], v[170:173]\n\t" \
  "v_mfma_f32_16x16x32_bf16 v[174:177], v[132:135], v[124:127], v[174:177]\n\t" \
  "s_waitcnt lgkmcnt(3)\n\t" \
  "v_mfma_f32_16x16x32_bf16 v[170:173], v[140:143], v[136:139], v[170:173]\n\t" \
  "v_mfma_f32_16x16x32_bf16 v[174:177], v[144:147], v[136:139], v[174:177]\n\t" \
  "s_waitcnt lgkmcnt(0)\n\t" \
  "v_mfma_f32_16x16x32_bf16 v[170:173], v[152:155], v[148:151], v[170:173]\n\t" \
  "v_mfma_f32_16x16x32_bf16 v[174:177], v[156:159], v[148:151], v[174:177]\n\t" \
  "s_nop 7\n\t" \
  "v_cvt_pk_bf16_f32 v244, v170, v171\n\t" \
  "v_cvt_pk_bf16_f32 v245, v172, v173\n\t" \
  "v_cvt_pk_bf16_f32 v246, v174, v175\n\t" \
  "v_cvt_pk_bf16_f32 v247, v176, v177\n\t" \
  "ds_write_b128 v46, v[244:247]\n\t" \
  "s_waitcnt vmcnt(3)\n\t" \
  "v_cmp_eq_u64 vcc, v[32:33], v[236:237]\n\t" \
  "s_nop 1\n\t" \
  "s_cmp_eq_u64 vcc, -1\n\t" \
  "s_cbranch_scc1 L_ck2_%=\n\t" \
  "s_mov_b32 s51, 0\n\t" \
  "L_sp2_%=:\n\t" \
  "buffer_load_dwordx2 v[56:57], v240, %3, 0 offen sc1\n\t" \
  "s_waitcnt vmcnt(0)\n\t" \
  "v_cmp_eq_u64 vcc, v[32:33], v[56:57]\n\t" \
  "s_add_u32 s51, s51, 1\n\t" \
  "s_nop 1\n\t" \
  "s_cmp_eq_u64 vcc, -1\n\t" \
  "s_cbranch_scc1 L_ok2_%=\n\t" \
  "s_sleep 1\n\t" \
  "s_cmp_lt_u32 s51, 0x40000\n\t" \
  "s_cbranch_scc1 L_sp2_%=\n\t" \
  "L_ok2_%=:\n\t" \
  "L_ck2_%=:\n\t" \
  "s_min_u32 s43, %13, 2\n\t" \
  "s_mul_i32 s45, s40, 3\n\t" \
  "s_add_u32 s45, s45, s43\n\t" \
  "s_lshl_b32 s45, s45, 11\n\t" \
  "s_add_u32 s45, s45, 4194304\n\t" \
  "v_lshlrev_b32 v241, 4, v16\n\t" \
  "buffer_load_dwordx4 a[104:107], v241, %3, s45 offen sc1\n\t" \
  "buffer_load_dwordx4 a[108:111], v241, %3, s45 offen offset:1024 sc1\n\t" \
  "s_lshl_b32 s43, s43, 11\n\t" \
  "s_add_u32 s43, s43, 139872\n\t" \
  "v_add_u32 v241, s43, v241\n\t" \
  "s_waitcnt lgkmcnt(0)\n\t" \
  "s_barrier\n\t" \
  "s_mov_b32 m0, s47\n\t" \
  "v_mov_b32 v122, 0x3f80\n\t" \
  "v_cvt_pk_bf16_f32 v123, v50, v51\n\t" \
  "v_cvt_pk_bf16_f32 v124, v180, v181\n\t" \
  "v_pk_mul_f32 v[106:107], v[180:181], v[180:181] op_sel:[0,0] op_sel_hi:[1,0]\n\t" \
  "ds_write_addtid_b32 v122 offset:11088\n\t" \
  "v_pk_mul_f32 v[108:109], v[178:179], v[50:51] op_sel:[0,0] op_sel_hi:[1,0]\n\t" \
  "v_pk_mul_f32 v[110:111], v[184:185], v[50:51] op_sel:[0,0] op_sel_hi:[1,0]\n\t" \
  "ds_write_addtid_b32 v123 offset:0\n\t" \
  "v_pk_mul_f32 v[112:113], v[182:183], v[180:181] op_sel:[0,0] op_sel_hi:[1,0]\n\t" \
  "v_pk_fma_f32 v[92:93], v[188:189], v[180:181], v[106:107] op_sel:[0,1,0] op_sel_hi:[1,1,1]\n\t" \
  "v_pk_fma_f32 v[96:97], v[186:187], v[50:51], v[108:109] op_sel:[0,1,0] op_sel_hi:[1,1,1]\n\t" \
  "ds_write_addtid_b32 v124 offset:11616\n\t" \
  "v_pk_fma_f32 v[98:99], v[192:193], v[50:51], v[110:111] op_sel:[0,1,0] op_sel_hi:[1,1,1]\n\t" \
  "v_pk_fma_f32 v[90:91], v[190:191], v[180:181], v[112:113] op_sel:[0,1,0] op_sel_hi:[1,1,1]\n\t" \
  "ds_read_u16 v202, v20 offset:7840\n\t" \
  "ds_read_u16 v203, v21 offset:7840\n\t" \
  "v_cvt_pk_bf16_f32 v125, v92, v93\n\t" \
  "v_cvt_pk_bf16_f32 v126, v96, v97\n\t" \
  "v_cvt_pk_bf16_f32 v127, v98, v99\n\t" \
  "v_cvt_pk_bf16_f32 v128, v182, v183\n\t" \
  "v_cvt_pk_bf16_f32 v129, v90, v91\n\t" \
  "v_pk_mul_f32 v[106:107], v[180:181], v[92:93] op_sel:[0,0] op_sel_hi:[1,0]\n\t" \
  "ds_write_addtid_b32 v125 offset:12144\n\t" \
  "v_pk_mul_f32 v[108:109], v[178:179], v[96:97] op_sel:[0,0] op_sel_hi:[1,0]\n\t" \
  "ds_write_addtid_b32 v126 offset:528\n\t" \
  "v_pk_mul_f32 v[110:111], v[184:185], v[98:99] op_sel:[0,0] op_sel_hi:[1,0]\n\t" \
  "v_pk_mul_f32 v[112:113], v[182:183], v[92:93] op_sel:[0,0] op_sel_hi:[1,0]\n\t" \
  "ds_write_addtid_b32 v127 offset:4224\n\t" \
  "v_pk_fma_f32 v[94:95], v[188:189], v[92:93], v[106:107] op_sel:[0,1,0] op_sel_hi:[1,1,1]\n\t" \
  "ds_write_addtid_b32 v128 offset:13200\n\t" \
  "v_pk_fma_f32 v[100:101], v[186:187], v[96:97], v[108:109] op_sel:[0,1,0] op_sel_hi:[1,1,1]\n\t" \
  "v_pk_fma_f32 v[98:99], v[192:193], v[98:99], v[110:111] op_sel:[0,1,0] op_sel_hi:[1,1,1]\n\t" \
  "ds_write_addtid_b32 v129 offset:13728\n\t" \
  "v_pk_fma_f32 v[92:93], v[190:191], v[92:93], v[112:113] op_sel:[0,1,0] op_sel_hi:[1,1,1]\n\t" \
  "ds_read_u16 v204, v22 offset:7840\n\t" \
  "ds_read_u16 v205, v23 offset:7840\n\t" \
  "v_cvt_pk_bf16_f32 v122, v94, v95\n\t" \
  "v_cvt_pk_bf16_f32 v123, v100, v101\n\t" \
  "v_cvt_pk_bf16_f32 v124, v98, v99\n\t" \
  "v_cvt_pk_bf16_f32 v125, v92, v93\n\t" \
  "v_pk_mul_f32 v[106:107], v[182:183], v[94:95] op_sel:[0,0] op_sel_hi:[1,0]\n\t" \
  "ds_write_addtid_b32 v122 offset:12672\n\t" \
  "v_pk_mul_f32 v[108:109], v[182:183], v[182:183] op_sel:[0,0] op_sel_hi:[1,0]\n\t" \
  "v_pk_mul_f32 v[110:111], v[182:183], v[90:91] op_sel:[0,0] op_sel_hi:[1,0]\n\t" \
  "v_pk_mul_f32 v[112:113], v[182:183], v[92:93] op_sel:[0,0] op_sel_hi:[1,0]\n\t" \
  "ds_write_addtid_b32 v123 offset:1056\n\t" \
  "v_pk_mul_f32 v[114:115], v[184:185], v[98:99] op_sel:[0,0] op_sel_hi:[1,0]\n\t" \
  "v_pk_mul_f32 v[116:117], v[178:179], v[100:101] op_sel:[0,0] op_sel_hi:[1,0]\n\t" \
  "v_pk_fma_f32 v[94:95], v[190:191], v[94:95], v[106:107] op_sel:[0,1,0] op_sel_hi:[1,1,1]\n\t" \
  "ds_write_addtid_b32 v124 offset:6336\n\t" \
  "v_pk_fma_f32 v[88:89], v[190:191], v[182:183], v[108:109] op_sel:[0,1,0] op_sel_hi:[1,1,1]\n\t" \
  "v_pk_fma_f32 v[90:91], v[190:191], v[90:91], v[110:111] op_sel:[0,1,0] op_sel_hi:[1,1,1]\n\t" \
  "v_pk_fma_f32 v[92:93], v[190:191], v[92:93], v[112:113] op_sel:[0,1,0] op_sel_hi:[1,1,1]\n\t" \
  "ds_write_addtid_b32 v125 offset:14256\n\t" \
  "v_pk_fma_f32 v[98:99], v[192:193], v[98:99], v[114:115] op_sel:[0,1,0] op_sel_hi:[1,1,1]\n\t" \
  "v_pk_fma_f32 v[96:97], v[186:187], v[100:101], v[116:117] op_sel:[0,1,0] op_sel_hi:[1,1,1]\n\t" \
  "ds_read_u16 v206, v24 offset:7840\n\t" \
  "ds_read_u16 v207, v25 offset:7840\n\t" \
  "v_cvt_pk_bf16_f32 v126, v94, v95\n\t" \
  "v_cvt_pk_bf16_f32 v127, v88, v89\n\t" \
  "v_cvt_pk_bf16_f32 v128, v90, v91\n\t" \
  "v_cvt_pk_bf16_f32 v129, v92, v93\n\t" \
  "v_cvt_pk_bf16_f32 v122, v98, v99\n\t" \
  "v_cvt_pk_bf16_f32 v123, v96, v97\n\t" \
  "v_pk_mul_f32 v[106:107], v[182:183], v[94:95] op_sel:[0,0] op_sel_hi:[1,0]\n\t" \
  "ds_write_addtid_b32 v126 offset:14784\n\t" \
  "v_pk_mul_f32 v[108:109], v[182:183], v[88:89] op_sel:[0,0] op_sel_hi:[1,0]\n\t" \
  "v_pk_mul_f32 v[110:111], v[182:183], v[90:91] op_sel:[0,0] op_sel_hi:[1,0]\n\t" \
  "ds_write_addtid_b32 v127 offset:15312\n\t" \
  "v_pk_mul_f32 v[112:113], v[182:183], v[92:93] op_sel:[0,0] op_sel_hi:[1,0]\n\t" \
  "v_pk_mul_f32 v[114:115], v[184:185], v[98:99] op_sel:[0,0] op_sel_hi:[1,0]\n\t" \
  "ds_write_addtid_b32 v128 offset:15840\n\t" \
  "v_pk_mul_f32 v[116:117], v[178:179], v[96:97] op_sel:[0,0] op_sel_hi:[1,0]\n\t" \
  "v_pk_mul_f32 v[118:119], v[184:185], v[100:101] op_sel:[0,0] op_sel_hi:[1,0]\n\t" \
  "v_pk_fma_f32 v[94:95], v[190:191], v[94:95], v[106:107] op_sel:[0,1,0] op_sel_hi:[1,1,1]\n\t" \
  "ds_write_addtid_b32 v129 offset:16368\n\t" \
  "v_pk_fma_f32 v[88:89], v[190:191], v[88:89], v[108:109] op_sel:[0,1,0] op_sel_hi:[1,1,1]\n\t" \
  "v_pk_fma_f32 v[90:91], v[190:191], v[90:91], v[110:111] op_sel:[0,1,0] op_sel_hi:[1,1,1]\n\t" \
  "ds_write_addtid_b32 v122 offset:7392\n\t" \
  "v_pk_fma_f32 v[92:93], v[190:191], v[92:93], v[112:113] op_sel:[0,1,0] op_sel_hi:[1,1,1]\n\t" \
  "v_pk_fma_f32 v[98:99], v[192:193], v[98:99], v[114:115] op_sel:[0,1,0] op_sel_hi:[1,1,1]\n\t" \
  "ds_write_addtid_b32 v123 offset:1584\n\t" \
  "v_pk_fma_f32 v[102:103], v[186:187], v[96:97], v[116:117] op_sel:[0,1,0] op_sel_hi:[1,1,1]\n\t" \
  "v_pk_fma_f32 v[100:101], v[192:193], v[100:101], v[118:119] op_sel:[0,1,0] op_sel_hi:[1,1,1]\n\t" \
  "ds_read_u16 v208, v26 offset:7840\n\t" \
  "ds_read_u16 v209, v27 offset:7840\n\t" \
  "v_cvt_pk_bf16_f32 v124, v94, v95\n\t" \
  "v_cvt_pk_bf16_f32 v125, v88, v89\n\t" \
  "v_cvt_pk_bf16_f32 v126, v90, v91\n\t" \
  "v_cvt_pk_bf16_f32 v127, v92, v93\n\t" \
  "v_cvt_pk_bf16_f32 v128, v98, v99\n\t" \
  "v_cvt_pk_bf16_f32 v129, v102, v103\n\t" \
  "v_cvt_pk_bf16_f32 v122, v100, v101\n\t" \
  "v_pk_mul_f32 v[106:107], v[182:183], v[94:95] op_sel:[0,0] op_sel_hi:[1,0]\n\t" \
  "ds_write_addtid_b32 v124 offset:16896\n\t" \
  "v_pk_mul_f32 v[108:109], v[182:183], v[88:89] op_sel:[0,0] op_sel_hi:[1,0]\n\t" \
  "ds_write_addtid_b32 v125 offset:17424\n\t" \
  "v_pk_mul_f32 v[110:111], v[182:183], v[90:91] op_sel:[0,0] op_sel_hi:[1,0]\n\t" \
  "v_pk_mul_f32 v[112:113], v[182:183], v[92:93] op_sel:[0,0] op_sel_hi:[1,0]\n\t" \
  "ds_write_addtid_b32 v126 offset:17952\n\t" \
  "v_pk_mul_f32 v[114:115], v[184:185], v[98:99] op_sel:[0,0] op_sel_hi:[1,0]\n\t" \
  "v_pk_mul_f32 v[116:117], v[178:179], v[102:103] op_sel:[0,0] op_sel_hi:[1,0]\n\t" \
  "ds_write_addtid_b32 v127 offset:18480\n\t" \
  "v_pk_fma_f32 v[94:95], v[190:191], v[94:95], v[106:107] op_sel:[0,1,0] op_sel_hi:[1,1,1]\n\t" \
  "ds_write_addtid_b32 v128 offset:8448\n\t" \
  "v_pk_fma_f32 v[88:89], v[190:191], v[88:89], v[108:109] op_sel:[0,1,0] op_sel_hi:[1,1,1]\n\t" \
  "v_pk_fma_f32 v[90:91], v[190:191], v[90:91], v[110:111] op_sel:[0,1,0] op_sel_hi:[1,1,1]\n\t" \
  "ds_write_addtid_b32 v129 offset:2112\n\t" \
  "v_pk_fma_f32 v[92:93], v[190:191], v[92:93], v[112:113] op_sel:[0,1,0] op_sel_hi:[1,1,1]\n\t" \
  "v_pk_fma_f32 v[98:99], v[192:193], v[98:99], v[114:115] op_sel:[0,1,0] op_sel_hi:[1,1,1]\n\t" \
  "ds_write_addtid_b32 v122 offset:4752\n\t" \
  "v_pk_fma_f32 v[96:97], v[186:187], v[102:103], v[116:117] op_sel:[0,1,0] op_sel_hi:[1,1,1]\n\t" \
  "ds_read_u16 v210, v28 offset:7840\n\t" \
  "ds_read_u16 v211, v20 offset:9888\n\t" \
  "v_cvt_pk_bf16_f32 v123, v94, v95\n\t" \
  "v_cvt_pk_bf16_f32 v124, v88, v89\n\t" \
  "v_cvt_pk_bf16_f32 v125, v90, v91\n\t" \
  "v_cvt_pk_bf16_f32 v126, v92, v93\n\t" \
  "v_cvt_pk_bf16_f32 v127, v98, v99\n\t" \
  "v_cvt_pk_bf16_f32 v128, v96, v97\n\t" \
  "v_pk_mul_f32 v[106:107], v[182:183], v[94:95] op_sel:[0,0] op_sel_hi:[1,0]\n\t" \
  "ds_write_addtid_b32 v123 offset:19008\n\t" \
  "v_pk_mul_f32 v[108:109], v[182:183], v[88:89] op_sel:[0,0] op_sel_hi:[1,0]\n\t" \
  "v_pk_mul_f32 v[110:111], v[182:183], v[90:91] op_sel:[0,0] op_sel_hi:[1,0]\n\t" \
  "ds_write_addtid_b32 v124 offset:19536\n\t" \
  "v_pk_mul_f32 v[112:113], v[182:183], v[92:93] op_sel:[0,0] op_sel_hi:[1,0]\n\t" \
  "v_pk_mul_f32 v[114:115], v[184:185], v[98:99] op_sel:[0,0] op_sel_hi:[1,0]\n\t" \
  "ds_write_addtid_b32 v125 offset:20064\n\t" \
  "v_pk_mul_f32 v[116:117], v[178:179], v[96:97] op_sel:[0,0] op_sel_hi:[1,0]\n\t" \
  "v_pk_mul_f32 v[118:119], v[184:185], v[102:103] op_sel:[0,0] op_sel_hi:[1,0]\n\t" \
  "v_pk_fma_f32 v[94:95], v[190:191], v[94:95], v[106:107] op_sel:[0,1,0] op_sel_hi:[1,1,1]\n\t" \
  "ds_write_addtid_b32 v126 offset:20592\n\t" \
  "v_pk_fma_f32 v[88:89], v[190:191], v[88:89], v[108:109] op_sel:[0,1,0] op_sel_hi:[1,1,1]\n\t" \
  "v_pk_fma_f32 v[90:91], v[190:191], v[90:91], v[110:111] op_sel:[0,1,0] op_sel_hi:[1,1,1]\n\t" \
  "ds_write_addtid_b32 v127 offset:8976\n\t" \
  "v_pk_fma_f32 v[92:93], v[190:191], v[92:93], v[112:113] op_sel:[0,1,0] op_sel_hi:[1,1,1]\n\t" \
  "v_pk_fma_f32 v[98:99], v[192:193], v[98:99], v[114:115] op_sel:[0,1,0] op_sel_hi:[1,1,1]\n\t" \
  "ds_write_addtid_b32 v128 offset:2640\n\t" \
  "v_pk_fma_f32 v[104:105], v[186:187], v[96:97], v[116:117] op_sel:[0,1,0] op_sel_hi:[1,1,1]\n\t" \
  "v_pk_fma_f32 v[102:103], v[192:193], v[102:103], v[118:119] op_sel:[0,1,0] op_sel_hi:[1,1,1]\n\t" \
  "ds_read_u16 v212, v21 offset:9888\n\t" \
  "ds_read_u16 v213, v22 offset:9888\n\t" \
  "v_cvt_pk_bf16_f32 v129, v94, v95\n\t" \
  "v_cvt_pk_bf16_f32 v122, v88, v89\n\t" \
  "v_cvt_pk_bf16_f32 v123, v90, v91\n\t" \
  "v_cvt_pk_bf16_f32 v124, v92, v93\n\t" \
  "v_cvt_pk_bf16_f32 v125, v98, v99\n\t" \
  "v_cvt_pk_bf16_f32 v126, v104, v105\n\t" \
  "v_cvt_pk_bf16_f32 v127, v102, v103\n\t" \
  "v_pk_mul_f32 v[106:107], v[182:183], v[94:95] op_sel:[0,0] op_sel_hi:[1,0]\n\t" \
  "ds_write_addtid_b32 v129 offset:21120\n\t" \
  "v_pk_mul_f32 v[108:109], v[182:183], v[88:89] op_sel:[0,0] op_sel_hi:[1,0]\n\t" \
  "v_pk_mul_f32 v[110:111], v[182:183], v[90:91] op_sel:[0,0] op_sel_hi:[1,0]\n\t" \
  "ds_write_addtid_b32 v122 offset:21648\n\t" \
  "v_pk_mul_f32 v[112:113], v[182:183], v[92:93] op_sel:[0,0] op_sel_hi:[1,0]\n\t" \
  "v_pk_mul_f32 v[114:115], v[184:185], v[98:99] op_sel:[0,0] op_sel_hi:[1,0]\n\t" \
  "ds_write_addtid_b32 v123 offset:22176\n\t" \
  "v_pk_mul_f32 v[116:117], v[178:179], v[104:105] op_sel:[0,0] op_sel_hi:[1,0]\n\t" \
  "v_pk_mul_f32 v[118:119], v[184:185], v[102:103] op_sel:[0,0] op_sel_hi:[1,0]\n\t" \
  "ds_write_addtid_b32 v124 offset:22704\n\t" \
  "v_pk_fma_f32 v[94:95], v[190:191], v[94:95], v[106:107] op_sel:[0,1,0] op_sel_hi:[1,1,1]\n\t" \
  "v_pk_fma_f32 v[88:89], v[190:191], v[88:89], v[108:109] op_sel:[0,1,0] op_sel_hi:[1,1,1]\n\t" \
  "ds_write_addtid_b32 v125 offset:9504\n\t" \
  "v_pk_fma_f32 v[90:91], v[190:191], v[90:91], v[110:111] op_sel:[0,1,0] op_sel_hi:[1,1,1]\n\t" \
  "v_pk_fma_f32 v[92:93], v[190:191], v[92:93], v[112:113] op_sel:[0,1,0] op_sel_hi:[1,1,1]\n\t" \
  "ds_write_addtid_b32 v126 offset:3168\n\t" \
  "v_pk_fma_f32 v[98:99], v[192:193], v[98:99], v[114:115] op_sel:[0,1,0] op_sel_hi:[1,1,1]\n\t" \
  "v_pk_fma_f32 v[96:97], v[186:187], v[104:105], v[116:117] op_sel:[0,1,0] op_sel_hi:[1,1,1]\n\t" \
  "ds_write_addtid_b32 v127 offset:5280\n\t" \
  "v_pk_fma_f32 v[102:103], v[192:193], v[102:103], v[118:119] op_sel:[0,1,0] op_sel_hi:[1,1,1]\n\t" \
  "ds_read_u16 v214, v23 offset:9888\n\t" \
  "ds_read_u16 v215, v24 offset:9888\n\t" \
  "v_cvt_pk_bf16_f32 v128, v94, v95\n\t" \
  "v_cvt_pk_bf16_f32 v129, v88, v89\n\t" \
  "v_cvt_pk_bf16_f32 v122, v90, v91\n\t" \
  "v_cvt_pk_bf16_f32 v123, v92, v93\n\t" \
  "v_cvt_pk_bf16_f32 v124, v98, v99\n\t" \
  "v_cvt_pk_bf16_f32 v125, v96, v97\n\t" \
  "v_cvt_pk_bf16_f32 v126, v102, v103\n\t" \
  "v_pk_mul_f32 v[106:107], v[182:183], v[94:95] op_sel:[0,0] op_sel_hi:[1,0]\n\t" \
  "ds_write_addtid_b32 v128 offset:23232\n\t" \
  "v_pk_mul_f32 v[108:109], v[182:183], v[88:89] op_sel:[0,0] op_sel_hi:[1,0]\n\t" \
  "v_pk_mul_f32 v[110:111], v[182:183], v[90:91] op_sel:[0,0] op_sel_hi:[1,0]\n\t" \
  "ds_write_addtid_b32 v129 offset:23760\n\t" \
  "v_pk_mul_f32 v[112:113], v[182:183], v[92:93] op_sel:[0,0] op_sel_hi:[1,0]\n\t" \
  "v_pk_mul_f32 v[114:115], v[184:185], v[98:99] op_sel:[0,0] op_sel_hi:[1,0]\n\t" \
  "ds_write_addtid_b32 v122 offset:24288\n\t" \
  "v_pk_mul_f32 v[116:117], v[184:185], v[102:103] op_sel:[0,0] op_sel_hi:[1,0]\n\t" \
  "v_pk_mul_f32 v[118:119], v[184:185], v[104:105] op_sel:[0,0] op_sel_hi:[1,0]\n\t" \
  "ds_write_addtid_b32 v123 offset:24816\n\t" \
  "v_pk_fma_f32 v[94:95], v[190:191], v[94:95], v[106:107] op_sel:[0,1,0] op_sel_hi:[1,1,1]\n\t" \
  "v_pk_fma_f32 v[88:89], v[190:191], v[88:89], v[108:109] op_sel:[0,1,0] op_sel_hi:[1,1,1]\n\t" \
  "ds_write_addtid_b32 v124 offset:10032\n\t" \
  "v_pk_fma_f32 v[90:91], v[190:191], v[90:91], v[110:111] op_sel:[0,1,0] op_sel_hi:[1,1,1]\n\t" \
  "v_pk_fma_f32 v[92:93], v[190:191], v[92:93], v[112:113] op_sel:[0,1,0] op_sel_hi:[1,1,1]\n\t" \
  "ds_write_addtid_b32 v125 offset:3696\n\t" \
  "v_pk_fma_f32 v[98:99], v[192:193], v[98:99], v[114:115] op_sel:[0,1,0] op_sel_hi:[1,1,1]\n\t" \
  "v_pk_fma_f32 v[102:103], v[192:193], v[102:103], v[116:117] op_sel:[0,1,0] op_sel_hi:[1,1,1]\n\t" \
  "ds_write_addtid_b32 v126 offset:6864\n\t" \
  "v_pk_fma_f32 v[104:105], v[192:193], v[104:105], v[118:119] op_sel:[0,1,0] op_sel_hi:[1,1,1]\n\t" \
  "ds_read_u16 v216, v25 offset:9888\n\t" \
  "ds_read_u16 v217, v26 offset:9888\n\t" \
  "v_cvt_pk_bf16_f32 v127, v94, v95\n\t" \
  "v_cvt_pk_bf16_f32 v128, v88, v89\n\t" \
  "v_cvt_pk_bf16_f32 v129, v90, v91\n\t" \
  "v_cvt_pk_bf16_f32 v122, v92, v93\n\t" \
  "v_cvt_pk_bf16_f32 v123, v98, v99\n\t" \
  "v_cvt_pk_bf16_f32 v124, v102, v103\n\t" \
  "v_cvt_pk_bf16_f32 v125, v104, v105\n\t" \
  "v_pk_mul_f32 v[106:107], v[182:183], v[94:95] op_sel:[0,0] op_sel_hi:[1,0]\n\t" \
  "ds_write_addtid_b32 v127 offset:25344\n\t" \
  "ds_write_addtid_b32 v128 offset:25872\n\t" \
  "ds_write_addtid_b32 v129 offset:26400\n\t" \
  "ds_write_addtid_b32 v122 offset:26928\n\t" \
  "v_pk_fma_f32 v[94:95], v[190:191], v[94:95], v[106:107] op_sel:[0,1,0] op_sel_hi:[1,1,1]\n\t" \
  "ds_write_addtid_b32 v123 offset:10560\n\t" \
  "ds_write_addtid_b32 v124 offset:7920\n\t" \
  "ds_write_addtid_b32 v125 offset:5808\n\t" \
  "ds_read_u16 v218, v27 offset:9888\n\t" \
  "ds_read_u16 v219, v28 offset:9888\n\t" \
  "v_cvt_pk_bf16_f32 v126, v94, v95\n\t" \
  "s_nop 0\n\t" \
  "ds_write_addtid_b32 v126 offset:27456\n\t" \
  "v_lshlrev_b32 v240, 7, v16\n\t" \
  "v_add_u32 v240, 32768, v240\n\t" \
  "s_waitcnt vmcnt(0)\n\t" \
  "buffer_load_dwordx2 v[236:237], v240, %3, 0 offen sc1\n\t" \
  "ds_write_b128 v241, a[104:107]\n\t" \
  "ds_write_b128 v241, a[108:111] offset:1024\n\t" \
  "s_waitcnt lgkmcnt(0)\n\t" \
  "s_barrier\n\t" \
  "ds_read_b128 v[64:67], v45 offset:0\n\t" \
  "v_lshl_or_b32 v164, v204, 16, v203\n\t" \
  "ds_read_b128 v[68:71], v44 offset:0\n\t" \
  "v_lshl_or_b32 v165, v206, 16, v205\n\t" \
  "ds_read_b128 v[72:75], v44 offset:8448\n\t" \
  "v_lshl_or_b32 v166, v208, 16, v207\n\t" \
  "ds_read_b128 v[76:79], v45 offset:16\n\t" \
  "v_lshl_or_b32 v167, v210, 16, v209\n\t" \
  "ds_read_b128 v[80:83], v44 offset:16\n\t" \
  "v_and_b32 v202, v202, v168\n\t" \
  "ds_read_b128 v[84:87], v44 offset:8464\n\t" \
  "ds_write_b128 v47, v[164:167] offset:21120\n\t" \
  "ds_read_b128 v[88:91], v45 offset:32\n\t" \
  "v_alignbit_b32 v161, v165, v164, 16\n\t" \
  "ds_read_b128 v[92:95], v44 offset:32\n\t" \
  "v_alignbit_b32 v162, v166, v165, 16\n\t" \
  "ds_read_b128 v[96:99], v44 offset:8480\n\t" \
  "v_alignbit_b32 v163, v167, v166, 16\n\t" \
  "s_waitcnt lgkmcnt(3)\n\t" \
  "v_mfma_f32_16x16x32_bf16 v[170:173], v[68:71], v[64:67], 0\n\t" \
  "v_mfma_f32_16x16x32_bf16 v[174:177], v[72:75], v[64:67], 0\n\t" \
  "ds_read_b128 v[100:103], v45 offset:48\n\t" \
  "v_lshl_or_b32 v160, v203, 16, v202\n\t" \
  "ds_read_b128 v[104:107], v44 offset:48\n\t" \
  "ds_write_b128 v47, v[160:163] offset:16896\n\t" \
  "ds_read_b128 v[108:111], v44 offset:8496\n\t" \
  "v_lshl_or_b32 v164, v213, 16, v212\n\t" \
  "s_waitcnt lgkmcnt(1)\n\t" \
  "v_mfma_f32_16x16x32_bf16 v[170:173], v[80:83], v[76:79], v[170:173]\n\t" \
  "v_mfma_f32_16x16x32_bf16 v[174:177], v[84:87], v[76:79], v[174:177]\n\t" \
  "ds_read_b128 v[112:115], v45 offset:64\n\t" \
  "v_lshl_or_b32 v165, v215, 16, v214\n\t" \
  "ds_read_b128 v[116:119], v44 offset:64\n\t" \
  "v_lshl_or_b32 v166, v217, 16, v216\n\t" \
  "ds_read_b128 v[120:123], v44 offset:8512\n\t" \
  "v_lshl_or_b32 v167, v219, 16, v218\n\t" \
  "v_mfma_f32_16x16x32_bf16 v[170:173], v[92:95], v[88:91], v[170:173]\n\t" \
  "v_mfma_f32_16x16x32_bf16 v[174:177], v[96:99], v[88:91], v[174:177]\n\t" \
  "ds_read_b128 v[124:127], v45 offset:80\n\t" \
  "v_and_b32 v211, v211, v168\n\t" \
  "ds_read_b128 v[128:131], v44 offset:80\n\t" \
  "ds_write_b128 v47, v[164:167] offset:29568\n\t" \
  "ds_read_b128 v[132:135], v44 offset:8528\n\t" \
  "v_alignbit_b32 v161, v165, v164, 16\n\t" \
  "s_waitcnt lgkmcnt(1)\n\t" \
  "v_mfma_f32_16x16x32_bf16 v[170:173], v[104:107], v[100:103], v[170:173]\n\t" \
  "v_mfma_f32_16x16x32_bf16 v[174:177], v[108:111], v[100:103], v[174:177]\n\t" \
  "ds_read_b128 v[136:139], v45 offset:96\n\t" \
  "v_alignbit_b32 v162, v166, v165, 16\n\t" \
  "ds_read_b128 v[140:143], v44 offset:96\n\t" \
  "v_alignbit_b32 v163, v167, v166, 16\n\t" \
  "ds_read_b128 v[144:147], v44 offset:8544\n\t" \
  "v_lshl_or_b32 v160, v212, 16, v211\n\t" \
  "v_mfma_f32_16x16x32_bf16 v[170:173], v[116:119], v[112:115], v[170:173]\n\t" \
  "v_mfma_f32_16x16x32_bf16 v[174:177], v[120:123], v[112:115], v[174:177]\n\t" \
  "ds_read_b128 v[148:151], v45 offset:112\n\t" \
  "ds_write_b128 v47, v[160:163] offset:25344\n\t" \
  "ds_read_b128 v[152:155], v44 offset:112\n\t" \
  "ds_read_b128 v[156:159], v44 offset:8560\n\t" \
  "s_waitcnt lgkmcnt(2)\n\t" \
  "v_mfma_f32_16x16x32_bf16 v[170:173], v[128:131], v[124:127], v[170:173]\n\t" \
  "v_mfma_f32_16x16x32_bf16 v[174:177], v[132:135], v[124:127], v[174:177]\n\t" \
  "v_mfma_f32_16x16x32_bf16 v[170:173], v[140:143], v[136:139], v[170:173]\n\t" \
  "v_mfma_f32_16x16x32_bf16 v[174:177], v[144:147], v[136:139], v[174:177]\n\t" \
  "s_waitcnt lgkmcnt(0)\n\t" \
  "v_mfma_f32_16x16x32_bf16 v[170:173], v[152:155], v[148:151], v[170:173]\n\t" \
  "v_mfma_f32_16x16x32_bf16 v[174:177], v[156:159], v[148:151], v[174:177]\n\t" \
  "s_nop 7\n\t" \
  "v_cvt_pk_bf16_f32 v244, v170, v171\n\t" \
  "v_cvt_pk_bf16_f32 v245, v172, v173\n\t" \
  "v_cvt_pk_bf16_f32 v246, v174, v175\n\t" \
  "v_cvt_pk_bf16_f32 v247, v176, v177\n\t" \
  "ds_write_b128 v46, v[244:247]\n\t" \
  "ds_read_u16 v202, v20 offset:0\n\t" \
  "ds_read_u16 v203, v21 offset:0\n\t" \
  "ds_read_u16 v204, v22 offset:0\n\t" \
  "ds_read_u16 v205, v23 offset:0\n\t" \
  "ds_read_u16 v206, v24 offset:0\n\t" \
  "ds_read_u16 v207, v25 offset:0\n\t" \
  "ds_read_u16 v208, v26 offset:0\n\t" \
  "ds_read_u16 v209, v27 offset:0\n\t" \
  "ds_read_u16 v210, v28 offset:0\n\t" \
  "s_cmp_lg_u32 %13, 0\n\t" \
  "s_cbranch_scc1 L_na_%=\n\t" \
  "s_waitcnt vmcnt(0)\n\t" \
  "v_cmp_eq_u64 vcc, v[32:33], v[236:237]\n\t" \
  "s_nop 1\n\t" \
  "s_cmp_eq_u64 vcc, -1\n\t" \
  "s_cbranch_scc1 L_ck0_%=\n\t" \
  "s_mov_b32 s51, 0\n\t" \
  "L_sp0_%=:\n\t" \
  "buffer_load_dwordx2 v[56:57], v240, %3, 0 offen sc1\n\t" \
  "s_waitcnt vmcnt(0)\n\t" \
  "v_cmp_eq_u64 vcc, v[32:33], v[56:57]\n\t" \
  "s_add_u32 s51, s51, 1\n\t" \
  "s_nop 1\n\t" \
  "s_cmp_eq_u64 vcc, -1\n\t" \
  "s_cbranch_scc1 L_ok0_%=\n\t" \
  "s_sleep 1\n\t" \
  "s_cmp_lt_u32 s51, 0x40000\n\t" \
  "s_cbranch_scc1 L_sp0_%=\n\t" \
  "L_ok0_%=:\n\t" \
  "L_ck0_%=:\n\t" \
  "buffer_inv sc1\n\t" \
  "s_waitcnt vmcnt(0)\n\t" \
  "L_na_%=:\n\t" \
  "s_mul_i32 s50, s41, 17920\n\t" \
  "s_lshl_b32 s43, s42, 13\n\t" \
  "s_add_u32 s50, s50, s43\n\t" \
  "s_add_u32 s50, s50, 76032\n\t" \
  "s_mul_i32 s49, s40, 9\n\t" \
  "s_add_u32 s49, s49, s41\n\t" \
  "s_mul_i32 s49, s49, 4608\n\t" \
  "s_lshl_b32 s43, s42, 11\n\t" \
  "s_add_u32 s49, s49, s43\n\t" \
  "s_add_u32 s49, s49, 1048576\n\t" \
  "s_waitcnt lgkmcnt(0)\n\t" \
  "s_barrier\n\t" \
  "s_add_u32 m0, s50, 0\n\t" \
  "s_add_u32 s43, s49, 0\n\t" \
  "buffer_load_dwordx4 v29, %3, s43 offen lds\n\t" \
  "s_add_u32 m0, s50, 1024\n\t" \
  "s_add_u32 s43, s49, 256\n\t" \
  "buffer_load_dwordx4 v30, %3, s43 offen lds\n\t" \
  "s_add_u32 m0, s50, 2048\n\t" \
  "s_add_u32 s43, s49, 512\n\t" \
  "buffer_load_dwordx4 v29, %3, s43 offen lds\n\t" \
  "s_add_u32 m0, s50, 3072\n\t" \
  "s_add_u32 s43, s49, 768\n\t" \
  "buffer_load_dwordx4 v30, %3, s43 offen lds\n\t" \
  "s_add_u32 m0, s50, 4096\n\t" \
  "s_add_u32 s43, s49, 1024\n\t" \
  "buffer_load_dwordx4 v29, %3, s43 offen lds\n\t" \
  "s_add_u32 m0, s50, 5120\n\t" \
  "s_add_u32 s43, s49, 1280\n\t" \
  "buffer_load_dwordx4 v30, %3, s43 offen lds\n\t" \
  "s_add_u32 m0, s50, 6144\n\t" \
  "s_add_u32 s43, s49, 1536\n\t" \
  "buffer_load_dwordx4 v29, %3, s43 offen lds\n\t" \
  "s_add_u32 m0, s50, 7168\n\t" \
  "s_add_u32 s43, s49, 1792\n\t" \
  "buffer_load_dwordx4 v30, %3, s43 offen lds\n\t" \
  "ds_read_u16 v211, v20 offset:2048\n\t" \
  "v_lshl_or_b32 v248, v204, 16, v203\n\t" \
  "ds_read_u16 v212, v21 offset:2048\n\t" \
  "v_lshl_or_b32 v249, v206, 16, v205\n\t" \
  "ds_read_u16 v213, v22 offset:2048\n\t" \
  "v_lshl_or_b32 v250, v208, 16, v207\n\t" \
  "ds_read_u16 v214, v23 offset:2048\n\t" \
  "v_lshl_or_b32 v251, v210, 16, v209\n\t" \
  "ds_read_u16 v215, v24 offset:2048\n\t" \
  "v_and_b32 v202, v202, v168\n\t" \
  "ds_write_b128 v169, v[248:251] offset:4224\n\t" \
  "ds_read_u16 v216, v25 offset:2048\n\t" \
  "v_alignbit_b32 v245, v249, v248, 16\n\t" \
  "ds_read_u16 v217, v26 offset:2048\n\t" \
  "v_alignbit_b32 v246, v250, v249, 16\n\t" \
  "ds_read_u16 v218, v27 offset:2048\n\t" \
  "v_alignbit_b32 v247, v251, v250, 16\n\t" \
  "ds_read_u16 v219, v28 offset:2048\n\t" \
  "v_lshl_or_b32 v244, v203, 16, v202\n\t" \
  "ds_write_b128 v169, v[244:247] offset:0\n\t" \
  "s_waitcnt lgkmcnt(1)\n\t" \
  "ds_read_u16 v202, v20 offset:4096\n\t" \
  "v_lshl_or_b32 v164, v213, 16, v212\n\t" \
  "ds_read_u16 v203, v21 offset:4096\n\t" \
  "v_lshl_or_b32 v165, v215, 16, v214\n\t" \
  "ds_read_u16 v204, v22 offset:4096\n\t" \
  "v_lshl_or_b32 v166, v217, 16, v216\n\t" \
  "ds_read_u16 v205, v23 offset:4096\n\t" \
  "v_lshl_or_b32 v167, v219, 16, v218\n\t" \
  "ds_read_u16 v206, v24 offset:4096\n\t" \
  "v_and_b32 v211, v211, v168\n\t" \
  "ds_write_b128 v169, v[164:167] offset:12672\n\t" \
  "ds_read_u16 v207, v25 offset:4096\n\t" \
  "v_alignbit_b32 v161, v165, v164, 16\n\t" \
  "ds_read_u16 v208, v26 offset:4096\n\t" \
  "v_alignbit_b32 v162, v166, v165, 16\n\t" \
  "ds_read_u16 v209, v27 offset:4096\n\t" \
  "v_alignbit_b32 v163, v167, v166, 16\n\t" \
  "ds_read_u16 v210, v28 offset:4096\n\t" \
  "v_lshl_or_b32 v160, v212, 16, v211\n\t" \
  "ds_write_b128 v169, v[160:163] offset:8448\n\t" \
  "s_waitcnt lgkmcnt(1)\n\t" \
  "ds_read_u16 v211, v20 offset:7840\n\t" \
  "v_lshl_or_b32 v248, v204, 16, v203\n\t" \
  "ds_read_u16 v212, v21 offset:7840\n\t" \
  "v_lshl_or_b32 v249, v206, 16, v205\n\t" \
  "ds_read_u16 v213, v22 offset:7840\n\t" \
  "v_lshl_or_b32 v250, v208, 16, v207\n\t" \
  "ds_read_u16 v214, v23 offset:7840\n\t" \
  "v_lshl_or_b32 v251, v210, 16, v209\n\t" \
  "ds_read_u16 v215, v24 offset:7840\n\t" \
  "v_and_b32 v202, v202, v168\n\t" \
  "ds_write_b128 v169, v[248:251] offset:21120\n\t" \
  "ds_read_u16 v216, v25 offset:7840\n\t" \
  "v_alignbit_b32 v245, v249, v248, 16\n\t" \
  "ds_read_u16 v217, v26 offset:7840\n\t" \
  "v_alignbit_b32 v246, v250, v249, 16\n\t" \
  "ds_read_u16 v218, v27 offset:7840\n\t" \
  "v_alignbit_b32 v247, v251, v250, 16\n\t" \
  "ds_read_u16 v219, v28 offset:7840\n\t" \
  "v_lshl_or_b32 v244, v203, 16, v202\n\t" \
  "ds_write_b128 v169, v[244:247] offset:16896\n\t" \
  "s_waitcnt lgkmcnt(1)\n\t" \
  "ds_read_u16 v202, v20 offset:9888\n\t" \
  "v_lshl_or_b32 v164, v213, 16, v212\n\t" \
  "ds_read_u16 v203, v21 offset:9888\n\t" \
  "v_lshl_or_b32 v165, v215, 16, v214\n\t" \
  "ds_read_u16 v204, v22 offset:9888\n\t" \
  "v_lshl_or_b32 v166, v217, 16, v216\n\t" \
  "ds_read_u16 v205, v23 offset:9888\n\t" \
  "v_lshl_or_b32 v167, v219, 16, v218\n\t" \
  "ds_read_u16 v206, v24 offset:9888\n\t" \
  "v_and_b32 v211, v211, v168\n\t" \
  "ds_write_b128 v47, v[164:167] offset:38016\n\t" \
  "ds_read_u16 v207, v25 offset:9888\n\t" \
  "v_alignbit_b32 v161, v165, v164, 16\n\t" \
  "ds_read_u16 v208, v26 offset:9888\n\t" \
  "v_alignbit_b32 v162, v166, v165, 16\n\t" \
  "ds_read_u16 v209, v27 offset:9888\n\t" \
  "v_alignbit_b32 v163, v167, v166, 16\n\t" \
  "ds_read_u16 v210, v28 offset:9888\n\t" \
  "v_lshl_or_b32 v160, v212, 16, v211\n\t" \
  "ds_write_b128 v47, v[160:163] offset:33792\n\t" \
  "s_waitcnt lgkmcnt(1)\n\t" \
  "v_lshl_or_b32 v248, v204, 16, v203\n\t" \
  "v_lshl_or_b32 v249, v206, 16, v205\n\t" \
  "v_lshl_or_b32 v250, v208, 16, v207\n\t" \
  "v_lshl_or_b32 v251, v210, 16, v209\n\t" \
  "v_and_b32 v202, v202, v168\n\t" \
  "ds_write_b128 v47, v[248:251] offset:46464\n\t" \
  "v_alignbit_b32 v245, v249, v248, 16\n\t" \
  "v_alignbit_b32 v246, v250, v249, 16\n\t" \
  "v_alignbit_b32 v247, v251, v250, 16\n\t" \
  "v_lshl_or_b32 v244, v203, 16, v202\n\t" \
  "ds_write_b128 v47, v[244:247] offset:42240\n\t" \
  "v_mov_b32 v64, 0\n\t" \
  "v_mov_b32 v65, 0\n\t" \
  "v_mov_b32 v66, 0\n\t" \
  "v_mov_b32 v67, 0\n\t" \
  "v_lshlrev_b32 v54, 3, v31\n\t" \
  "v_min_u32 v54, 1520, v54\n\t" \
  "s_mul_i32 s43, s41, 17920\n\t" \
  "s_add_u32 s44, s43, 92416\n\t" \
  "v_add_u32 v54, s44, v54\n\t" \
  "ds_write_b128 v54, v[64:67]\n\t" \
  "ds_write_b128 v54, v[64:67] offset:35840\n\t" \
  "s_add_u32 s43, s43, 76032\n\t" \
  "v_and_b32 v54, 3, v16\n\t" \
  "v_sub_u32 v55, 3, v54\n\t" \
  "v_bfe_u32 v56, v16, 2, 2\n\t" \
  "v_lshlrev_b32 v57, 7, v56\n\t" \
  "v_add_u32 v57, s43, v57\n\t" \
  "v_add_u32 v58, 0, v55\n\t" \
  "v_lshl_add_u32 v59, v58, 9, v57\n\t" \
  "v_and_b32 v58, 3, v58\n\t" \
  "v_lshlrev_b32 v58, 1, v58\n\t" \
  "v_xor_b32 v58, v58, v18\n\t" \
  "v_lshl_add_u32 v20, v58, 4, v59\n\t" \
  "v_xor_b32 v21, 64, v20\n\t" \
  "v_add_u32 v58, 1, v55\n\t" \
  "v_lshl_add_u32 v59, v58, 9, v57\n\t" \
  "v_and_b32 v58, 3, v58\n\t" \
  "v_lshlrev_b32 v58, 1, v58\n\t" \
  "v_xor_b32 v58, v58, v18\n\t" \
  "v_lshl_add_u32 v22, v58, 4, v59\n\t" \
  "v_xor_b32 v23, 64, v22\n\t" \
  "v_add_u32 v58, 2, v55\n\t" \
  "v_lshl_add_u32 v59, v58, 9, v57\n\t" \
  "v_and_b32 v58, 3, v58\n\t" \
  "v_lshlrev_b32 v58, 1, v58\n\t" \
  "v_xor_b32 v58, v58, v18\n\t" \
  "v_lshl_add_u32 v24, v58, 4, v59\n\t" \
  "v_xor_b32 v25, 64, v24\n\t" \
  "v_add_u32 v58, 3, v55\n\t" \
  "v_lshl_add_u32 v59, v58, 9, v57\n\t" \
  "v_and_b32 v58, 3, v58\n\t" \
  "v_lshlrev_b32 v58, 1, v58\n\t" \
  "v_xor_b32 v58, v58, v18\n\t" \
  "v_lshl_add_u32 v26, v58, 4, v59\n\t" \
  "v_xor_b32 v27, 64, v26\n\t" \
  "v_and_b32 v58, 1, v16\n\t" \
  "v_mul_u32_u24 v28, 4224, v58\n\t" \
  "v_lshrrev_b32 v58, 1, v17\n\t" \
  "v_add_u32 v58, v58, v18\n\t" \
  "v_lshl_add_u32 v28, v58, 4, v28\n\t" \
  "s_mul_i32 s43, s41, 8448\n\t" \
  "v_add_u32 v28, s43, v28\n\t" \
  "v_mul_u32_u24 v44, 144, v54\n\t" \
  "v_lshl_add_u32 v44, v18, 5, v44\n\t" \
  "v_lshl_add_u32 v44, v56, 2, v44\n\t" \
  "v_lshlrev_b32 v46, 3, v31\n\t" \
  "v_lshrrev_b32 v54, 4, v31\n\t" \
  "v_lshl_add_u32 v45, v54, 4, v46\n\t" \
  "s_mul_i32 s62, s41, 4608\n\t" \
  "s_add_u32 s62, s62, 147712\n\t" \
  "s_mul_i32 s48, s40, 9\n\t" \
  "s_add_u32 s48, s48, s41\n\t" \
  "s_mov_b32 s61, 0\n\t" \
  "v_mov_b32 v53, 0x70000000\n\t" \
  "s_mov_b32 s46, 0\n\t" \
  "s_mov_b32 s63, 35840\n\t" \
  "v_mov_b32 v62, v45\n\t" \
  "L_cv_%=:\n\t" \
  "s_lshl_b32 s43, s46, 1\n\t" \
  "s_add_u32 s43, s43, s41\n\t" \
  "s_add_u32 s44, s43, 2\n\t" \
  "s_min_u32 s44, s44, 8\n\t" \
  "s_mul_i32 s49, s40, 9\n\t" \
  "s_add_u32 s49, s49, s44\n\t" \
  "s_mul_i32 s49, s49, 4608\n\t" \
  "s_lshl_b32 s44, s42, 11\n\t" \
  "s_add_u32 s49, s49, s44\n\t" \
  "s_add_u32 s49, s49, 1048576\n\t" \
  "s_and_b32 s44, s46, 1\n\t" \
  "s_xor_b32 s44, s44, 1\n\t" \
  "s_lshl_b32 s44, s44, 1\n\t" \
  "s_add_u32 s44, s44, s41\n\t" \
  "s_mul_i32 s50, s44, 17920\n\t" \
  "s_lshl_b32 s44, s42, 13\n\t" \
  "s_add_u32 s50, s50, s44\n\t" \
  "s_add_u32 s50, s50, 76032\n\t" \
  "s_waitcnt vmcnt(0) lgkmcnt(0)\n\t" \
  "s_barrier\n\t" \
  "s_cmp_eq_u32 s43, 9\n\t" \
  "s_cbranch_scc1 L_cv_idle_%=\n\t" \
  "s_cmp_eq_u32 s42, 1\n\t" \
  "s_cbranch_scc1 L_cv_y_%=\n\t" \
  "ds_read_b128 v[64:67], v28 offset:0\n\t" \
  "ds_read_b128 v[128:131], v20 offset:14336\n\t" \
  "ds_read_b128 v[140:143], v20 offset:8192\n\t" \
  "ds_read_b128 v[76:79], v28 offset:1536\n\t" \
  "ds_read_b128 v[144:147], v20 offset:6144\n\t" \
  "ds_read_b128 v[80:83], v28 offset:2048\n\t" \
  "ds_read_b128 v[68:71], v28 offset:512\n\t" \
  "ds_read_b128 v[136:139], v20 offset:10240\n\t" \
  "ds_read_b128 v[132:135], v20 offset:12288\n\t" \
  "ds_read_b128 v[224:227], v62\n\t" \
  "ds_read_b128 v[228:231], v62 offset:2304\n\t" \
  "s_waitcnt lgkmcnt(7)\n\t" \
  "v_mfma_f32_16x16x32_bf16 v[240:243], v[64:67], v[128:131], 0\n\t" \
  "ds_read_b128 v[72:75], v28 offset:1024\n\t" \
  "ds_read_b128 v[156:159], v20 offset:0\n\t" \
  "v_mfma_f32_16x16x32_bf16 v[244:247], v[64:67], v[140:143], 0\n\t" \
  "ds_read_b128 v[92:95], v28 offset:3584\n\t" \
  "ds_read_b128 v[152:155], v20 offset:2048\n\t" \
  "v_mfma_f32_16x16x32_bf16 v[244:247], v[76:79], v[128:131], v[244:247]\n\t" \
  "ds_read_b128 v[84:87], v28 offset:2560\n\t" \
  "s_waitcnt lgkmcnt(10)\n\t" \
  "v_mfma_f32_16x16x32_bf16 v[248:251], v[64:67], v[144:147], 0\n\t" \
  "s_add_u32 m0, s50, 0\n\t" \
  "s_add_u32 s44, s49, 0\n\t" \
  "v_mfma_f32_16x16x32_bf16 v[252:255], v[76:79], v[144:147], 0\n\t" \
  "buffer_load_dwordx4 v29, %3, s44 offen lds\n\t" \
  "v_mfma_f32_16x16x32_bf16 v[248:251], v[80:83], v[128:131], v[248:251]\n\t" \
  "ds_read_b128 v[88:91], v28 offset:3072\n\t" \
  "s_waitcnt lgkmcnt(6)\n\t" \
  "v_mfma_f32_16x16x32_bf16 v[252:255], v[80:83], v[140:143], v[252:255]\n\t" \
  "ds_read_b128 v[148:151], v20 offset:4096\n\t" \
  "ds_read_b128 v[96:99], v28 offset:64\n\t" \
  "v_mfma_f32_16x16x32_bf16 v[248:251], v[68:71], v[140:143], v[248:251]\n\t" \
  "ds_read_b128 v[160:163], v21 offset:14336\n\t" \
  "ds_read_b128 v[172:175], v21 offset:8192\n\t" \
  "v_mfma_f32_16x16x32_bf16 v[244:247], v[68:71], v[136:139], v[244:247]\n\t" \
  "ds_read_b128 v[108:111], v28 offset:1600\n\t" \
  "s_waitcnt lgkmcnt(10)\n\t" \
  "v_mfma_f32_16x16x32_bf16 v[248:251], v[76:79], v[132:135], v[248:251]\n\t" \
  "s_add_u32 m0, s50, 1024\n\t" \
  "s_add_u32 s44, s49, 256\n\t" \
  "v_mfma_f32_16x16x32_bf16 v[244:247], v[72:75], v[132:135], v[244:247]\n\t" \
  "buffer_load_dwordx4 v30, %3, s44 offen lds\n\t" \
  "v_mfma_f32_16x16x32_bf16 v[248:251], v[72:75], v[136:139], v[248:251]\n\t" \
  "ds_read_b128 v[176:179], v21 offset:6144\n\t" \
  "s_waitcnt lgkmcnt(8)\n\t" \
  "v_mfma_f32_16x16x32_bf16 v[252:255], v[64:67], v[156:159], v[252:255]\n\t" \
  "ds_read_b128 v[112:115], v28 offset:2112\n\t" \
  "ds_read_b128 v[100:103], v28 offset:576\n\t" \
  "v_mfma_f32_16x16x32_bf16 v[252:255], v[92:95], v[128:131], v[252:255]\n\t" \
  "buffer_store_dwordx4 v[224:227], v53, %3, s61 offen offset:0 sc1\n\t" \
  "v_mfma_f32_16x16x32_bf16 v[252:255], v[68:71], v[152:155], v[252:255]\n\t" \
  "ds_read_b128 v[168:171], v21 offset:10240\n\t" \
  "s_waitcnt lgkmcnt(8)\n\t" \
  "v_mfma_f32_16x16x32_bf16 v[252:255], v[84:87], v[136:139], v[252:255]\n\t" \
  "ds_read_b128 v[164:167], v21 offset:12288\n\t" \
  "ds_read_b128 v[104:107], v28 offset:1088\n\t" \
  "v_mfma_f32_16x16x32_bf16 v[252:255], v[88:91], v[132:135], v[252:255]\n\t" \
  "buffer_store_dwordx4 v[228:231], v53, %3, s61 offen offset:2048 sc1\n\t" \
  "v_mfma_f32_16x16x32_bf16 v[252:255], v[72:75], v[148:151], v[252:255]\n\t" \
  "ds_read_b128 v[188:191], v21 offset:0\n\t" \
  "s_waitcnt lgkmcnt(7)\n\t" \
  "v_mfma_f32_16x16x32_bf16 v[240:243], v[96:99], v[160:163], v[240:243]\n\t" \
  "ds_read_b128 v[124:127], v28 offset:3648\n\t" \
  "v_mfma_f32_16x16x32_bf16 v[244:247], v[96:99], v[172:175], v[244:247]\n\t" \
  "ds_read_b128 v[184:187], v21 offset:2048\n\t" \
  "v_mfma_f32_16x16x32_bf16 v[244:247], v[108:111], v[160:163], v[244:247]\n\t" \
  "ds_read_b128 v[116:119], v28 offset:2624\n\t" \
  "s_waitcnt lgkmcnt(8)\n\t" \
  "v_mfma_f32_16x16x32_bf16 v[248:251], v[96:99], v[176:179], v[248:251]\n\t" \
  "s_add_u32 m0, s50, 2048\n\t" \
  "s_add_u32 s44, s49, 512\n\t" \
  "v_mfma_f32_16x16x32_bf16 v[252:255], v[108:111], v[176:179], v[252:255]\n\t" \
  "buffer_load_dwordx4 v29, %3, s44 offen lds\n\t" \
  "v_mfma_f32_16x16x32_bf16 v[248:251], v[112:115], v[160:163], v[248:251]\n\t" \
  "ds_read_b128 v[120:123], v28 offset:3136\n\t" \
  "s_waitcnt lgkmcnt(7)\n\t" \
  "v_mfma_f32_16x16x32_bf16 v[252:255], v[112:115], v[172:175], v[252:255]\n\t" \
  "ds_read_b128 v[180:183], v21 offset:4096\n\t" \
  "ds_read_b128 v[64:67], v28 offset:128\n\t" \
  "v_mfma_f32_16x16x32_bf16 v[248:251], v[100:103], v[172:175], v[248:251]\n\t" \
  "ds_read_b128 v[128:131], v22 offset:14336\n\t" \
  "ds_read_b128 v[140:143], v22 offset:8192\n\t" \
  "v_mfma_f32_16x16x32_bf16 v[244:247], v[100:103], v[168:171], v[244:247]\n\t" \
  "ds_read_b128 v[76:79], v28 offset:1664\n\t" \
  "s_waitcnt lgkmcnt(10)\n\t" \
  "v_mfma_f32_16x16x32_bf16 v[248:251], v[108:111], v[164:167], v[248:251]\n\t" \
  "s_add_u32 m0, s50, 3072\n\t" \
  "s_add_u32 s44, s49, 768\n\t" \
  "v_mfma_f32_16x16x32_bf16 v[244:247], v[104:107], v[164:167], v[244:247]\n\t" \
  "buffer_load_dwordx4 v30, %3, s44 offen lds\n\t" \
  "v_mfma_f32_16x16x32_bf16 v[248:251], v[104:107], v[168:171], v[248:251]\n\t" \
  "ds_read_b128 v[144:147], v22 offset:6144\n\t" \
  "s_waitcnt lgkmcnt(8)\n\t" \
  "v_mfma_f32_16x16x32_bf16 v[252:255], v[96:99], v[188:191], v[252:255]\n\t" \
  "ds_read_b128 v[80:83], v28 offset:2176\n\t" \
  "ds_read_b128 v[68:71], v28 offset:640\n\t" \
  "v_mfma_f32_16x16x32_bf16 v[252:255], v[124:127], v[160:163], v[252:255]\n\t" \
  "ds_read_b128 v[136:139], v22 offset:10240\n\t" \
  "v_mfma_f32_16x16x32_bf16 v[252:255], v[100:103], v[184:187], v[252:255]\n\t" \
  "ds_read_b128 v[132:135], v22 offset:12288\n\t" \
  "s_waitcnt lgkmcnt(9)\n\t" \
  "v_mfma_f32_16x16x32_bf16 v[252:255], v[116:119], v[168:171], v[252:255]\n\t" \
  "ds_read_b128 v[72:75], v28 offset:1152\n\t" \
  "v_mfma_f32_16x16x32_bf16 v[252:255], v[120:123], v[164:167], v[252:255]\n\t" \
  "v_mfma_f32_16x16x32_bf16 v[252:255], v[104:107], v[180:183], v[252:255]\n\t" \
  "ds_read_b128 v[156:159], v22 offset:0\n\t" \
  "s_waitcnt lgkmcnt(7)\n\t" \
  "v_mfma_f32_16x16x32_bf16 v[240:243], v[64:67], v[128:131], v[240:243]\n\t" \
  "ds_read_b128 v[92:95], v28 offset:3712\n\t" \
  "v_mfma_f32_16x16x32_bf16 v[244:247], v[64:67], v[140:143], v[244:247]\n\t" \
  "ds_read_b128 v[152:155], v22 offset:2048\n\t" \
  "v_mfma_f32_16x16x32_bf16 v[244:247], v[76:79], v[128:131], v[244:247]\n\t" \
  "ds_read_b128 v[84:87], v28 offset:2688\n\t" \
  "s_waitcnt lgkmcnt(8)\n\t" \
  "v_mfma_f32_16x16x32_bf16 v[248:251], v[64:67], v[144:147], v[248:251]\n\t" \
  "s_add_u32 m0, s50, 4096\n\t" \
  "s_add_u32 s44, s49, 1024\n\t" \
  "v_mfma_f32_16x16x32_bf16 v[252:255], v[76:79], v[144:147], v[252:255]\n\t" \
  "buffer_load_dwordx4 v29, %3, s44 offen lds\n\t" \
  "v_mfma_f32_16x16x32_bf16 v[248:251], v[80:83], v[128:131], v[248:251]\n\t" \
  "ds_read_b128 v[88:91], v28 offset:3200\n\t" \
  "s_waitcnt lgkmcnt(7)\n\t" \
  "v_mfma_f32_16x16x32_bf16 v[252:255], v[80:83], v[140:143], v[252:255]\n\t" \
  "ds_read_b128 v[148:151], v22 offset:4096\n\t" \
  "ds_read_b128 v[96:99], v28 offset:192\n\t" \
  "v_mfma_f32_16x16x32_bf16 v[248:251], v[68:71], v[140:143], v[248:251]\n\t" \
  "ds_read_b128 v[160:163], v23 offset:14336\n\t" \
  "ds_read_b128 v[172:175], v23 offset:8192\n\t" \
  "v_mfma_f32_16x16x32_bf16 v[244:247], v[68:71], v[136:139], v[244:247]\n\t" \
  "ds_read_b128 v[108:111], v28 offset:1728\n\t" \
  "s_waitcnt lgkmcnt(10)\n\t" \
  "v_mfma_f32_16x16x32_bf16 v[248:251], v[76:79], v[132:135], v[248:251]\n\t" \
  "s_add_u32 m0, s50, 5120\n\t" \
  "s_add_u32 s44, s49, 1280\n\t" \
  "v_mfma_f32_16x16x32_bf16 v[244:247], v[72:75], v[132:135], v[244:247]\n\t" \
  "buffer_load_dwordx4 v30, %3, s44 offen lds\n\t" \
  "v_mfma_f32_16x16x32_bf16 v[248:251], v[72:75], v[136:139], v[248:251]\n\t" \
  "ds_read_b128 v[176:179], v23 offset:6144\n\t" \
  "s_waitcnt lgkmcnt(8)\n\t" \
  "v_mfma_f32_16x16x32_bf16 v[252:255], v[64:67], v[156:159], v[252:255]\n\t" \
  "ds_read_b128 v[112:115], v28 offset:2240\n\t" \
  "ds_read_b128 v[100:103], v28 offset:704\n\t" \
  "v_mfma_f32_16x16x32_bf16 v[252:255], v[92:95], v[128:131], v[252:255]\n\t" \
  "ds_read_b128 v[168:171], v23 offset:10240\n\t" \
  "v_mfma_f32_16x16x32_bf16 v[252:255], v[68:71], v[152:155], v[252:255]\n\t" \
  "ds_read_b128 v[164:167], v23 offset:12288\n\t" \
  "s_waitcnt lgkmcnt(9)\n\t" \
  "v_mfma_f32_16x16x32_bf16 v[252:255], v[84:87], v[136:139], v[252:255]\n\t" \
  "ds_read_b128 v[104:107], v28 offset:1216\n\t" \
  "v_mfma_f32_16x16x32_bf16 v[252:255], v[88:91], v[132:135], v[252:255]\n\t" \
  "v_mfma_f32_16x16x32_bf16 v[252:255], v[72:75], v[148:151], v[252:255]\n\t" \
  "ds_read_b128 v[188:191], v23 offset:0\n\t" \
  "s_waitcnt lgkmcnt(7)\n\t" \
  "v_mfma_f32_16x16x32_bf16 v[240:243], v[96:99], v[160:163], v[240:243]\n\t" \
  "ds_read_b128 v[124:127], v28 offset:3776\n\t" \
  "v_mfma_f32_16x16x32_bf16 v[244:247], v[96:99], v[172:175], v[244:247]\n\t" \
  "ds_read_b128 v[184:187], v23 offset:2048\n\t" \
  "v_mfma_f32_16x16x32_bf16 v[244:247], v[108:111], v[160:163], v[244:247]\n\t" \
  "ds_read_b128 v[116:119], v28 offset:2752\n\t" \
  "s_waitcnt lgkmcnt(8)\n\t" \
  "v_mfma_f32_16x16x32_bf16 v[248:251], v[96:99], v[176:179], v[248:251]\n\t" \
  "s_add_u32 m0, s50, 6144\n\t" \
  "s_add_u32 s44, s49, 1536\n\t" \
  "v_mfma_f32_16x16x32_bf16 v[252:255], v[108:111], v[176:179], v[252:255]\n\t" \
  "buffer_load_dwordx4 v29, %3, s44 offen lds\n\t" \
  "v_mfma_f32_16x16x32_bf16 v[248:251], v[112:115], v[160:163], v[248:251]\n\t" \
  "ds_read_b128 v[120:123], v28 offset:3264\n\t" \
  "s_waitcnt lgkmcnt(7)\n\t" \
  "v_mfma_f32_16x16x32_bf16 v[252:255], v[112:115], v[172:175], v[252:255]\n\t" \
  "ds_read_b128 v[180:183], v23 offset:4096\n\t" \
  "ds_read_b128 v[64:67], v28 offset:256\n\t" \
  "v_mfma_f32_16x16x32_bf16 v[248:251], v[100:103], v[172:175], v[248:251]\n\t" \
  "ds_read_b128 v[128:131], v24 offset:14336\n\t" \
  "ds_read_b128 v[140:143], v24 offset:8192\n\t" \
  "v_mfma_f32_16x16x32_bf16 v[244:247], v[100:103], v[168:171], v[244:247]\n\t" \
  "ds_read_b128 v[76:79], v28 offset:1792\n\t" \
  "s_waitcnt lgkmcnt(10)\n\t" \
  "v_mfma_f32_16x16x32_bf16 v[248:251], v[108:111], v[164:167], v[248:251]\n\t" \
  "s_add_u32 m0, s50, 7168\n\t" \
  "s_add_u32 s44, s49, 1792\n\t" \
  "v_mfma_f32_16x16x32_bf16 v[244:247], v[104:107], v[164:167], v[244:247]\n\t" \
  "buffer_load_dwordx4 v30, %3, s44 offen lds\n\t" \
  "v_mfma_f32_16x16x32_bf16 v[248:251], v[104:107], v[168:171], v[248:251]\n\t" \
  "ds_read_b128 v[144:147], v24 offset:6144\n\t" \
  "s_waitcnt lgkmcnt(8)\n\t" \
  "v_mfma_f32_16x16x32_bf16 v[252:255], v[96:99], v[188:191], v[252:255]\n\t" \
  "ds_read_b128 v[80:83], v28 offset:2304\n\t" \
  "ds_read_b128 v[68:71], v28 offset:768\n\t" \
  "v_mfma_f32_16x16x32_bf16 v[252:255], v[124:127], v[160:163], v[252:255]\n\t" \
  "ds_read_b128 v[136:139], v24 offset:10240\n\t" \
  "v_mfma_f32_16x16x32_bf16 v[252:255], v[100:103], v[184:187], v[252:255]\n\t" \
  "ds_read_b128 v[132:135], v24 offset:12288\n\t" \
  "s_waitcnt lgkmcnt(9)\n\t" \
  "v_mfma_f32_16x16x32_bf16 v[252:255], v[116:119], v[168:171], v[252:255]\n\t" \
  "ds_read_b128 v[72:75], v28 offset:1280\n\t" \
  "v_mfma_f32_16x16x32_bf16 v[252:255], v[120:123], v[164:167], v[252:255]\n\t" \
  "v_mfma_f32_16x16x32_bf16 v[252:255], v[104:107], v[180:183], v[252:255]\n\t" \
  "ds_read_b128 v[156:159], v24 offset:0\n\t" \
  "s_waitcnt lgkmcnt(7)\n\t" \
  "v_mfma_f32_16x16x32_bf16 v[240:243], v[64:67], v[128:131], v[240:243]\n\t" \
  "ds_read_b128 v[92:95], v28 offset:3840\n\t" \
  "v_mfma_f32_16x16x32_bf16 v[244:247], v[64:67], v[140:143], v[244:247]\n\t" \
  "ds_read_b128 v[152:155], v24 offset:2048\n\t" \
  "v_mfma_f32_16x16x32_bf16 v[244:247], v[76:79], v[128:131], v[244:247]\n\t" \
  "ds_read_b128 v[84:87], v28 offset:2816\n\t" \
  "s_waitcnt lgkmcnt(8)\n\t" \
  "v_mfma_f32_16x16x32_bf16 v[248:251], v[64:67], v[144:147], v[248:251]\n\t" \
  "ds_read_b128 v[88:91], v28 offset:3328\n\t" \
  "v_mfma_f32_16x16x32_bf16 v[252:255], v[76:79], v[144:147], v[252:255]\n\t" \
  "ds_read_b128 v[148:151], v24 offset:4096\n\t" \
  "v_mfma_f32_16x16x32_bf16 v[248:251], v[80:83], v[128:131], v[248:251]\n\t" \
  "ds_read_b128 v[96:99], v28 offset:320\n\t" \
  "s_waitcnt lgkmcnt(9)\n\t" \
  "v_mfma_f32_16x16x32_bf16 v[252:255], v[80:83], v[140:143], v[252:255]\n\t" \
  "ds_read_b128 v[160:163], v25 offset:14336\n\t" \
  "ds_read_b128 v[172:175], v25 offset:8192\n\t" \
  "v_mfma_f32_16x16x32_bf16 v[248:251], v[68:71], v[140:143], v[248:251]\n\t" \
  "ds_read_b128 v[108:111], v28 offset:1856\n\t" \
  "v_mfma_f32_16x16x32_bf16 v[244:247], v[68:71], v[136:139], v[244:247]\n\t" \
  "ds_read_b128 v[176:179], v25 offset:6144\n\t" \
  "s_waitcnt lgkmcnt(11)\n\t" \
  "v_mfma_f32_16x16x32_bf16 v[248:251], v[76:79], v[132:135], v[248:251]\n\t" \
  "v_mfma_f32_16x16x32_bf16 v[244:247], v[72:75], v[132:135], v[244:247]\n\t" \
  "ds_read_b128 v[112:115], v28 offset:2368\n\t" \
  "v_mfma_f32_16x16x32_bf16 v[248:251], v[72:75], v[136:139], v[248:251]\n\t" \
  "s_waitcnt lgkmcnt(9)\n\t" \
  "v_mfma_f32_16x16x32_bf16 v[252:255], v[64:67], v[156:159], v[252:255]\n\t" \
  "ds_read_b128 v[100:103], v28 offset:832\n\t" \
  "v_mfma_f32_16x16x32_bf16 v[252:255], v[92:95], v[128:131], v[252:255]\n\t" \
  "ds_read_b128 v[168:171], v25 offset:10240\n\t" \
  "v_mfma_f32_16x16x32_bf16 v[252:255], v[68:71], v[152:155], v[252:255]\n\t" \
  "ds_read_b128 v[164:167], v25 offset:12288\n\t" \
  "s_waitcnt lgkmcnt(9)\n\t" \
  "v_mfma_f32_16x16x32_bf16 v[252:255], v[84:87], v[136:139], v[252:255]\n\t" \
  "ds_read_b128 v[104:107], v28 offset:1344\n\t" \
  "v_mfma_f32_16x16x32_bf16 v[252:255], v[88:91], v[132:135], v[252:255]\n\t" \
  "v_mfma_f32_16x16x32_bf16 v[252:255], v[72:75], v[148:151], v[252:255]\n\t" \
  "ds_read_b128 v[188:191], v25 offset:0\n\t" \
  "s_waitcnt lgkmcnt(7)\n\t" \
  "v_mfma_f32_16x16x32_bf16 v[240:243], v[96:99], v[160:163], v[240:243]\n\t" \
  "ds_read_b128 v[124:127], v28 offset:3904\n\t" \
  "v_mfma_f32_16x16x32_bf16 v[244:247], v[96:99], v[172:175], v[244:247]\n\t" \
  "ds_read_b128 v[184:187], v25 offset:2048\n\t" \
  "v_mfma_f32_16x16x32_bf16 v[244:247], v[108:111], v[160:163], v[244:247]\n\t" \
  "ds_read_b128 v[116:119], v28 offset:2880\n\t" \
  "s_waitcnt lgkmcnt(8)\n\t" \
  "v_mfma_f32_16x16x32_bf16 v[248:251], v[96:99], v[176:179], v[248:251]\n\t" \
  "ds_read_b128 v[120:123], v28 offset:3392\n\t" \
  "v_mfma_f32_16x16x32_bf16 v[252:255], v[108:111], v[176:179], v[252:255]\n\t" \
  "ds_read_b128 v[180:183], v25 offset:4096\n\t" \
  "v_mfma_f32_16x16x32_bf16 v[248:251], v[112:115], v[160:163], v[248:251]\n\t" \
  "ds_read_b128 v[64:67], v28 offset:384\n\t" \
  "s_waitcnt lgkmcnt(9)\n\t" \
  "v_mfma_f32_16x16x32_bf16 v[252:255], v[112:115], v[172:175], v[252:255]\n\t" \
  "ds_read_b128 v[128:131], v26 offset:14336\n\t" \
  "ds_read_b128 v[140:143], v26 offset:8192\n\t" \
  "v_mfma_f32_16x16x32_bf16 v[248:251], v[100:103], v[172:175], v[248:251]\n\t" \
  "ds_read_b128 v[76:79], v28 offset:1920\n\t" \
  "v_mfma_f32_16x16x32_bf16 v[244:247], v[100:103], v[168:171], v[244:247]\n\t" \
  "ds_read_b128 v[144:147], v26 offset:6144\n\t" \
  "s_waitcnt lgkmcnt(11)\n\t" \
  "v_mfma_f32_16x16x32_bf16 v[248:251], v[108:111], v[164:167], v[248:251]\n\t" \
  "v_mfma_f32_16x16x32_bf16 v[244:247], v[104:107], v[164:167], v[244:247]\n\t" \
  "ds_read_b128 v[80:83], v28 offset:2432\n\t" \
  "v_mfma_f32_16x16x32_bf16 v[248:251], v[104:107], v[168:171], v[248:251]\n\t" \
  "s_waitcnt lgkmcnt(9)\n\t" \
  "v_mfma_f32_16x16x32_bf16 v[252:255], v[96:99], v[188:191], v[252:255]\n\t" \
  "ds_read_b128 v[68:71], v28 offset:896\n\t" \
  "v_mfma_f32_16x16x32_bf16 v[252:255], v[124:127], v[160:163], v[252:255]\n\t" \
  "ds_read_b128 v[136:139], v26 offset:10240\n\t" \
  "v_mfma_f32_16x16x32_bf16 v[252:255], v[100:103], v[184:187], v[252:255]\n\t" \
  "ds_read_b128 v[132:135], v26 offset:12288\n\t" \
  "s_waitcnt lgkmcnt(9)\n\t" \
  "v_mfma_f32_16x16x32_bf16 v[252:255], v[116:119], v[168:171], v[252:255]\n\t" \
  "ds_read_b128 v[72:75], v28 offset:1408\n\t" \
  "v_mfma_f32_16x16x32_bf16 v[252:255], v[120:123], v[164:167], v[252:255]\n\t" \
  "v_mfma_f32_16x16x32_bf16 v[252:255], v[104:107], v[180:183], v[252:255]\n\t" \
  "ds_read_b128 v[156:159], v26 offset:0\n\t" \
  "s_waitcnt lgkmcnt(7)\n\t" \
  "v_mfma_f32_16x16x32_bf16 v[240:243], v[64:67], v[128:131], v[240:243]\n\t" \
  "ds_read_b128 v[92:95], v28 offset:3968\n\t" \
  "v_mfma_f32_16x16x32_bf16 v[244:247], v[64:67], v[140:143], v[244:247]\n\t" \
  "ds_read_b128 v[152:155], v26 offset:2048\n\t" \
  "v_mfma_f32_16x16x32_bf16 v[244:247], v[76:79], v[128:131], v[244:247]\n\t" \
  "ds_read_b128 v[84:87], v28 offset:2944\n\t" \
  "s_waitcnt lgkmcnt(8)\n\t" \
  "v_mfma_f32_16x16x32_bf16 v[248:251], v[64:67], v[144:147], v[248:251]\n\t" \
  "ds_read_b128 v[88:91], v28 offset:3456\n\t" \
  "v_mfma_f32_16x16x32_bf16 v[252:255], v[76:79], v[144:147], v[252:255]\n\t" \
  "ds_read_b128 v[148:151], v26 offset:4096\n\t" \
  "v_mfma_f32_16x16x32_bf16 v[248:251], v[80:83], v[128:131], v[248:251]\n\t" \
  "ds_read_b128 v[96:99], v28 offset:448\n\t" \
  "s_waitcnt lgkmcnt(9)\n\t" \
  "v_mfma_f32_16x16x32_bf16 v[252:255], v[80:83], v[140:143], v[252:255]\n\t" \
  "ds_read_b128 v[160:163], v27 offset:14336\n\t" \
  "ds_read_b128 v[172:175], v27 offset:8192\n\t" \
  "v_mfma_f32_16x16x32_bf16 v[248:251], v[68:71], v[140:143], v[248:251]\n\t" \
  "ds_read_b128 v[108:111], v28 offset:1984\n\t" \
  "v_mfma_f32_16x16x32_bf16 v[244:247], v[68:71], v[136:139], v[244:247]\n\t" \
  "ds_read_b128 v[176:179], v27 offset:6144\n\t" \
  "s_waitcnt lgkmcnt(11)\n\t" \
  "v_mfma_f32_16x16x32_bf16 v[248:251], v[76:79], v[132:135], v[248:251]\n\t" \
  "v_mfma_f32_16x16x32_bf16 v[244:247], v[72:75], v[132:135], v[244:247]\n\t" \
  "ds_read_b128 v[112:115], v28 offset:2496\n\t" \
  "v_mfma_f32_16x16x32_bf16 v[248:251], v[72:75], v[136:139], v[248:251]\n\t" \
  "s_waitcnt lgkmcnt(9)\n\t" \
  "v_mfma_f32_16x16x32_bf16 v[252:255], v[64:67], v[156:159], v[252:255]\n\t" \
  "ds_read_b128 v[100:103], v28 offset:960\n\t" \
  "v_mfma_f32_16x16x32_bf16 v[252:255], v[92:95], v[128:131], v[252:255]\n\t" \
  "ds_read_b128 v[168:171], v27 offset:10240\n\t" \
  "v_mfma_f32_16x16x32_bf16 v[252:255], v[68:71], v[152:155], v[252:255]\n\t" \
  "ds_read_b128 v[164:167], v27 offset:12288\n\t" \
  "s_waitcnt lgkmcnt(9)\n\t" \
  "v_mfma_f32_16x16x32_bf16 v[252:255], v[84:87], v[136:139], v[252:255]\n\t" \
  "ds_read_b128 v[104:107], v28 offset:1472\n\t" \
  "v_mfma_f32_16x16x32_bf16 v[252:255], v[88:91], v[132:135], v[252:255]\n\t" \
  "v_mfma_f32_16x16x32_bf16 v[252:255], v[72:75], v[148:151], v[252:255]\n\t" \
  "ds_read_b128 v[188:191], v27 offset:0\n\t" \
  "s_waitcnt lgkmcnt(7)\n\t" \
  "v_mfma_f32_16x16x32_bf16 v[240:243], v[96:99], v[160:163], v[240:243]\n\t" \
  "ds_read_b128 v[124:127], v28 offset:4032\n\t" \
  "v_mfma_f32_16x16x32_bf16 v[244:247], v[96:99], v[172:175], v[244:247]\n\t" \
  "ds_read_b128 v[184:187], v27 offset:2048\n\t" \
  "v_mfma_f32_16x16x32_bf16 v[244:247], v[108:111], v[160:163], v[244:247]\n\t" \
  "ds_read_b128 v[116:119], v28 offset:3008\n\t" \
  "s_waitcnt lgkmcnt(8)\n\t" \
  "v_mfma_f32_16x16x32_bf16 v[248:251], v[96:99], v[176:179], v[248:251]\n\t" \
  "ds_read_b128 v[120:123], v28 offset:3520\n\t" \
  "v_mfma_f32_16x16x32_bf16 v[252:255], v[108:111], v[176:179], v[252:255]\n\t" \
  "ds_read_b128 v[180:183], v27 offset:4096\n\t" \
  "v_mfma_f32_16x16x32_bf16 v[248:251], v[112:115], v[160:163], v[248:251]\n\t" \
  "v_add_u32 v20, s63, v20\n\t" \
  "s_waitcnt lgkmcnt(8)\n\t" \
  "v_mfma_f32_16x16x32_bf16 v[252:255], v[112:115], v[172:175], v[252:255]\n\t" \
  "v_add_u32 v21, s63, v21\n\t" \
  "v_add_u32 v22, s63, v22\n\t" \
  "v_mfma_f32_16x16x32_bf16 v[248:251], v[100:103], v[172:175], v[248:251]\n\t" \
  "v_add_u32 v23, s63, v23\n\t" \
  "v_add_u32 v24, s63, v24\n\t" \
  "v_mfma_f32_16x16x32_bf16 v[244:247], v[100:103], v[168:171], v[244:247]\n\t" \
  "v_add_u32 v25, s63, v25\n\t" \
  "s_waitcnt lgkmcnt(6)\n\t" \
  "v_mfma_f32_16x16x32_bf16 v[248:251], v[108:111], v[164:167], v[248:251]\n\t" \
  "v_add_u32 v54, s62, v44\n\t" \
  "v_add_u32 v62, s62, v45\n\t" \
  "v_mfma_f32_16x16x32_bf16 v[244:247], v[104:107], v[164:167], v[244:247]\n\t" \
  "v_add_u32 v26, s63, v26\n\t" \
  "v_add_u32 v27, s63, v27\n\t" \
  "v_mfma_f32_16x16x32_bf16 v[248:251], v[104:107], v[168:171], v[248:251]\n\t" \
  "v_add_u32 v28, 16896, v28\n\t" \
  "s_waitcnt lgkmcnt(3)\n\t" \
  "v_mfma_f32_16x16x32_bf16 v[252:255], v[96:99], v[188:191], v[252:255]\n\t" \
  "v_mfma_f32_16x16x32_bf16 v[252:255], v[124:127], v[160:163], v[252:255]\n\t" \
  "v_mfma_f32_16x16x32_bf16 v[252:255], v[100:103], v[184:187], v[252:255]\n\t" \
  "s_waitcnt lgkmcnt(0)\n\t" \
  "v_mfma_f32_16x16x32_bf16 v[252:255], v[116:119], v[168:171], v[252:255]\n\t" \
  "v_mfma_f32_16x16x32_bf16 v[252:255], v[120:123], v[164:167], v[252:255]\n\t" \
  "v_mfma_f32_16x16x32_bf16 v[252:255], v[104:107], v[180:183], v[252:255]\n\t" \
  "s_nop 7\n\t" \
  "v_cvt_pk_bf16_f32 v240, v240, v241\n\t" \
  "v_cvt_pk_bf16_f32 v241, v242, v243\n\t" \
  "v_cvt_pk_bf16_f32 v244, v244, v245\n\t" \
  "v_cvt_pk_bf16_f32 v245, v246, v247\n\t" \
  "v_cvt_pk_bf16_f32 v248, v248, v249\n\t" \
  "v_cvt_pk_bf16_f32 v249, v250, v251\n\t" \
  "v_cvt_pk_bf16_f32 v252, v252, v253\n\t" \
  "v_cvt_pk_bf16_f32 v253, v254, v255\n\t" \
  "ds_write_b32 v54, v240 offset:0\n\t" \
  "ds_write_b32 v54, v241 offset:16\n\t" \
  "ds_write_b32 v54, v244 offset:1728\n\t" \
  "ds_write_b32 v54, v245 offset:1744\n\t" \
  "ds_write_b32 v54, v248 offset:2304\n\t" \
  "ds_write_b32 v54, v249 offset:2320\n\t" \
  "ds_write_b32 v54, v252 offset:4032\n\t" \
  "ds_write_b32 v54, v253 offset:4048\n\t" \
  "s_branch L_cv_done_%=\n\t" \
  "L_cv_y_%=:\n\t" \
  "ds_read_b128 v[64:67], v28 offset:0\n\t" \
  "ds_read_b128 v[132:135], v20 offset:12288\n\t" \
  "ds_read_b128 v[136:139], v20 offset:10240\n\t" \
  "ds_read_b128 v[68:71], v28 offset:512\n\t" \
  "ds_read_b128 v[128:131], v20 offset:14336\n\t" \
  "ds_read_b128 v[72:75], v28 offset:1024\n\t" \
  "ds_read_b128 v[148:151], v20 offset:4096\n\t" \
  "ds_read_b128 v[80:83], v28 offset:2048\n\t" \
  "ds_read_b128 v[76:79], v28 offset:1536\n\t" \
  "ds_read_b128 v[224:227], v62\n\t" \
  "ds_read_b128 v[228:231], v62 offset:2304\n\t" \
  "s_waitcnt lgkmcnt(7)\n\t" \
  "v_mfma_f32_16x16x32_bf16 v[240:243], v[64:67], v[132:135], 0\n\t" \
  "ds_read_b128 v[144:147], v20 offset:6144\n\t" \
  "ds_read_b128 v[84:87], v28 offset:2560\n\t" \
  "v_mfma_f32_16x16x32_bf16 v[244:247], v[64:67], v[136:139], 0\n\t" \
  "ds_read_b128 v[140:143], v20 offset:8192\n\t" \
  "v_mfma_f32_16x16x32_bf16 v[244:247], v[68:71], v[132:135], v[244:247]\n\t" \
  "s_waitcnt lgkmcnt(7)\n\t" \
  "v_mfma_f32_16x16x32_bf16 v[240:243], v[68:71], v[128:131], v[240:243]\n\t" \
  "s_add_u32 m0, s50, 0\n\t" \
  "s_add_u32 s44, s49, 0\n\t" \
  "v_mfma_f32_16x16x32_bf16 v[244:247], v[72:75], v[128:131], v[244:247]\n\t" \
  "buffer_load_dwordx4 v29, %3, s44 offen lds\n\t" \
  "v_mfma_f32_16x16x32_bf16 v[248:251], v[64:67], v[148:151], 0\n\t" \
  "ds_read_b128 v[152:155], v20 offset:2048\n\t" \
  "s_waitcnt lgkmcnt(4)\n\t" \
  "v_mfma_f32_16x16x32_bf16 v[252:255], v[68:71], v[148:151], 0\n\t" \
  "ds_read_b128 v[88:91], v28 offset:3072\n\t" \
  "ds_read_b128 v[96:99], v28 offset:64\n\t" \
  "v_mfma_f32_16x16x32_bf16 v[248:251], v[80:83], v[132:135], v[248:251]\n\t" \
  "ds_read_b128 v[164:167], v21 offset:12288\n\t" \
  "ds_read_b128 v[168:171], v21 offset:10240\n\t" \
  "v_mfma_f32_16x16x32_bf16 v[252:255], v[80:83], v[136:139], v[252:255]\n\t" \
  "ds_read_b128 v[100:103], v28 offset:576\n\t" \
  "s_waitcnt lgkmcnt(8)\n\t" \
  "v_mfma_f32_16x16x32_bf16 v[248:251], v[76:79], v[136:139], v[248:251]\n\t" \
  "s_add_u32 m0, s50, 1024\n\t" \
  "s_add_u32 s44, s49, 256\n\t" \
  "v_mfma_f32_16x16x32_bf16 v[248:251], v[68:71], v[144:147], v[248:251]\n\t" \
  "buffer_load_dwordx4 v30, %3, s44 offen lds\n\t" \
  "v_mfma_f32_16x16x32_bf16 v[252:255], v[72:75], v[144:147], v[252:255]\n\t" \
  "ds_read_b128 v[160:163], v21 offset:14336\n\t" \
  "s_waitcnt lgkmcnt(7)\n\t" \
  "v_mfma_f32_16x16x32_bf16 v[248:251], v[84:87], v[128:131], v[248:251]\n\t" \
  "ds_read_b128 v[104:107], v28 offset:1088\n\t" \
  "ds_read_b128 v[180:183], v21 offset:4096\n\t" \
  "v_mfma_f32_16x16x32_bf16 v[252:255], v[84:87], v[132:135], v[252:255]\n\t" \
  "buffer_store_dwordx4 v[224:227], v53, %3, s61 offen offset:0 sc1\n\t" \
  "v_mfma_f32_16x16x32_bf16 v[248:251], v[72:75], v[140:143], v[248:251]\n\t" \
  "ds_read_b128 v[112:115], v28 offset:2112\n\t" \
  "s_waitcnt lgkmcnt(8)\n\t" \
  "v_mfma_f32_16x16x32_bf16 v[252:255], v[76:79], v[140:143], v[252:255]\n\t" \
  "ds_read_b128 v[108:111], v28 offset:1600\n\t" \
  "ds_read_b128 v[176:179], v21 offset:6144\n\t" \
  "v_mfma_f32_16x16x32_bf16 v[252:255], v[64:67], v[152:155], v[252:255]\n\t" \
  "buffer_store_dwordx4 v[228:231], v53, %3, s61 offen offset:2048 sc1\n\t" \
  "v_mfma_f32_16x16x32_bf16 v[252:255], v[88:91], v[128:131], v[252:255]\n\t" \
  "ds_read_b128 v[116:119], v28 offset:2624\n\t" \
  "s_waitcnt lgkmcnt(7)\n\t" \
  "v_mfma_f32_16x16x32_bf16 v[240:243], v[96:99], v[164:167], v[240:243]\n\t" \
  "v_mfma_f32_16x16x32_bf16 v[244:247], v[96:99], v[168:171], v[244:247]\n\t" \
  "ds_read_b128 v[172:175], v21 offset:8192\n\t" \
  "v_mfma_f32_16x16x32_bf16 v[244:247], v[100:103], v[164:167], v[244:247]\n\t" \
  "s_waitcnt lgkmcnt(5)\n\t" \
  "v_mfma_f32_16x16x32_bf16 v[240:243], v[100:103], v[160:163], v[240:243]\n\t" \
  "s_add_u32 m0, s50, 2048\n\t" \
  "s_add_u32 s44, s49, 512\n\t" \
  "v_mfma_f32_16x16x32_bf16 v[244:247], v[104:107], v[160:163], v[244:247]\n\t" \
  "buffer_load_dwordx4 v29, %3, s44 offen lds\n\t" \
  "v_mfma_f32_16x16x32_bf16 v[248:251], v[96:99], v[180:183], v[248:251]\n\t" \
  "ds_read_b128 v[184:187], v21 offset:2048\n\t" \
  "s_waitcnt lgkmcnt(5)\n\t" \
  "v_mfma_f32_16x16x32_bf16 v[252:255], v[100:103], v[180:183], v[252:255]\n\t" \
  "ds_read_b128 v[120:123], v28 offset:3136\n\t" \
  "ds_read_b128 v[64:67], v28 offset:128\n\t" \
  "v_mfma_f32_16x16x32_bf16 v[248:251], v[112:115], v[164:167], v[248:251]\n\t" \
  "ds_read_b128 v[132:135], v22 offset:12288\n\t" \
  "ds_read_b128 v[136:139], v22 offset:10240\n\t" \
  "v_mfma_f32_16x16x32_bf16 v[252:255], v[112:115], v[168:171], v[252:255]\n\t" \
  "ds_read_b128 v[68:71], v28 offset:640\n\t" \
  "s_waitcnt lgkmcnt(8)\n\t" \
  "v_mfma_f32_16x16x32_bf16 v[248:251], v[108:111], v[168:171], v[248:251]\n\t" \
  "s_add_u32 m0, s50, 3072\n\t" \
  "s_add_u32 s44, s49, 768\n\t" \
  "v_mfma_f32_16x16x32_bf16 v[248:251], v[100:103], v[176:179], v[248:251]\n\t" \
  "buffer_load_dwordx4 v30, %3, s44 offen lds\n\t" \
  "v_mfma_f32_16x16x32_bf16 v[252:255], v[104:107], v[176:179], v[252:255]\n\t" \
  "ds_read_b128 v[128:131], v22 offset:14336\n\t" \
  "s_waitcnt lgkmcnt(7)\n\t" \
  "v_mfma_f32_16x16x32_bf16 v[248:251], v[116:119], v[160:163], v[248:251]\n\t" \
  "ds_read_b128 v[72:75], v28 offset:1152\n\t" \
  "ds_read_b128 v[148:151], v22 offset:4096\n\t" \
  "v_mfma_f32_16x16x32_bf16 v[252:255], v[116:119], v[164:167], v[252:255]\n\t" \
  "ds_read_b128 v[80:83], v28 offset:2176\n\t" \
  "v_mfma_f32_16x16x32_bf16 v[248:251], v[104:107], v[172:175], v[248:251]\n\t" \
  "ds_read_b128 v[76:79], v28 offset:1664\n\t" \
  "s_waitcnt lgkmcnt(9)\n\t" \
  "v_mfma_f32_16x16x32_bf16 v[252:255], v[108:111], v[172:175], v[252:255]\n\t" \
  "ds_read_b128 v[144:147], v22 offset:6144\n\t" \
  "v_mfma_f32_16x16x32_bf16 v[252:255], v[96:99], v[184:187], v[252:255]\n\t" \
  "v_mfma_f32_16x16x32_bf16 v[252:255], v[120:123], v[160:163], v[252:255]\n\t" \
  "ds_read_b128 v[84:87], v28 offset:2688\n\t" \
  "s_waitcnt lgkmcnt(7)\n\t" \
  "v_mfma_f32_16x16x32_bf16 v[240:243], v[64:67], v[132:135], v[240:243]\n\t" \
  "v_mfma_f32_16x16x32_bf16 v[244:247], v[64:67], v[136:139], v[244:247]\n\t" \
  "ds_read_b128 v[140:143], v22 offset:8192\n\t" \
  "v_mfma_f32_16x16x32_bf16 v[244:247], v[68:71], v[132:135], v[244:247]\n\t" \
  "s_waitcnt lgkmcnt(5)\n\t" \
  "v_mfma_f32_16x16x32_bf16 v[240:243], v[68:71], v[128:131], v[240:243]\n\t" \
  "s_add_u32 m0, s50, 4096\n\t" \
  "s_add_u32 s44, s49, 1024\n\t" \
  "v_mfma_f32_16x16x32_bf16 v[244:247], v[72:75], v[128:131], v[244:247]\n\t" \
  "buffer_load_dwordx4 v29, %3, s44 offen lds\n\t" \
  "v_mfma_f32_16x16x32_bf16 v[248:251], v[64:67], v[148:151], v[248:251]\n\t" \
  "ds_read_b128 v[152:155], v22 offset:2048\n\t" \
  "s_waitcnt lgkmcnt(5)\n\t" \
  "v_mfma_f32_16x16x32_bf16 v[252:255], v[68:71], v[148:151], v[252:255]\n\t" \
  "ds_read_b128 v[88:91], v28 offset:3200\n\t" \
  "ds_read_b128 v[96:99], v28 offset:192\n\t" \
  "v_mfma_f32_16x16x32_bf16 v[248:251], v[80:83], v[132:135], v[248:251]\n\t" \
  "ds_read_b128 v[164:167], v23 offset:12288\n\t" \
  "ds_read_b128 v[168:171], v23 offset:10240\n\t" \
  "v_mfma_f32_16x16x32_bf16 v[252:255], v[80:83], v[136:139], v[252:255]\n\t" \
  "ds_read_b128 v[100:103], v28 offset:704\n\t" \
  "s_waitcnt lgkmcnt(8)\n\t" \
  "v_mfma_f32_16x16x32_bf16 v[248:251], v[76:79], v[136:139], v[248:251]\n\t" \
  "s_add_u32 m0, s50, 5120\n\t" \
  "s_add_u32 s44, s49, 1280\n\t" \
  "v_mfma_f32_16x16x32_bf16 v[248:251], v[68:71], v[144:147], v[248:251]\n\t" \
  "buffer_load_dwordx4 v30, %3, s44 offen lds\n\t" \
  "v_mfma_f32_16x16x32_bf16 v[252:255], v[72:75], v[144:147], v[252:255]\n\t" \
  "ds_read_b128 v[160:163], v23 offset:14336\n\t" \
  "s_waitcnt lgkmcnt(7)\n\t" \
  "v_mfma_f32_16x16x32_bf16 v[248:251], v[84:87], v[128:131], v[248:251]\n\t" \
  "ds_read_b128 v[104:107], v28 offset:1216\n\t" \
  "ds_read_b128 v[180:183], v23 offset:4096\n\t" \
  "v_mfma_f32_16x16x32_bf16 v[252:255], v[84:87], v[132:135], v[252:255]\n\t" \
  "ds_read_b128 v[112:115], v28 offset:2240\n\t" \
  "v_mfma_f32_16x16x32_bf16 v[248:251], v[72:75], v[140:143], v[248:251]\n\t" \
  "ds_read_b128 v[108:111], v28 offset:1728\n\t" \
  "s_waitcnt lgkmcnt(9)\n\t" \
  "v_mfma_f32_16x16x32_bf16 v[252:255], v[76:79], v[140:143], v[252:255]\n\t" \
  "ds_read_b128 v[176:179], v23 offset:6144\n\t" \
  "v_mfma_f32_16x16x32_bf16 v[252:255], v[64:67], v[152:155], v[252:255]\n\t" \
  "v_mfma_f32_16x16x32_bf16 v[252:255], v[88:91], v[128:131], v[252:255]\n\t" \
  "ds_read_b128 v[116:119], v28 offset:2752\n\t" \
  "s_waitcnt lgkmcnt(7)\n\t" \
  "v_mfma_f32_16x16x32_bf16 v[240:243], v[96:99], v[164:167], v[240:243]\n\t" \
  "v_mfma_f32_16x16x32_bf16 v[244:247], v[96:99], v[168:171], v[244:247]\n\t" \
  "ds_read_b128 v[172:175], v23 offset:8192\n\t" \
  "v_mfma_f32_16x16x32_bf16 v[244:247], v[100:103], v[164:167], v[244:247]\n\t" \
  "s_waitcnt lgkmcnt(5)\n\t" \
  "v_mfma_f32_16x16x32_bf16 v[240:243], v[100:103], v[160:163], v[240:243]\n\t" \
  "s_add_u32 m0, s50, 6144\n\t" \
  "s_add_u32 s44, s49, 1536\n\t" \
  "v_mfma_f32_16x16x32_bf16 v[244:247], v[104:107], v[160:163], v[244:247]\n\t" \
  "buffer_load_dwordx4 v29, %3, s44 offen lds\n\t" \
  "v_mfma_f32_16x16x32_bf16 v[248:251], v[96:99], v[180:183], v[248:251]\n\t" \
  "ds_read_b128 v[184:187], v23 offset:2048\n\t" \
  "s_waitcnt lgkmcnt(5)\n\t" \
  "v_mfma_f32_16x16x32_bf16 v[252:255], v[100:103], v[180:183], v[252:255]\n\t" \
  "ds_read_b128 v[120:123], v28 offset:3264\n\t" \
  "ds_read_b128 v[64:67], v28 offset:256\n\t" \
  "v_mfma_f32_16x16x32_bf16 v[248:251], v[112:115], v[164:167], v[248:251]\n\t" \
  "ds_read_b128 v[132:135], v24 offset:12288\n\t" \
  "ds_read_b128 v[136:139], v24 offset:10240\n\t" \
  "v_mfma_f32_16x16x32_bf16 v[252:255], v[112:115], v[168:171], v[252:255]\n\t" \
  "ds_read_b128 v[68:71], v28 offset:768\n\t" \
  "s_waitcnt lgkmcnt(8)\n\t" \
  "v_mfma_f32_16x16x32_bf16 v[248:251], v[108:111], v[168:171], v[248:251]\n\t" \
  "s_add_u32 m0, s50, 7168\n\t" \
  "s_add_u32 s44, s49, 1792\n\t" \
  "v_mfma_f32_16x16x32_bf16 v[248:251], v[100:103], v[176:179], v[248:251]\n\t" \
  "buffer_load_dwordx4 v30, %3, s44 offen lds\n\t" \
  "v_mfma_f32_16x16x32_bf16 v[252:255], v[104:107], v[176:179], v[252:255]\n\t" \
  "ds_read_b128 v[128:131], v24 offset:14336\n\t" \
  "s_waitcnt lgkmcnt(7)\n\t" \
  "v_mfma_f32_16x16x32_bf16 v[248:251], v[116:119], v[160:163], v[248:251]\n\t" \
  "ds_read_b128 v[72:75], v28 offset:1280\n\t" \
  "ds_read_b128 v[148:151], v24 offset:4096\n\t" \
  "v_mfma_f32_16x16x32_bf16 v[252:255], v[116:119], v[164:167], v[252:255]\n\t" \
  "ds_read_b128 v[80:83], v28 offset:2304\n\t" \
  "v_mfma_f32_16x16x32_bf16 v[248:251], v[104:107], v[172:175], v[248:251]\n\t" \
  "ds_read_b128 v[76:79], v28 offset:1792\n\t" \
  "s_waitcnt lgkmcnt(9)\n\t" \
  "v_mfma_f32_16x16x32_bf16 v[252:255], v[108:111], v[172:175], v[252:255]\n\t" \
  "ds_read_b128 v[144:147], v24 offset:6144\n\t" \
  "v_mfma_f32_16x16x32_bf16 v[252:255], v[96:99], v[184:187], v[252:255]\n\t" \
  "v_mfma_f32_16x16x32_bf16 v[252:255], v[120:123], v[160:163], v[252:255]\n\t" \
  "ds_read_b128 v[84:87], v28 offset:2816\n\t" \
  "s_waitcnt lgkmcnt(7)\n\t" \
  "v_mfma_f32_16x16x32_bf16 v[240:243], v[64:67], v[132:135], v[240:243]\n\t" \
  "v_mfma_f32_16x16x32_bf16 v[244:247], v[64:67], v[136:139], v[244:247]\n\t" \
  "ds_read_b128 v[140:143], v24 offset:8192\n\t" \
  "v_mfma_f32_16x16x32_bf16 v[244:247], v[68:71], v[132:135], v[244:247]\n\t" \
  "s_waitcnt lgkmcnt(5)\n\t" \
  "v_mfma_f32_16x16x32_bf16 v[240:243], v[68:71], v[128:131], v[240:243]\n\t" \
  "ds_read_b128 v[152:155], v24 offset:2048\n\t" \
  "v_mfma_f32_16x16x32_bf16 v[244:247], v[72:75], v[128:131], v[244:247]\n\t" \
  "ds_read_b128 v[88:91], v28 offset:3328\n\t" \
  "v_mfma_f32_16x16x32_bf16 v[248:251], v[64:67], v[148:151], v[248:251]\n\t" \
  "ds_read_b128 v[96:99], v28 offset:320\n\t" \
  "s_waitcnt lgkmcnt(7)\n\t" \
  "v_mfma_f32_16x16x32_bf16 v[252:255], v[68:71], v[148:151], v[252:255]\n\t" \
  "ds_read_b128 v[164:167], v25 offset:12288\n\t" \
  "ds_read_b128 v[168:171], v25 offset:10240\n\t" \
  "v_mfma_f32_16x16x32_bf16 v[248:251], v[80:83], v[132:135], v[248:251]\n\t" \
  "ds_read_b128 v[100:103], v28 offset:832\n\t" \
  "v_mfma_f32_16x16x32_bf16 v[252:255], v[80:83], v[136:139], v[252:255]\n\t" \
  "ds_read_b128 v[160:163], v25 offset:14336\n\t" \
  "s_waitcnt lgkmcnt(9)\n\t" \
  "v_mfma_f32_16x16x32_bf16 v[248:251], v[76:79], v[136:139], v[248:251]\n\t" \
  "ds_read_b128 v[104:107], v28 offset:1344\n\t" \
  "v_mfma_f32_16x16x32_bf16 v[248:251], v[68:71], v[144:147], v[248:251]\n\t" \
  "ds_read_b128 v[180:183], v25 offset:4096\n\t" \
  "v_mfma_f32_16x16x32_bf16 v[252:255], v[72:75], v[144:147], v[252:255]\n\t" \
  "s_waitcnt lgkmcnt(9)\n\t" \
  "v_mfma_f32_16x16x32_bf16 v[248:251], v[84:87], v[128:131], v[248:251]\n\t" \
  "ds_read_b128 v[112:115], v28 offset:2368\n\t" \
  "v_mfma_f32_16x16x32_bf16 v[252:255], v[84:87], v[132:135], v[252:255]\n\t" \
  "v_mfma_f32_16x16x32_bf16 v[248:251], v[72:75], v[140:143], v[248:251]\n\t" \
  "ds_read_b128 v[108:111], v28 offset:1856\n\t" \
  "s_waitcnt lgkmcnt(9)\n\t" \
  "v_mfma_f32_16x16x32_bf16 v[252:255], v[76:79], v[140:143], v[252:255]\n\t" \
  "ds_read_b128 v[176:179], v25 offset:6144\n\t" \
  "v_mfma_f32_16x16x32_bf16 v[252:255], v[64:67], v[152:155], v[252:255]\n\t" \
  "v_mfma_f32_16x16x32_bf16 v[252:255], v[88:91], v[128:131], v[252:255]\n\t" \
  "ds_read_b128 v[116:119], v28 offset:2880\n\t" \
  "s_waitcnt lgkmcnt(7)\n\t" \
  "v_mfma_f32_16x16x32_bf16 v[240:243], v[96:99], v[164:167], v[240:243]\n\t" \
  "v_mfma_f32_16x16x32_bf16 v[244:247], v[96:99], v[168:171], v[244:247]\n\t" \
  "ds_read_b128 v[172:175], v25 offset:8192\n\t" \
  "v_mfma_f32_16x16x32_bf16 v[244:247], v[100:103], v[164:167], v[244:247]\n\t" \
  "s_waitcnt lgkmcnt(5)\n\t" \
  "v_mfma_f32_16x16x32_bf16 v[240:243], v[100:103], v[160:163], v[240:243]\n\t" \
  "ds_read_b128 v[184:187], v25 offset:2048\n\t" \
  "v_mfma_f32_16x16x32_bf16 v[244:247], v[104:107], v[160:163], v[244:247]\n\t" \
  "ds_read_b128 v[120:123], v28 offset:3392\n\t" \
  "v_mfma_f32_16x16x32_bf16 v[248:251], v[96:99], v[180:183], v[248:251]\n\t" \
  "ds_read_b128 v[64:67], v28 offset:384\n\t" \
  "s_waitcnt lgkmcnt(7)\n\t" \
  "v_mfma_f32_16x16x32_bf16 v[252:255], v[100:103], v[180:183], v[252:255]\n\t" \
  "ds_read_b128 v[132:135], v26 offset:12288\n\t" \
  "ds_read_b128 v[136:139], v26 offset:10240\n\t" \
  "v_mfma_f32_16x16x32_bf16 v[248:251], v[112:115], v[164:167], v[248:251]\n\t" \
  "ds_read_b128 v[68:71], v28 offset:896\n\t" \
  "v_mfma_f32_16x16x32_bf16 v[252:255], v[112:115], v[168:171], v[252:255]\n\t" \
  "ds_read_b128 v[128:131], v26 offset:14336\n\t" \
  "s_waitcnt lgkmcnt(9)\n\t" \
  "v_mfma_f32_16x16x32_bf16 v[248:251], v[108:111], v[168:171], v[248:251]\n\t" \
  "ds_read_b128 v[72:75], v28 offset:1408\n\t" \
  "v_mfma_f32_16x16x32_bf16 v[248:251], v[100:103], v[176:179], v[248:251]\n\t" \
  "ds_read_b128 v[148:151], v26 offset:4096\n\t" \
  "v_mfma_f32_16x16x32_bf16 v[252:255], v[104:107], v[176:179], v[252:255]\n\t" \
  "s_waitcnt lgkmcnt(9)\n\t" \
  "v_mfma_f32_16x16x32_bf16 v[248:251], v[116:119], v[160:163], v[248:251]\n\t" \
  "ds_read_b128 v[80:83], v28 offset:2432\n\t" \
  "v_mfma_f32_16x16x32_bf16 v[252:255], v[116:119], v[164:167], v[252:255]\n\t" \
  "v_mfma_f32_16x16x32_bf16 v[248:251], v[104:107], v[172:175], v[248:251]\n\t" \
  "ds_read_b128 v[76:79], v28 offset:1920\n\t" \
  "s_waitcnt lgkmcnt(9)\n\t" \
  "v_mfma_f32_16x16x32_bf16 v[252:255], v[108:111], v[172:175], v[252:255]\n\t" \
  "ds_read_b128 v[144:147], v26 offset:6144\n\t" \
  "v_mfma_f32_16x16x32_bf16 v[252:255], v[96:99], v[184:187], v[252:255]\n\t" \
  "v_mfma_f32_16x16x32_bf16 v[252:255], v[120:123], v[160:163], v[252:255]\n\t" \
  "ds_read_b128 v[84:87], v28 offset:2944\n\t" \
  "s_waitcnt lgkmcnt(7)\n\t" \
  "v_mfma_f32_16x16x32_bf16 v[240:243], v[64:67], v[132:135], v[240:243]\n\t" \
  "v_mfma_f32_16x16x32_bf16 v[244:247], v[64:67], v[136:139], v[244:247]\n\t" \
  "ds_read_b128 v[140:143], v26 offset:8192\n\t" \
  "v_mfma_f32_16x16x32_bf16 v[244:247], v[68:71], v[132:135], v[244:247]\n\t" \
  "s_waitcnt lgkmcnt(5)\n\t" \
  "v_mfma_f32_16x16x32_bf16 v[240:243], v[68:71], v[128:131], v[240:243]\n\t" \
  "ds_read_b128 v[152:155], v26 offset:2048\n\t" \
  "v_mfma_f32_16x16x32_bf16 v[244:247], v[72:75], v[128:131], v[244:247]\n\t" \
  "ds_read_b128 v[88:91], v28 offset:3456\n\t" \
  "v_mfma_f32_16x16x32_bf16 v[248:251], v[64:67], v[148:151], v[248:251]\n\t" \
  "ds_read_b128 v[96:99], v28 offset:448\n\t" \
  "s_waitcnt lgkmcnt(7)\n\t" \
  "v_mfma_f32_16x16x32_bf16 v[252:255], v[68:71], v[148:151], v[252:255]\n\t" \
  "ds_read_b128 v[164:167], v27 offset:12288\n\t" \
  "ds_read_b128 v[168:171], v27 offset:10240\n\t" \
  "v_mfma_f32_16x16x32_bf16 v[248:251], v[80:83], v[132:135], v[248:251]\n\t" \
  "ds_read_b128 v[100:103], v28 offset:960\n\t" \
  "v_mfma_f32_16x16x32_bf16 v[252:255], v[80:83], v[136:139], v[252:255]\n\t" \
  "ds_read_b128 v[160:163], v27 offset:14336\n\t" \
  "s_waitcnt lgkmcnt(9)\n\t" \
  "v_mfma_f32_16x16x32_bf16 v[248:251], v[76:79], v[136:139], v[248:251]\n\t" \
  "ds_read_b128 v[104:107], v28 offset:1472\n\t" \
  "v_mfma_f32_16x16x32_bf16 v[248:251], v[68:71], v[144:147], v[248:251]\n\t" \
  "ds_read_b128 v[180:183], v27 offset:4096\n\t" \
  "v_mfma_f32_16x16x32_bf16 v[252:255], v[72:75], v[144:147], v[252:255]\n\t" \
  "s_waitcnt lgkmcnt(9)\n\t" \
  "v_mfma_f32_16x16x32_bf16 v[248:251], v[84:87], v[128:131], v[248:251]\n\t" \
  "ds_read_b128 v[112:115], v28 offset:2496\n\t" \
  "v_mfma_f32_16x16x32_bf16 v[252:255], v[84:87], v[132:135], v[252:255]\n\t" \
  "v_mfma_f32_16x16x32_bf16 v[248:251], v[72:75], v[140:143], v[248:251]\n\t" \
  "ds_read_b128 v[108:111], v28 offset:1984\n\t" \
  "s_waitcnt lgkmcnt(9)\n\t" \
  "v_mfma_f32_16x16x32_bf16 v[252:255], v[76:79], v[140:143], v[252:255]\n\t" \
  "ds_read_b128 v[176:179], v27 offset:6144\n\t" \
  "v_mfma_f32_16x16x32_bf16 v[252:255], v[64:67], v[152:155], v[252:255]\n\t" \
  "v_mfma_f32_16x16x32_bf16 v[252:255], v[88:91], v[128:131], v[252:255]\n\t" \
  "ds_read_b128 v[116:119], v28 offset:3008\n\t" \
  "s_waitcnt lgkmcnt(7)\n\t" \
  "v_mfma_f32_16x16x32_bf16 v[240:243], v[96:99], v[164:167], v[240:243]\n\t" \
  "v_mfma_f32_16x16x32_bf16 v[244:247], v[96:99], v[168:171], v[244:247]\n\t" \
  "ds_read_b128 v[172:175], v27 offset:8192\n\t" \
  "v_mfma_f32_16x16x32_bf16 v[244:247], v[100:103], v[164:167], v[244:247]\n\t" \
  "s_waitcnt lgkmcnt(5)\n\t" \
  "v_mfma_f32_16x16x32_bf16 v[240:243], v[100:103], v[160:163], v[240:243]\n\t" \
  "ds_read_b128 v[184:187], v27 offset:2048\n\t" \
  "v_mfma_f32_16x16x32_bf16 v[244:247], v[104:107], v[160:163], v[244:247]\n\t" \
  "ds_read_b128 v[120:123], v28 offset:3520\n\t" \
  "v_mfma_f32_16x16x32_bf16 v[248:251], v[96:99], v[180:183], v[248:251]\n\t" \
  "v_add_u32 v20, s63, v20\n\t" \
  "s_waitcnt lgkmcnt(6)\n\t" \
  "v_mfma_f32_16x16x32_bf16 v[252:255], v[100:103], v[180:183], v[252:255]\n\t" \
  "v_add_u32 v21, s63, v21\n\t" \
  "v_add_u32 v22, s63, v22\n\t" \
  "v_mfma_f32_16x16x32_bf16 v[248:251], v[112:115], v[164:167], v[248:251]\n\t" \
  "v_add_u32 v23, s63, v23\n\t" \
  "v_add_u32 v24, s63, v24\n\t" \
  "v_mfma_f32_16x16x32_bf16 v[252:255], v[112:115], v[168:171], v[252:255]\n\t" \
  "v_add_u32 v25, s63, v25\n\t" \
  "s_waitcnt lgkmcnt(4)\n\t" \
  "v_mfma_f32_16x16x32_bf16 v[248:251], v[108:111], v[168:171], v[248:251]\n\t" \
  "v_add_u32 v54, s62, v44\n\t" \
  "v_add_u32 v62, s62, v45\n\t" \
  "v_mfma_f32_16x16x32_bf16 v[248:251], v[100:103], v[176:179], v[248:251]\n\t" \
  "v_add_u32 v26, s63, v26\n\t" \
  "v_add_u32 v27, s63, v27\n\t" \
  "v_mfma_f32_16x16x32_bf16 v[252:255], v[104:107], v[176:179], v[252:255]\n\t" \
  "v_add_u32 v28, 16896, v28\n\t" \
  "s_waitcnt lgkmcnt(2)\n\t" \
  "v_mfma_f32_16x16x32_bf16 v[248:251], v[116:119], v[160:163], v[248:251]\n\t" \
  "v_mfma_f32_16x16x32_bf16 v[252:255], v[116:119], v[164:167], v[252:255]\n\t" \
  "v_mfma_f32_16x16x32_bf16 v[248:251], v[104:107], v[172:175], v[248:251]\n\t" \
  "s_waitcnt lgkmcnt(0)\n\t" \
  "v_mfma_f32_16x16x32_bf16 v[252:255], v[108:111], v[172:175], v[252:255]\n\t" \
  "v_mfma_f32_16x16x32_bf16 v[252:255], v[96:99], v[184:187], v[252:255]\n\t" \
  "v_mfma_f32_16x16x32_bf16 v[252:255], v[120:123], v[160:163], v[252:255]\n\t" \
  "s_nop 7\n\t" \
  "v_cvt_pk_bf16_f32 v240, v240, v241\n\t" \
  "v_cvt_pk_bf16_f32 v241, v242, v243\n\t" \
  "v_cvt_pk_bf16_f32 v244, v244, v245\n\t" \
  "v_cvt_pk_bf16_f32 v245, v246, v247\n\t" \
  "v_cvt_pk_bf16_f32 v248, v248, v249\n\t" \
  "v_cvt_pk_bf16_f32 v249, v250, v251\n\t" \
  "v_cvt_pk_bf16_f32 v252, v252, v253\n\t" \
  "v_cvt_pk_bf16_f32 v253, v254, v255\n\t" \
  "ds_write_b32 v54, v240 offset:576\n\t" \
  "ds_write_b32 v54, v241 offset:592\n\t" \
  "ds_write_b32 v54, v244 offset:1152\n\t" \
  "ds_write_b32 v54, v245 offset:1168\n\t" \
  "ds_write_b32 v54, v248 offset:2880\n\t" \
  "ds_write_b32 v54, v249 offset:2896\n\t" \
  "ds_write_b32 v54, v252 offset:3456\n\t" \
  "ds_write_b32 v54, v253 offset:3472\n\t" \
  "L_cv_done_%=:\n\t" \
  "s_lshl_b32 s61, s48, 12\n\t" \
  "s_add_u32 s61, s61, 7340032\n\t" \
  "s_add_u32 s48, s48, 2\n\t" \
  "s_sub_u32 s63, 0, s63\n\t" \
  "v_mov_b32 v53, v46\n\t" \
  "s_mul_i32 s62, s43, 8448\n\t" \
  "s_add_u32 s46, s46, 1\n\t" \
  "s_cmp_lt_u32 s46, 5\n\t" \
  "s_cbranch_scc1 L_cv_%=\n\t" \
  "s_branch L_cv_last_%=\n\t" \
  "L_cv_idle_%=:\n\t" \
  "ds_read_b128 v[224:227], v62\n\t" \
  "ds_read_b128 v[228:231], v62 offset:2304\n\t" \
  "s_waitcnt lgkmcnt(0)\n\t" \
  "buffer_store_dwordx4 v[224:227], v53, %3, s61 offen sc1\n\t" \
  "buffer_store_dwordx4 v[228:231], v53, %3, s61 offen offset:2048 sc1\n\t" \
  "s_nop 1\n\t" \
  "v_mov_b32 v53, 0x70000000\n\t" \
  "L_cv_last_%=:\n\t" \
  "s_waitcnt lgkmcnt(0)\n\t" \
  "s_barrier\n\t" \
  "ds_read_b128 v[224:227], v62\n\t" \
  "ds_read_b128 v[228:231], v62 offset:2304\n\t" \
  "s_waitcnt lgkmcnt(0)\n\t" \
  "buffer_store_dwordx4 v[224:227], v53, %3, s61 offen sc1\n\t" \
  "buffer_store_dwordx4 v[228:231], v53, %3, s61 offen offset:2048 sc1\n\t" \
  "s_lshl_b32 s43, s40, 7\n\t" \
  "s_add_u32 s43, s43, 49152\n\t" \
  "s_waitcnt vmcnt(0)\n\t" \
  "s_barrier\n\t" \
  "buffer_store_dwordx2 v[32:33], v34, %3, s43 offen sc1\n\t" \
  "s_cmp_lg_u32 s40, 0\n\t" \
  "s_cbranch_scc1 L_fin_%=\n\t" \
  "v_lshlrev_b32 v54, 6, v19\n\t" \
  "v_add_u32 v54, 4096, v54\n\t" \
  "s_mov_b32 s51, 0\n\t" \
  "L_sp1_%=:\n\t" \
  "buffer_load_dwordx2 v[56:57], v54, %3, 0 offen sc1\n\t" \
  "s_waitcnt vmcnt(0)\n\t" \
  "v_cmp_eq_u64 vcc, v[32:33], v[56:57]\n\t" \
  "s_add_u32 s51, s51, 1\n\t" \
  "s_nop 1\n\t" \
  "s_cmp_eq_u64 vcc, -1\n\t" \
  "s_cbranch_scc1 L_ok1_%=\n\t" \
  "s_sleep 1\n\t" \
  "s_cmp_lt_u32 s51, 0x40000\n\t" \
  "s_cbranch_scc1 L_sp1_%=\n\t" \
  "L_ok1_%=:\n\t" \
  "s_barrier\n\t" \
  "buffer_store_dwordx2 v[32:33], v34, %3, 0 offen offset:0 sc1\n\t" \
  "L_fin_%=:\n\t" \
  "s_waitcnt vmcnt(0) lgkmcnt(0)\n\t" \
  "L_send_%=:\n\t"
#define SSM_CLOB "v16","v17","v18","v19","v20","v21","v22","v23","v24","v25","v26","v27","v28","v29","v30","v31","v32","v33","v34","v35","v36","v37","v38","v39","v40","v41","v42","v43","v44","v45","v46","v47","v48","v49","v50","v51","v52","v53","v54","v55","v56","v57","v58","v59","v60","v61","v62","v63","v64","v65","v66","v67","v68","v69","v70","v71","v72","v73","v74","v75","v76","v77","v78","v79","v80","v81","v82","v83","v84","v85","v86","v87","v88","v89","v90","v91","v92","v93","v94","v95","v96","v97","v98","v99","v100","v101","v102","v103","v104","v105","v106","v107","v108","v109","v110","v111","v112","v113","v114","v115","v116","v117","v118","v119","v120","v121","v122","v123","v124","v125","v126","v127","v128","v129","v130","v131","v132","v133","v134","v135","v136","v137","v138","v139","v140","v141","v142","v143","v144","v145","v146","v147","v148","v149","v150","v151","v152","v153","v154","v155","v156","v157","v158","v159","v160","v161","v162","v163","v164","v165","v166","v167","v168","v169","v170","v171","v172","v173","v174","v175","v176","v177","v178","v179","v180","v181","v182","v183","v184","v185","v186","v187","v188","v189","v190","v191","v192","v193","v194","v195","v196","v197","v198","v199","v200","v201","v202","v203","v204","v205","v206","v207","v208","v209","v210","v211","v212","v213","v214","v215","v216","v217","v218","v219","v220","v221","v222","v223","v224","v225","v226","v227","v228","v229","v230","v231","v232","v233","v234","v235","v236","v237","v238","v239","v240","v241","v242","v243","v244","v245","v246","v247","v248","v249","v250","v251","v252","v253","v254","v255","a0","a1","a2","a3","a4","a5","a6","a7","a8","a9","a10","a11","a12","a13","a14","a15","a16","a17","a18","a19","a20","a21","a22","a23","a24","a25","a26","a27","a28","a29","a30","a31","a32","a33","a34","a35","a36","a37","a38","a39","a40","a41","a42","a43","a44","a45","a46","a47","a48","a49","a50","a51","a52","a53","a54","a55","a56","a57","a58","a59","a60","a61","a62","a63","a64","a65","a66","a67","a68","a69","a70","a71","a72","a73","a74","a75","a76","a77","a78","a79","a80","a81","a82","a83","a84","a85","a86","a87","a88","a89","a90","a91","a92","a93","a94","a95","a96","a97","a98","a99","a100","a101","a102","a103","a104","a105","a106","a107","a108","a109","a110","a111","a112","a113","a114","a115","a116","a117","a118","a119","a120","a121","a122","a123","a124","a125","a126","a127","s40","s41","s42","s43","s44","s45","s46","s47","s48","s49","s50","s51","s52","s53","s54","s55","s56","s57","s58","s59","s60","s61","s62","s63","s64","s65","s66","s67","s68","s69","s70","s71","s72","s73","s74","s75","s76","s77","s78","s79","s80","s81","s82","s83","s84","s85","s86","s87","s88","s89","s90","s91","s92","s93","s94","s95","s96","s97","s98","s99","vcc","m0","scc","memory"

typedef unsigned short u16;
typedef unsigned long long u64;
typedef unsigned v4u __attribute__((ext_vector_type(4)));
typedef unsigned v2u __attribute__((ext_vector_type(2)));
typedef float v4f __attribute__((ext_vector_type(4)));
typedef float v2f __attribute__((ext_vector_type(2)));
typedef __bf16 v2bf __attribute__((ext_vector_type(2)));
#define DI __device__ __forceinline__
#define SC1 16
constexpr int L_ = 2048, D_ = 576, FF_ = 4608;
constexpr int LDS_BYTES = 163840;

struct Params {
  const u16 *x, *Are, *Aim, *Bre, *Bim, *Cre, *Cim, *logdt, *Dskip, *w1, *w2;
  u16* out; char* ws;
};

__global__ __launch_bounds__(256, 1) void fused_kernel(Params p) {
  const int b = blockIdx.x, xcd = b & 7, jb = b >> 3;
  auto ws = __builtin_amdgcn_make_buffer_rsrc((void*)p.ws, 0, WS_END, 0x00020000);
  if (jb < 8) {
    const int w = __builtin_amdgcn_readfirstlane(threadIdx.x >> 6);
    auto srdX = __builtin_amdgcn_make_buffer_rsrc((void*)p.x, 0, L_ * D_ * 2, 0x00020000);
    auto srdW1 = __builtin_amdgcn_make_buffer_rsrc((void*)p.w1, 0, D_ * FF_ * 2, 0x00020000);
    auto srdW2 = __builtin_amdgcn_make_buffer_rsrc((void*)p.w2, 0, D_ * FF_ * 2, 0x00020000);
    asm volatile(SSM_ASM :: "s"(srdX), "s"(srdW1), "s"(srdW2), "s"(ws), "s"(p.Are), "s"(p.Aim), "s"(p.Bre), "s"(p.Bim), "s"(p.Cre), "s"(p.Cim), "s"(p.logdt),
                 "s"(xcd), "s"(jb), "s"(w) : SSM_CLOB);
  } else {
    const int w = __builtin_amdgcn_readfirstlane(threadIdx.x >> 6);
    const int rt = (jb - 8) & 7, q = (jb - 8) >> 3, kg = xcd;
    auto srdXb = __builtin_amdgcn_make_buffer_rsrc((void*)((const char*)p.x - SRD_BIAS), 0, L_ * D_ * 2 + SRD_BIAS, 0x00020000);
    auto srdW1 = __builtin_amdgcn_make_buffer_rsrc((void*)((const char*)p.w1 - SRD_BIAS), 0, D_ * FF_ * 2 + SRD_BIAS, 0x00020000);
    auto srdW2 = __builtin_amdgcn_make_buffer_rsrc((void*)((const char*)p.w2 - SRD_BIAS), 0, D_ * FF_ * 2 + SRD_BIAS, 0x00020000);
    auto srdO = __builtin_amdgcn_make_buffer_rsrc((void*)((char*)p.out - SRD_BIAS), 0, L_ * D_ * 2 + SRD_BIAS, 0x00020000);
    auto srdD = __builtin_amdgcn_make_buffer_rsrc((void*)p.Dskip, 0, D_ * 2, 0x00020000);
    asm volatile(MLP_ASM :: "s"(srdXb), "s"(srdW1), "s"(srdW2), "s"(ws), "s"(rt), "s"(kg), "s"(q), "s"(w), "s"(srdO), "s"(srdD), "s"(__builtin_amdgcn_kernarg_segment_ptr()) : MLP_CLOB);
  }
}

extern "C" void kernel_launch(void* const* d_in, const int* in_sizes, int n_in, void* d_out, int out_size, void* d_ws, size_t ws_size, hipStream_t stream) {
  Params p;
  p.x = (const u16*)d_in[0]; p.Are = (const u16*)d_in[1]; p.Aim = (const u16*)d_in[2]; p.Bre = (const u16*)d_in[3]; p.Bim = (const u16*)d_in[4];
  p.Cre = (const u16*)d_in[5]; p.Cim = (const u16*)d_in[6]; p.logdt = (const u16*)d_in[7]; p.Dskip = (const u16*)d_in[8];
  p.w1 = (const u16*)d_in[10]; p.w2 = (const u16*)d_in[11]; p.out = (u16*)d_out; p.ws = (char*)d_ws;
  hipFuncSetAttribute((const void*)fused_kernel, hipFuncAttributeMaxDynamicSharedMemorySize, LDS_BYTES);
  fused_kernel<<<256, 256, LDS_BYTES, stream>>>(p);
}
```
